```python
import torch

def _softmax(x, axis=-1):
    return torch.softmax(x, dim=axis)
def _gelu(x):
    return torch.nn.functional.gelu(x, approximate="tanh")
def _silu(x):
    return torch.nn.functional.silu(x)
def _sigmoid(x):
    return torch.sigmoid(x)
def _layernorm(x, axis=-1, eps=1e-5):
    m = x.mean(dim=axis, keepdim=True)
    v = x.var(dim=axis, keepdim=True, unbiased=False)
    return (x - m) / torch.sqrt(v + eps)
def _scatter_add(tgt, idx, src, axis=0):
    return tgt.clone().index_add_(axis, idx, src)
def _segment_sum(src, seg, n):
    out = torch.zeros((n,) + src.shape[1:], dtype=src.dtype)
    return out.index_add_(0, seg, src)
def _pad(x, pw):
    flat = tuple(v for pair in reversed(pw) for v in pair)
    return torch.nn.functional.pad(x, flat)

kernel_name = 'sequential_preLN__ssm_conv_s4d__geglu_ffn__prompt'

def setup_inputs():

    _w = (lambda a: (lambda t: t.to(torch.bfloat16) if t.is_floating_point() else t)(torch.as_tensor(a)))
    def _A_setup():
        Bsz, L, D, N = 1, 2048, 576, 128
        return {
            "u": _w((lambda *s: torch.randn(*s, dtype=torch.bfloat16) * 0.05)(Bsz, L, D)),
            "log_A_real": _w((lambda *s: torch.randn(*s, dtype=torch.bfloat16) * 6.4)(D, N) * 0.5),
            "A_imag": _w((lambda *s: torch.randn(*s, dtype=torch.bfloat16) * 0.05)(D, N) * 3.14),
            "C_real": _w((lambda *s: torch.randn(*s, dtype=torch.bfloat16) * 0.05)(D, N) / N ** 0.5),
            "C_imag": _w((lambda *s: torch.randn(*s, dtype=torch.bfloat16) * 0.05)(D, N) / N ** 0.5),
            "log_dt": _w((lambda *s: torch.randn(*s, dtype=torch.bfloat16) * 0.05)(D) * 0.5 - 4.0),
            "D_skip": _w((lambda *s: torch.randn(*s, dtype=torch.bfloat16) * 0.05)(D)),
        }
    _A_in = _A_setup()

    def _B_setup():
        B, S, D, F = 1, 2048, 576, 4608
        return {
            "x": _w((lambda *s: torch.randn(*s, dtype=torch.bfloat16) * 0.05)(B, S, D)),
            "w_gate": _w((lambda *s: torch.randn(*s, dtype=torch.bfloat16) * 0.05)(D, F)),
            "w_up": _w((lambda *s: torch.randn(*s, dtype=torch.bfloat16) * 0.05)(D, F)),
            "w_down": _w((lambda *s: torch.randn(*s, dtype=torch.bfloat16) * 0.05)(F, D)),
        }
    _B_in = _B_setup()

    return {
        "A_u": _A_in["u"],
        "A_log_A_real": _A_in["log_A_real"],
        "A_A_imag": _A_in["A_imag"],
        "A_C_real": _A_in["C_real"],
        "A_C_imag": _A_in["C_imag"],
        "A_log_dt": _A_in["log_dt"],
        "A_D_skip": _A_in["D_skip"],
        "B_x": _B_in["x"],
        "B_w_gate": _B_in["w_gate"],
        "B_w_up": _B_in["w_up"],
        "B_w_down": _B_in["w_down"],
    }

def reference(A_u, A_log_A_real, A_A_imag, A_C_real, A_C_imag, A_log_dt, A_D_skip, B_x, B_w_gate, B_w_up, B_w_down):
    def _A_ref(u, log_A_real, A_imag, C_real, C_imag, log_dt, D_skip):
        L = u.shape[1]
        dt = torch.exp(log_dt)
        A = -torch.exp(log_A_real) + 1j * A_imag
        C = (C_real + 1j * C_imag) * dt[:, None]
        pos = (lambda x, dt: x.to(dt))(torch.arange(L), log_dt.dtype)
        P = torch.exp((A * dt[:, None])[:, :, None] * pos[None, None, :])
        K = 2.0 * torch.einsum("dn,dnl->dl", C, P).real
        L2 = 2 * L
        u_t = (lambda x, *d: x.permute(*d))(u, 0, 2, 1)
        Kf = (lambda x, *a, **k: torch.fft.rfft(x.float(), *a, **k))(K, L2)
        Uf = (lambda x, *a, **k: torch.fft.rfft(x.float(), *a, **k))(u_t, L2)
        y = (lambda x, *a, **k: torch.fft.irfft(x, *a, **k).to(torch.bfloat16))(Uf * Kf[None, :, :], L2)[:, :, :L]
        y = (lambda x, *d: x.permute(*d))(y, 0, 2, 1)
        return y + u * D_skip[None, None, :]

    def _B_ref(x, w_gate, w_up, w_down):
        return (_gelu(x @ w_gate) * (x @ w_up)) @ w_down

    x = A_u
    h = x + _A_ref(_layernorm(x, axis=-1), A_log_A_real, A_A_imag, A_C_real, A_C_imag, A_log_dt, A_D_skip)
    h2 = h + _B_ref(_layernorm(h, axis=-1), B_w_gate, B_w_up, B_w_down)
    return h2
```

```cpp
#include <hip/hip_runtime.h>
#include <hip/hip_bf16.h>

constexpr int L_ = 2048, D_ = 576, NM_ = 128, F_ = 4608;
constexpr size_t OFF_G  = 0;
constexpr size_t OFF_HF = 0x240000;
constexpr size_t OFF_K  = 0x480000;
constexpr size_t OFF_W1 = 0x990000;
constexpr size_t OFF_W2 = 0x13B0000;
constexpr size_t OFF_H  = 0x18C0000;
constexpr size_t OFF_RS = 0x2AC0000;
constexpr size_t OFF_STAMP = 200u << 20;

typedef unsigned u32;
typedef unsigned short u16;
typedef u32   u32x2 __attribute__((ext_vector_type(2)));
typedef u32   u32x4 __attribute__((ext_vector_type(4)));
typedef float f32x2 __attribute__((ext_vector_type(2)));
typedef float f32x4 __attribute__((ext_vector_type(4)));
typedef __bf16 bf16x2 __attribute__((ext_vector_type(2)));
typedef __bf16 bf16x8 __attribute__((ext_vector_type(8)));
typedef _Float16 f16x2 __attribute__((ext_vector_type(2)));
typedef _Float16 f16x8 __attribute__((ext_vector_type(8)));
#if defined(__HIP_DEVICE_COMPILE__)
#define NOPK __attribute__((target("no-packed-fp32-ops")))
#else
#define NOPK
#endif
#define DI __device__ __forceinline__ NOPK
#define GAS __attribute__((address_space(1)))
#define LAS __attribute__((address_space(3)))

DI int tidx() { return (int)__builtin_amdgcn_workitem_id_x(); }
DI int bidx_() { return (int)__builtin_amdgcn_workgroup_id_x(); }
DI float as_f(u32 x) { return __builtin_bit_cast(float, x); }
DI u32 as_u(float x) { return __builtin_bit_cast(u32, x); }
DI void bar() { __builtin_amdgcn_fence(__ATOMIC_RELEASE, "workgroup"); __builtin_amdgcn_s_barrier(); __builtin_amdgcn_fence(__ATOMIC_ACQUIRE, "workgroup"); }
template <int M> DI float shx(float v) {
  if constexpr (M == 1) return as_f(__builtin_amdgcn_mov_dpp(as_u(v), 0xB1, 0xF, 0xF, true));
  else if constexpr (M == 2) return as_f(__builtin_amdgcn_mov_dpp(as_u(v), 0x4E, 0xF, 0xF, true));
  else if constexpr (M == 32) { const auto r = __builtin_amdgcn_permlane32_swap(as_u(v), as_u(v), false, false); return (__builtin_amdgcn_workitem_id_x() & 32) ? as_f(r[0]) : as_f(r[1]); }
  else return as_f(__builtin_amdgcn_ds_swizzle(as_u(v), (M << 10) | 0x1F));
}
DI float fexp(float x) { return __builtin_amdgcn_exp2f(x * 1.44269504088896f); }
DI u32 pk_bf16(float lo, float hi) { f32x2 v = {lo, hi}; return __builtin_bit_cast(u32, __builtin_convertvector(v, bf16x2)); }
DI u32 pk_f16(float lo, float hi) { f32x2 v = {lo, hi}; return __builtin_bit_cast(u32, __builtin_convertvector(v, f16x2)); }
DI float bf_lo(u32 w) { return as_f(w << 16); }
DI float bf_hi(u32 w) { return as_f(w & 0xffff0000u); }
DI float bf1(u16 h) { return as_f((u32)h << 16); }
DI float rbf(float x) { return bf_lo(pk_bf16(x, x)); }
DI f32x4 mfma_bf16(u32x4 a, u32x4 b, f32x4 c) { return __builtin_amdgcn_mfma_f32_16x16x32_bf16(__builtin_bit_cast(bf16x8, a), __builtin_bit_cast(bf16x8, b), c, 0, 0, 0); }
DI f32x4 mfma_f16(u32x4 a, u32x4 b, f32x4 c) { return __builtin_amdgcn_mfma_f32_16x16x32_f16(__builtin_bit_cast(f16x8, a), __builtin_bit_cast(f16x8, b), c, 0, 0, 0); }
DI void glds16(const void* g, void* l) { __builtin_amdgcn_global_load_lds((const GAS void*)g, (LAS void*)l, 16, 0, 0); }
DI u32 lds_off(const void* p) { return (u32)(size_t)(const LAS char*)p; }
DI void wait_vm0() { asm volatile("s_waitcnt vmcnt(0)" ::: "memory"); }
DI void st16_wt(void* p, u32x4 v) { asm volatile("global_store_dwordx4 %0, %1, off sc1\n\ts_nop 1" :: "v"(p), "v"(v) : "memory"); }

#define STAMP_BEGIN
#define STAMP_END(K)

DI void role_ln1(int bidx, const u16* __restrict__ x, const u16* __restrict__ dskip, char* ws, char* smem) {
  const int tid = tidx(), lane = tid & 63, w = tid >> 6;
  const int b = (bidx & 7) * 8 + (bidx >> 3);
  const int rloc = 8 * w + (lane >> 3), row = 32 * b + rloc, c = lane & 7;
  u32x4 xv[9], dv[9];
#pragma unroll
  for (int j = 0; j < 9; ++j) xv[j] = *(const u32x4*)(x + row * D_ + (c + 8 * j) * 8);
#pragma unroll
  for (int j = 0; j < 9; ++j) dv[j] = *(const u32x4*)(dskip + (c + 8 * j) * 8);
  float s = 0.f;
#pragma unroll
  for (int j = 0; j < 9; ++j)
#pragma unroll
    for (int e = 0; e < 4; ++e) s += bf_lo(xv[j][e]) + bf_hi(xv[j][e]);
  s += shx<1>(s); s += shx<2>(s); s += shx<4>(s);
  const float mean = s * (1.0f / D_);
  float q = 0.f;
#pragma unroll
  for (int j = 0; j < 9; ++j)
#pragma unroll
    for (int e = 0; e < 4; ++e) { float a = bf_lo(xv[j][e]) - mean, bb = bf_hi(xv[j][e]) - mean; q = __builtin_fmaf(a, a, q); q = __builtin_fmaf(bb, bb, q); }
  q += shx<1>(q); q += shx<2>(q); q += shx<4>(q);
  const float mb16 = rbf(mean);
  const float sd = rbf(__builtin_sqrtf(rbf(rbf(q * (1.0f / D_)) + 1e-5f)));
  const float inv = 1.0f / sd;
  char* tile = smem + rloc * 1216 + ((rloc >> 3) & 1) * 32;
  char* rf = ws + OFF_HF + ((size_t)(row >> 4) * 72 * 16 + (row & 15)) * 16;
#pragma unroll
  for (int j = 0; j < 9; ++j) {
    u32x4 uu, rr;
#pragma unroll
    for (int e = 0; e < 4; ++e) {
      const float x0 = bf_lo(xv[j][e]), x1 = bf_hi(xv[j][e]);
      const u32 dd = pk_bf16(x0 - mb16, x1 - mb16);
      const u32 up = pk_bf16(bf_lo(dd) * inv, bf_hi(dd) * inv);
      const u32 tp = pk_bf16(bf_lo(up) * bf_lo(dv[j][e]), bf_hi(up) * bf_hi(dv[j][e]));
      uu[e] = up; rr[e] = pk_bf16(x0 + bf_lo(tp), x1 + bf_hi(tp));
    }
    *(u32x4*)(tile + (c + 8 * j) * 16) = uu;
    *(u32x4*)(rf + (c + 8 * j) * 256) = rr;
  }
  bar();
  const int g = lane >> 4, a = (lane >> 2) & 3, p = lane & 3;
  const int r0 = 31 - 8 * g - a, r1 = r0 - 4;
  const u32 ad0 = lds_off(smem) + r0 * 1216 + ((r0 >> 3) & 1) * 32 + p * 8 + w * 9 * 32;
  const u32 ad1 = lds_off(smem) + r1 * 1216 + ((r1 >> 3) & 1) * 32 + p * 8 + w * 9 * 32;
  u32x2 lo[9], hi[9];
#pragma unroll
  for (int j = 0; j < 9; ++j) {
    asm volatile("ds_read_b64_tr_b16 %0, %1 offset:%2" : "=v"(lo[j]) : "v"(ad0), "i"(j * 32) : "memory");
    asm volatile("ds_read_b64_tr_b16 %0, %1 offset:%2" : "=v"(hi[j]) : "v"(ad1), "i"(j * 32) : "memory");
  }
  asm volatile("s_waitcnt lgkmcnt(0)" : "+v"(lo[0]), "+v"(lo[1]), "+v"(lo[2]), "+v"(lo[3]), "+v"(lo[4]), "+v"(lo[5]), "+v"(lo[6]), "+v"(lo[7]), "+v"(lo[8]),
               "+v"(hi[0]), "+v"(hi[1]), "+v"(hi[2]), "+v"(hi[3]), "+v"(hi[4]), "+v"(hi[5]), "+v"(hi[6]), "+v"(hi[7]), "+v"(hi[8]) :: "memory");
  char* gp = ws + OFF_G + (size_t)(w * 144 + (lane & 15)) * 4096 + (2016 - 32 * b + 8 * g) * 2;
#pragma unroll
  for (int j = 0; j < 9; ++j) *(u32x4*)(gp + (size_t)j * 16 * 4096) = u32x4{lo[j][0], lo[j][1], hi[j][0], hi[j][1]};
}

DI void role_kgen(int d, const u16* __restrict__ logA, const u16* __restrict__ Aim, const u16* __restrict__ Cre, const u16* __restrict__ Cim,
                  const u16* __restrict__ logdt, char* ws, char* smem) {
  const int tid = tidx(), lane = tid & 63, w = tid >> 6;
  const int mode = d * NM_ + 32 * w + (lane & 31);
  const u16 h_la = logA[mode], h_ai = Aim[mode], h_cr = Cre[mode], h_ci = Cim[mode], h_dt = logdt[d];
  u32 slot[9];
#pragma unroll
  for (int e = 0; e < 9; ++e) {
    const int j = 8 * tid - 1 + e;
    const int p = (int)rbf((float)j);
    int idx;
    if (p < 512) idx = (p & 7) * 64 + (p >> 3);
    else if (p < 1024) idx = (8 + ((p >> 2) & 1)) * 64 + ((p - 512) >> 3);
    else idx = (10 + ((p - 1024) >> 9)) * 64 + ((p & 511) >> 3);
    if (j < 0) idx = 1024;
    slot[e] = idx * 4;
  }
  float* tab = (float*)(smem + w * 768);
  float* part = (float*)(smem + 4096);
  float* fin = (float*)(smem + 4096 + 16384);
  const float dt = rbf(fexp(bf1(h_dt)));
  const float ar = -rbf(fexp(bf1(h_la))) * dt * 1.44269504088896f;
  const float ai = bf1(h_ai) * dt * 0.159154943091895f;
  float cr = bf1(h_cr) * dt, ci = bf1(h_ci) * dt;
  float mx = __builtin_fmaxf(__builtin_fabsf(cr), __builtin_fabsf(ci));
  mx = __builtin_fmaxf(mx, shx<1>(mx)); mx = __builtin_fmaxf(mx, shx<2>(mx)); mx = __builtin_fmaxf(mx, shx<4>(mx)); mx = __builtin_fmaxf(mx, shx<8>(mx)); mx = __builtin_fmaxf(mx, shx<16>(mx));
  const int ex = (mx > 0.f && mx < 3.0e38f) ? __builtin_amdgcn_frexp_expf(mx) : 0;
  cr = __builtin_amdgcn_ldexpf(cr, -ex); ci = __builtin_amdgcn_ldexpf(ci, -ex);
  {
    const float mg = __builtin_amdgcn_exp2f(ar * 128.f), ph = __builtin_amdgcn_fractf(ai * 128.f);
    const float er = mg * __builtin_amdgcn_cosf(ph), ei = mg * __builtin_amdgcn_sinf(ph);
    if (lane < 32) { tab[lane] = ar; tab[32 + lane] = ai; tab[64 + lane] = cr; }
    else { tab[96 + lane - 32] = ci; tab[128 + lane - 32] = er; tab[160 + lane - 32] = ei; }
  }
  if (tid == 0) fin[1024] = 0.f;
  __builtin_amdgcn_fence(__ATOMIC_RELEASE, "wavefront"); __builtin_amdgcn_wave_barrier(); __builtin_amdgcn_fence(__ATOMIC_ACQUIRE, "wavefront");
  const int i = lane & 15, q = lane >> 4;
  const float p1 = i < 8 ? (float)i : (i == 8 ? 512.f : i == 9 ? 516.f : i < 13 ? (float)(512 * (i - 8)) : 0.f);
  const float p2 = (float)(8 * i);
  f32x4 acc[4] = {};
#pragma unroll
  for (int s = 0; s < 2; ++s) {
    const f32x4 tar = *(const f32x4*)(tab + 16 * s + 4 * q), tai = *(const f32x4*)(tab + 32 + 16 * s + 4 * q);
    const f32x4 tcr = *(const f32x4*)(tab + 64 + 16 * s + 4 * q), tci = *(const f32x4*)(tab + 96 + 16 * s + 4 * q);
    const f32x4 ter = *(const f32x4*)(tab + 128 + 16 * s + 4 * q), tei = *(const f32x4*)(tab + 160 + 16 * s + 4 * q);
    u32x4 xa, yb[4];
#pragma unroll
    for (int m = 0; m < 4; ++m) {
      const float mg = __builtin_amdgcn_exp2f(tar[m] * p1), ph = __builtin_amdgcn_fractf(tai[m] * p1);
      const float er = mg * __builtin_amdgcn_cosf(ph), ei = mg * __builtin_amdgcn_sinf(ph);
      xa[m] = pk_f16(tcr[m] * er - tci[m] * ei, -(tcr[m] * ei + tci[m] * er));
      const float mg2 = __builtin_amdgcn_exp2f(tar[m] * p2), ph2 = __builtin_amdgcn_fractf(tai[m] * p2);
      float yr = mg2 * __builtin_amdgcn_cosf(ph2), yi = mg2 * __builtin_amdgcn_sinf(ph2);
      yb[0][m] = pk_f16(yr, yi);
#pragma unroll
      for (int ct = 1; ct < 4; ++ct) {
        const float nr = yr * ter[m] - yi * tei[m], ni = yr * tei[m] + yi * ter[m];
        yr = nr; yi = ni; yb[ct][m] = pk_f16(yr, yi);
      }
    }
#pragma unroll
    for (int ct = 0; ct < 4; ++ct) acc[ct] = mfma_f16(xa, yb[ct], acc[ct]);
  }
  const float unsc = __builtin_amdgcn_ldexpf(2.0f, ex);
#pragma unroll
  for (int ct = 0; ct < 4; ++ct) *(f32x4*)(part + ((w * 4 + ct) * 64 + lane) * 4) = acc[ct] * unsc;
  bar();
  {
    const float* pp = part + (w * 64 + lane) * 4;
    const f32x4 sum = (*(const f32x4*)pp + *(const f32x4*)(pp + 1024)) + (*(const f32x4*)(pp + 2048) + *(const f32x4*)(pp + 3072));
#pragma unroll
    for (int r = 0; r < 4; ++r) fin[(4 * q + r) * 64 + 16 * w + i] = sum[r];
  }
  bar();
  float v[9];
#pragma unroll
  for (int e = 0; e < 9; ++e) v[e] = *(const float*)((const char*)fin + slot[e]);
  char* kp = ws + OFF_K + (size_t)d * 9216;
  *(u32x4*)(kp + (28 + tid) * 16) = u32x4{pk_bf16(v[0], v[1]), pk_bf16(v[2], v[3]), pk_bf16(v[4], v[5]), pk_bf16(v[6], v[7])};
  *(u32x4*)(kp + 4640 + (28 + tid) * 16) = u32x4{pk_bf16(v[1], v[2]), pk_bf16(v[3], v[4]), pk_bf16(v[5], v[6]), pk_bf16(v[7], v[8])};
  if (tid < 56) *(u32x4*)(kp + (tid < 28 ? tid * 16 : 4640 + (tid - 28) * 16)) = u32x4{0, 0, 0, 0};
}

DI void role_w1(int u, const u16* __restrict__ wg, const u16* __restrict__ wu, char* ws, char* smem) {
  const int tid = tidx(), lane = tid & 63, w = __builtin_amdgcn_readfirstlane(tid >> 6);
  const int hi = u >> 3, lo = u & 7;
  const int strip = 2 * ((hi >> 2) * 8 + lo) + (hi & 1), gu = (hi >> 1) & 1;
  const u16* src = (gu ? wu : wg) + strip * 32;
  {
    const int r = lane >> 2, ch = (lane & 3) ^ (((r >> 3) & 1) << 1);
    const u16* p = src + (size_t)(w * 144 + r) * F_ + ch * 8;
#pragma unroll
    for (int j = 0; j < 9; ++j) glds16(p + (size_t)j * 16 * F_, smem + (w * 9 + j) * 1024);
  }
  wait_vm0();
  bar();
  const int c = w & 1, kh = w >> 1, g = lane >> 4, a = (lane >> 2) & 3, p = lane & 3;
  const u32 ad = lds_off(smem) + (kh * 288 + 8 * g + a) * 64 + ((c ^ (g & 1)) * 32) + p * 8;
  u32x2 f0[9], f1[9];
#pragma unroll
  for (int j = 0; j < 9; ++j) {
    asm volatile("ds_read_b64_tr_b16 %0, %1 offset:%2" : "=v"(f0[j]) : "v"(ad), "i"(j * 2048) : "memory");
    asm volatile("ds_read_b64_tr_b16 %0, %1 offset:%2" : "=v"(f1[j]) : "v"(ad), "i"(j * 2048 + 256) : "memory");
  }
  asm volatile("s_waitcnt lgkmcnt(0)" : "+v"(f0[0]), "+v"(f0[1]), "+v"(f0[2]), "+v"(f0[3]), "+v"(f0[4]), "+v"(f0[5]), "+v"(f0[6]), "+v"(f0[7]), "+v"(f0[8]),
               "+v"(f1[0]), "+v"(f1[1]), "+v"(f1[2]), "+v"(f1[3]), "+v"(f1[4]), "+v"(f1[5]), "+v"(f1[6]), "+v"(f1[7]), "+v"(f1[8]) :: "memory");
  f32x4 cs = {0.f, 0.f, 0.f, 0.f};
  const u32x4 ones = {0x3f803f80u, 0x3f803f80u, 0x3f803f80u, 0x3f803f80u};
#pragma unroll
  for (int j = 0; j < 9; ++j) cs = mfma_bf16(ones, u32x4{f0[j][0], f0[j][1], f1[j][0], f1[j][1]}, cs);
  float* ex = (float*)(smem + 36864);
  ex[tid] = cs[0];
  bar();
  const float mean = (kh ? ex[tid ^ 128] + cs[0] : cs[0] + ex[tid ^ 128]) * (1.0f / D_);
  char* dst = ws + OFF_W1 + ((size_t)((2 * strip + c) * 18 + kh * 9) * 2 + gu) * 1024 + lane * 16;
#pragma unroll
  for (int j = 0; j < 9; ++j) {
    u32x4 o;
    o[0] = pk_bf16(bf_lo(f0[j][0]) - mean, bf_hi(f0[j][0]) - mean); o[1] = pk_bf16(bf_lo(f0[j][1]) - mean, bf_hi(f0[j][1]) - mean);
    o[2] = pk_bf16(bf_lo(f1[j][0]) - mean, bf_hi(f1[j][0]) - mean); o[3] = pk_bf16(bf_lo(f1[j][1]) - mean, bf_hi(f1[j][1]) - mean);
    st16_wt(dst + j * 2048, o);
  }
}

DI void role_w2(int s, const u16* __restrict__ wd, char* ws, char* smem) {
  const int tid = tidx(), lane = tid & 63, w = __builtin_amdgcn_readfirstlane(tid >> 6);
  const char* src = (const char*)wd + (size_t)s * 36864 + w * 9216 + lane * 16;
#pragma unroll
  for (int j = 0; j < 9; ++j) glds16(src + j * 1024, smem + (w * 9 + j) * 1024);
  wait_vm0();
  bar();
  const int g = lane >> 4, a = (lane >> 2) & 3, p = lane & 3;
  const u32 adp = lds_off(smem) + (4 * g + a) * 1152 + w * 288 + p * 16;
  const u32 ads = lds_off(smem) + (4 * g + a) * 1152 + w * 288 + 256 + p * 8;
  u32x2 f0[9], f1[9];
#pragma unroll
  for (int t = 0; t < 8; ++t) {
    asm volatile("ds_read_b64_tr_b16 %0, %1 offset:%2" : "=v"(f0[t]) : "v"(adp), "i"((t >> 1) * 64 + (t & 1) * 8) : "memory");
    asm volatile("ds_read_b64_tr_b16 %0, %1 offset:%2" : "=v"(f1[t]) : "v"(adp), "i"((t >> 1) * 64 + (t & 1) * 8 + 16 * 1152) : "memory");
  }
  asm volatile("ds_read_b64_tr_b16 %0, %1" : "=v"(f0[8]) : "v"(ads) : "memory");
  asm volatile("ds_read_b64_tr_b16 %0, %1 offset:%2" : "=v"(f1[8]) : "v"(ads), "i"(16 * 1152) : "memory");
  asm volatile("s_waitcnt lgkmcnt(0)" : "+v"(f0[0]), "+v"(f0[1]), "+v"(f0[2]), "+v"(f0[3]), "+v"(f0[4]), "+v"(f0[5]), "+v"(f0[6]), "+v"(f0[7]), "+v"(f0[8]),
               "+v"(f1[0]), "+v"(f1[1]), "+v"(f1[2]), "+v"(f1[3]), "+v"(f1[4]), "+v"(f1[5]), "+v"(f1[6]), "+v"(f1[7]), "+v"(f1[8]) :: "memory");
  char* dst = ws + OFF_W2 + (size_t)(w * 144 + s) * 9216 + lane * 16;
#pragma unroll
  for (int t = 0; t < 9; ++t) st16_wt(dst + t * 1024, u32x4{f0[t][0], f0[t][1], f1[t][0], f1[t][1]});
}

DI void role_prep(int u, const u16* wg, const u16* wu, const u16* wd, char* ws, char* smem) {
  if (u < 288) role_w1(u, wg, wu, ws, smem); else role_w2(u - 288, wd, ws, smem);
}

#define NPREP1 208
constexpr int K1_LDS = 39168;
__global__ NOPK __launch_bounds__(256, 4) void k1_front(const u16* __restrict__ x, const u16* __restrict__ logA, const u16* __restrict__ Aim, const u16* __restrict__ Cre,
                                                const u16* __restrict__ Cim, const u16* __restrict__ logdt, const u16* __restrict__ dskip,
                                                const u16* __restrict__ wg, const u16* __restrict__ wu, const u16* __restrict__ wd, char* ws) {
  extern __shared__ __attribute__((aligned(16))) char smem[];
  STAMP_BEGIN
  const int b = bidx_();
  if (b < 64) role_ln1(b, x, dskip, ws, smem);
  else if (b < 640) role_kgen(b - 64, logA, Aim, Cre, Cim, logdt, ws, smem);
  else role_prep(b - 640, wg, wu, wd, ws, smem);
  STAMP_END(0);
}

constexpr int CH_LDS = 17664, K2_LDS = 4 * CH_LDS;
DI void role_conv(int bidx, char* ws, char* smem) {
  const int tid = tidx(), lane = tid & 63, w = __builtin_amdgcn_readfirstlane(tid >> 6);
  const int cg = ((bidx >> 3) & 1) + 2 * ((bidx & 7) + 8 * (bidx >> 4));
  char* reg = smem + w * CH_LDS;
  {
    const char* kp = ws + OFF_K + (size_t)(4 * cg + w) * 9216 + lane * 16;
    const char* gp = ws + OFF_G + (size_t)(4 * cg + w) * 4096 + lane * 16;
#pragma unroll
    for (int j = 0; j < 4; ++j) { glds16(gp + j * 1024, reg + 9216 + j * 1024); glds16(gp + j * 1024, reg + 9216 + 4228 + j * 1024); }
#pragma unroll
    for (int j = 0; j < 9; ++j) glds16(kp + j * 1024, reg + j * 1024);
  }
  char* hp = ws + OFF_HF + ((size_t)((tid >> 4) * 72 + (cg >> 1)) * 16 + (tid & 15)) * 16 + (cg & 1) * 8;
  u32x2 rv[8];
#pragma unroll
  for (int it = 0; it < 8; ++it) asm volatile("global_load_dwordx2 %0, %1, off" : "=v"(rv[it]) : "v"(hp + (size_t)it * 16 * 72 * 256) : "memory");
  if (lane < 32) *(u32*)(reg + 9216 + 4096 + (lane >> 4) * 4228 + (lane & 15) * 4) = 0u;
  asm volatile("s_waitcnt vmcnt(8) lgkmcnt(0)" ::: "memory");
  const int i = lane & 15, q = lane >> 4;
  const char* ka = reg + (i & 1) * 4640 + 64 * (i >> 1) + 16 * q;
  const char* ub = reg + 9216 + ((i & 1) ? 0 : 4228) + 3644 - 4 * i + 16 * q - 512 * 7;
  f32x4 acc[8] = {};
  u32x4 A[2][8], B[2][8];
#define CONV_LOAD(buf, t) _Pragma("unroll") for (int k = 0; k < 8; ++k) { \
      A[buf][k] = *(const u32x4*)(ka + 512 * k + 64 * (t)); \
      const u32x2 b0 = *(const u32x2*)(ub + 512 * (7 - k) + 64 * (t)), b1 = *(const u32x2*)(ub + 512 * (7 - k) + 64 * (t) + 8); \
      B[buf][k] = u32x4{b0[0], b0[1], b1[0], b1[1]}; }
#define CONV_COMP(buf) _Pragma("unroll") for (int T = 0; T < 8; ++T) _Pragma("unroll") for (int dd = 0; dd <= T; ++dd) acc[T] = mfma_bf16(A[buf][dd], B[buf][T - dd], acc[T]);
  CONV_LOAD(0, 0)
#pragma unroll 1
  for (int t = 0; t < 8; t += 2) {
    CONV_LOAD(1, t + 1)
    CONV_COMP(0)
    if (t < 6) { CONV_LOAD(0, t + 2) }
    CONV_COMP(1)
  }
  float* yw = (float*)reg + 64 * q + 2 * i;
#pragma unroll
  for (int T = 0; T < 8; ++T) {
    *(f32x2*)(yw + 256 * T) = f32x2{acc[T][0], acc[T][1]};
    *(f32x2*)(yw + 256 * T + 32) = f32x2{acc[T][2], acc[T][3]};
  }
  asm volatile("s_waitcnt vmcnt(0)" : "+v"(rv[0]), "+v"(rv[1]), "+v"(rv[2]), "+v"(rv[3]), "+v"(rv[4]), "+v"(rv[5]), "+v"(rv[6]), "+v"(rv[7]) :: "memory");
  bar();
#pragma unroll
  for (int it = 0; it < 8; ++it) {
    const float* y = (const float*)smem + 256 * it + tid;
    const u32x2 o = {pk_bf16(bf_lo(rv[it][0]) + y[0], bf_hi(rv[it][0]) + y[CH_LDS / 4]), pk_bf16(bf_lo(rv[it][1]) + y[2 * (CH_LDS / 4)], bf_hi(rv[it][1]) + y[3 * (CH_LDS / 4)])};
    *(u32x2*)(hp + (size_t)it * 16 * 72 * 256) = o;
  }
}

__global__ NOPK __launch_bounds__(256, 2) void k2_conv(const u16* __restrict__ wg, const u16* __restrict__ wu, const u16* __restrict__ wd, char* ws) {
  extern __shared__ __attribute__((aligned(16))) char smem[];
  STAMP_BEGIN
  const int b = bidx_();
  if (b < 144) role_conv(b, ws, smem);
  else role_prep(b - 144 + NPREP1, wg, wu, wd, ws, smem);
  STAMP_END(1);
}

__global__ NOPK __launch_bounds__(256) void ks_stats(char* ws) {
  STAMP_BEGIN
  const int lane = tidx() & 63, mb = bidx_() * 4 + (tidx() >> 6);
  const u32x4* hp = (const u32x4*)(ws + OFF_HF) + (size_t)mb * 72 * 16 + lane;
  float s = 0.f, q = 0.f;
  for (int j = 0; j < 18; ++j) { const u32x4 v = hp[j * 64];
    for (int e = 0; e < 4; ++e) { const float a = bf_lo(v[e]), b = bf_hi(v[e]); s += a + b; q = __builtin_fmaf(a, a, q); q = __builtin_fmaf(b, b, q); } }
  s += shx<16>(s); s += shx<32>(s); q += shx<16>(q); q += shx<32>(q);
  const float m = s * (1.0f / D_), var = __builtin_fmaxf(q * (1.0f / D_) - m * m, 0.f);
  if (lane < 16) ((float*)(ws + OFF_RS))[mb * 16 + lane] = 1.0f / __builtin_sqrtf(var + 1e-5f);
  STAMP_END(2);
}
__global__ NOPK __launch_bounds__(256) void ks_gemm1(char* ws) {
  STAMP_BEGIN
  const int lane = tidx() & 63, wv = bidx_() * 4 + (tidx() >> 6);
  const int mb = wv & 127, s = wv >> 7;
  const u32x4* xp = (const u32x4*)(ws + OFF_HF) + (size_t)mb * 18 * 64 + lane;
  const u32x4* wp = (const u32x4*)(ws + OFF_W1) + (size_t)s * 2 * 36 * 64 + lane;
  f32x4 acc[4] = {};
  for (int ks = 0; ks < 18; ++ks) { const u32x4 xv = xp[ks * 64];
    for (int t = 0; t < 2; ++t) for (int gu = 0; gu < 2; ++gu) acc[t * 2 + gu] = mfma_bf16(wp[((t * 18 + ks) * 2 + gu) * 64], xv, acc[t * 2 + gu]); }
  const float r = ((const float*)(ws + OFF_RS))[mb * 16 + (lane & 15)];
  const float k1 = -2.302208198f * r, k2 = -0.1029432397f * r * r * r;
  float o[8];
  for (int t = 0; t < 2; ++t) for (int e = 0; e < 4; ++e) { const float a = acc[t * 2][e], b = acc[t * 2 + 1][e];
    const float z = a * __builtin_fmaf(a * a, k2, k1); o[t * 4 + e] = a * b * __builtin_amdgcn_rcpf(1.0f + __builtin_amdgcn_exp2f(z)); }
  ((u32x4*)(ws + OFF_H))[((size_t)mb * 144 + s) * 64 + lane] = u32x4{pk_bf16(o[0], o[1]), pk_bf16(o[2], o[3]), pk_bf16(o[4], o[5]), pk_bf16(o[6], o[7])};
  STAMP_END(3);
}
__global__ NOPK __launch_bounds__(256) void ks_gemm2(char* ws, u16* __restrict__ out) {
  STAMP_BEGIN
  const int lane = tidx() & 63, wv = bidx_() * 4 + (tidx() >> 6);
  const int mb = wv >> 2, nb = wv & 3, m = lane & 15, q = lane >> 4;
  const u32x4* hp = (const u32x4*)(ws + OFF_H) + (size_t)mb * 144 * 64 + lane;
  const u32x4* wp = (const u32x4*)(ws + OFF_W2) + (size_t)nb * 144 * 9 * 64 + lane;
  f32x4 acc[9] = {};
  for (int s = 0; s < 144; ++s) { const u32x4 hv = hp[s * 64];
#pragma unroll
    for (int t = 0; t < 9; ++t) acc[t] = mfma_bf16(wp[(s * 9 + t) * 64], hv, acc[t]); }
  const float r = ((const float*)(ws + OFF_RS))[mb * 16 + m], r2 = r * r;
  const char* res = ws + OFF_HF + ((size_t)(mb * 72 + nb * 18) * 16 + m) * 16;
  u16* op = out + (size_t)(mb * 16 + m) * D_ + nb * 144;
#pragma unroll
  for (int a = 0; a < 4; ++a) { const u32x4 h = *(const u32x4*)(res + (4 * a + q) * 256); const f32x4 c0 = acc[2 * a], c1 = acc[2 * a + 1];
    *(u32x4*)(op + 32 * a + 8 * q) = u32x4{pk_bf16(__builtin_fmaf(c0[0], r2, bf_lo(h[0])), __builtin_fmaf(c0[1], r2, bf_hi(h[0]))), pk_bf16(__builtin_fmaf(c0[2], r2, bf_lo(h[1])), __builtin_fmaf(c0[3], r2, bf_hi(h[1]))),
                                           pk_bf16(__builtin_fmaf(c1[0], r2, bf_lo(h[2])), __builtin_fmaf(c1[1], r2, bf_hi(h[2]))), pk_bf16(__builtin_fmaf(c1[2], r2, bf_lo(h[3])), __builtin_fmaf(c1[3], r2, bf_hi(h[3])))}; }
  { const u32x2 h = *(const u32x2*)(res + (16 + (q >> 1)) * 256 + (q & 1) * 8); const f32x4 c0 = acc[8];
    *(u32x2*)(op + 128 + 4 * q) = u32x2{pk_bf16(__builtin_fmaf(c0[0], r2, bf_lo(h[0])), __builtin_fmaf(c0[1], r2, bf_hi(h[0]))), pk_bf16(__builtin_fmaf(c0[2], r2, bf_lo(h[1])), __builtin_fmaf(c0[3], r2, bf_hi(h[1])))}; }
  STAMP_END(4);
}

extern "C" void kernel_launch(void* const* d_in, const int* in_sizes, int n_in, void* d_out, int out_size, void* d_ws, size_t ws_size, hipStream_t stream) {
  const u16* x = (const u16*)d_in[0];
  const u16 *logA = (const u16*)d_in[1], *Aim = (const u16*)d_in[2], *Cre = (const u16*)d_in[3], *Cim = (const u16*)d_in[4];
  const u16 *logdt = (const u16*)d_in[5], *dskip = (const u16*)d_in[6];
  const u16 *wg = (const u16*)d_in[8], *wu = (const u16*)d_in[9], *wd = (const u16*)d_in[10];
  char* ws = (char*)d_ws;
  (void)hipFuncSetAttribute((const void*)k2_conv, hipFuncAttributeMaxDynamicSharedMemorySize, K2_LDS);
  k1_front<<<640 + NPREP1, 256, K1_LDS, stream>>>(x, logA, Aim, Cre, Cim, logdt, dskip, wg, wu, wd, ws);
  k2_conv<<<144 + 432 - NPREP1, 256, K2_LDS, stream>>>(wg, wu, wd, ws);
  ks_stats<<<32, 256, 0, stream>>>(ws);
  ks_gemm1<<<128 * 144 / 4, 256, 0, stream>>>(ws);
  ks_gemm2<<<128, 256, 0, stream>>>(ws, (u16*)d_out);
}
```

```cpp
#include <hip/hip_runtime.h>
#include <hip/hip_bf16.h>

constexpr int D_ = 576, NM_ = 128, F_ = 4608;
constexpr size_t OFF_HF = 0x240000;
constexpr size_t OFF_K  = 0x480000;
constexpr size_t OFF_W1 = 0x990000;
constexpr size_t OFF_W2 = 0x13B0000;
constexpr size_t OFF_RS = 0x2AC0000;
constexpr size_t OFF_FL = 0x2AD0000;

typedef unsigned u32;
typedef unsigned short u16;
typedef u32   u32x2 __attribute__((ext_vector_type(2)));
typedef u32   u32x4 __attribute__((ext_vector_type(4)));
typedef float f32x2 __attribute__((ext_vector_type(2)));
typedef float f32x4 __attribute__((ext_vector_type(4)));
typedef __bf16 bf16x2 __attribute__((ext_vector_type(2)));
typedef __bf16 bf16x8 __attribute__((ext_vector_type(8)));
typedef _Float16 f16x2 __attribute__((ext_vector_type(2)));
typedef _Float16 f16x8 __attribute__((ext_vector_type(8)));
#if defined(__HIP_DEVICE_COMPILE__)
#define NOPK __attribute__((target("no-packed-fp32-ops")))
#else
#define NOPK
#endif
#define DI __device__ __forceinline__ NOPK
#define GAS __attribute__((address_space(1)))
#define LAS __attribute__((address_space(3)))

DI int tidx() { return (int)__builtin_amdgcn_workitem_id_x(); }
DI int bidx_() { return (int)__builtin_amdgcn_workgroup_id_x(); }
DI float as_f(u32 x) { return __builtin_bit_cast(float, x); }
DI u32 as_u(float x) { return __builtin_bit_cast(u32, x); }
DI void bar() { __builtin_amdgcn_fence(__ATOMIC_RELEASE, "workgroup"); __builtin_amdgcn_s_barrier(); __builtin_amdgcn_fence(__ATOMIC_ACQUIRE, "workgroup"); }
DI float fexp(float x) { return __builtin_amdgcn_exp2f(x * 1.44269504088896f); }
DI u32 pk_bf16(float lo, float hi) { f32x2 v = {lo, hi}; return __builtin_bit_cast(u32, __builtin_convertvector(v, bf16x2)); }
DI u32 pk_f16(float lo, float hi) { f32x2 v = {lo, hi}; return __builtin_bit_cast(u32, __builtin_convertvector(v, f16x2)); }
DI float bf_lo(u32 w) { return as_f(w << 16); }
DI float bf_hi(u32 w) { return as_f(w & 0xffff0000u); }
DI float bf1(u16 h) { return as_f((u32)h << 16); }
DI float rbf(float x) { return bf_lo(pk_bf16(x, x)); }
DI f32x4 mfma_bf16(u32x4 a, u32x4 b, f32x4 c) { return __builtin_amdgcn_mfma_f32_16x16x32_bf16(__builtin_bit_cast(bf16x8, a), __builtin_bit_cast(bf16x8, b), c, 0, 0, 0); }
DI f32x4 mfma_f16(u32x4 a, u32x4 b, f32x4 c) { return __builtin_amdgcn_mfma_f32_16x16x32_f16(__builtin_bit_cast(f16x8, a), __builtin_bit_cast(f16x8, b), c, 0, 0, 0); }
DI void glds16(const void* g, void* l) { __builtin_amdgcn_global_load_lds((const GAS void*)g, (LAS void*)l, 16, 0, 0); }
DI u32 lds_off(const void* p) { return (u32)(size_t)(const LAS char*)p; }
DI void wait_vm0() { asm volatile("s_waitcnt vmcnt(0)" ::: "memory"); }
DI void st16_wt(void* p, u32x4 v) { asm volatile("global_store_dwordx4 %0, %1, off sc1\n\ts_nop 1" :: "v"(p), "v"(v) : "memory"); }
#define W1STORE(p, v) (*(GAS u32x4*)(p) = (v))

DI float row16_sum(float v) {
  v += as_f(__builtin_amdgcn_mov_dpp(as_u(v), 0x128, 0xF, 0xF, true)); v += as_f(__builtin_amdgcn_mov_dpp(as_u(v), 0x124, 0xF, 0xF, true));
  v += as_f(__builtin_amdgcn_mov_dpp(as_u(v), 0x122, 0xF, 0xF, true)); v += as_f(__builtin_amdgcn_mov_dpp(as_u(v), 0x121, 0xF, 0xF, true));
  return v;
}
DI void role_ln1(int bidx, const u16* __restrict__ x, const u16* __restrict__ dskip, char* ws, char* gbuf, char* smem) {
  const int tid = tidx(), lane = tid & 63, w = tid >> 6;
  const int b = (bidx & 7) * 8 + (bidx >> 3);
  const int rloc = tid >> 4, c = lane & 15;
  const bool last = c < 8;
  const GAS u32x4* xp = (const GAS u32x4*)(x + (32 * b + rloc) * D_) + c;
  const GAS u32x4* dp = (const GAS u32x4*)dskip + c;
  u32x4 xv[5], dv[5];
#pragma unroll
  for (int j = 0; j < 4; ++j) dv[j] = dp[16 * j];
  dv[4] = dp[last ? 64 : 0];
#pragma unroll
  for (int j = 0; j < 4; ++j) xv[j] = xp[16 * j];
  xv[4] = xp[last ? 64 : 0];
  if (tid < 32) ((u32*)(ws + OFF_RS))[32 * b + tid] = 0u;
  else if (tid >= 256) *(GAS u32x4*)(ws + OFF_FL + b * 4096 + (tid - 256) * 16) = u32x4{0u, 0u, 0u, 0u};
  float f[40], dk[40];
#pragma unroll
  for (int k = 0; k < 20; ++k) { dk[2 * k] = bf_lo(dv[k >> 2][k & 3]); dk[2 * k + 1] = bf_hi(dv[k >> 2][k & 3]); asm volatile("" : "+v"(dk[2 * k]), "+v"(dk[2 * k + 1])); }
  __builtin_amdgcn_sched_barrier(0);
  if (!last) xv[4] = u32x4{0u, 0u, 0u, 0u};
#pragma unroll
  for (int k = 0; k < 20; ++k) { f[2 * k] = bf_lo(xv[k >> 2][k & 3]); f[2 * k + 1] = bf_hi(xv[k >> 2][k & 3]); }
  float s0 = f[0], s1 = f[1], q0 = f[0] * f[0], q1 = f[1] * f[1];
#pragma unroll
  for (int k = 2; k < 40; k += 2) { s0 += f[k]; s1 += f[k + 1]; q0 = __builtin_fmaf(f[k], f[k], q0); q1 = __builtin_fmaf(f[k + 1], f[k + 1], q1); }
  const float mean = row16_sum(s0 + s1) * (1.0f / D_);
  const float var = __builtin_fmaxf(row16_sum(q0 + q1) * (1.0f / D_) - mean * mean, 0.f);
  const float inv = __builtin_amdgcn_rcpf(rbf(__builtin_amdgcn_sqrtf(rbf(rbf(var) + 1e-5f))));
  const float mb16 = rbf(mean), sh = -mb16 * inv;
  if (c == 0) ((float*)(ws + OFF_RS + 8192))[32 * b + rloc] = mb16;
  char* tile = smem + rloc * 1216 + ((rloc >> 3) & 1) * 32 + c * 16;
  char* rf = ws + OFF_HF + (size_t)(2 * b + (rloc >> 4)) * 18432 + c * 256 + (rloc & 15) * 16;
#pragma unroll
  for (int j = 0; j < 5; ++j) {
    u32x4 uu, rr;
#pragma unroll
    for (int e = 0; e < 4; ++e) {
      const int k = 8 * j + 2 * e;
      const float u0 = __builtin_fmaf(f[k], inv, sh), u1 = __builtin_fmaf(f[k + 1], inv, sh);
      uu[e] = pk_bf16(u0, u1);
      rr[e] = pk_bf16(__builtin_fmaf(u0, dk[k], f[k] - mb16), __builtin_fmaf(u1, dk[k + 1], f[k + 1] - mb16));
    }
    if (j < 4 || last) { *(u32x4*)(tile + j * 256) = uu; *(u32x4*)(rf + j * 4096) = rr; }
  }
  bar();
  const int g = lane >> 4, a = (lane >> 2) & 3, p = lane & 3;
  const u32 ad = lds_off(smem) + (31 - 8 * g - a) * 1216 + ((g & 1) ^ 1) * 32 + p * 8 + w * 32;
  u32x2 lo[5], hi[5];
#pragma unroll
  for (int j = 0; j < 5; ++j) {
    asm volatile("ds_read_b64_tr_b16 %0, %1 offset:%2" : "=v"(lo[j]) : "v"(ad), "i"(j * 256) : "memory");
    asm volatile("ds_read_b64_tr_b16 %0, %1 offset:%2" : "=v"(hi[j]) : "v"(ad - 4 * 1216), "i"(j * 256) : "memory");
  }
  asm volatile("s_waitcnt lgkmcnt(0)" : "+v"(lo[0]), "+v"(lo[1]), "+v"(lo[2]), "+v"(lo[3]), "+v"(lo[4]), "+v"(hi[0]), "+v"(hi[1]), "+v"(hi[2]), "+v"(hi[3]), "+v"(hi[4]) :: "memory");
  char* gp = gbuf + (size_t)(w * 16 + (lane & 15)) * 4096 + (2016 - 32 * b + 8 * g) * 2;
#pragma unroll
  for (int j = 0; j < 5; ++j) if (j < 4 || w < 4) *(u32x4*)(gp + (size_t)j * 128 * 4096) = u32x4{lo[j][0], lo[j][1], hi[j][0], hi[j][1]};
}

DI f16x2 cmul16(f16x2 y, u32 rr, u32 ii) {
  return __builtin_elementwise_fma(__builtin_shufflevector(y, y, 1, 0), __builtin_bit_cast(f16x2, ii), y * __builtin_bit_cast(f16x2, rr));
}
DI void role_kgen(int d2, const u16* __restrict__ logA, const u16* __restrict__ Aim, const u16* __restrict__ Cre, const u16* __restrict__ Cim,
                  const u16* __restrict__ logdt, char* ws, char* smem) {
  const int tid = tidx() & 255, lane = tid & 63, w8 = __builtin_amdgcn_readfirstlane(tidx() >> 6), w = w8 & 3, d = 2 * d2 + (w8 >> 2);
  smem += (w8 >> 2) * 20480;
  const int mode = d * NM_ + 32 * w + (lane & 31);
  const u16 h_la = logA[mode], h_ai = Aim[mode], h_cr = Cre[mode], h_ci = Cim[mode], h_dt = logdt[d];
  const int i = lane & 15, q = lane >> 4, col = 16 * w + i;
  int slot[4];
#pragma unroll
  for (int r = 0; r < 4; ++r) {
    const int lowr = w < 2 ? 1 + 4 * q + r + 8 * col : (r & 1) ? 660 : 129 + 2 * q + (r >> 1) + 4 * col;
    const int midr = r < 2 ? 385 + r + 2 * col : 513 + 64 * (r - 2) + col;
    const int topr = col != 0 || r > 1 ? 660 : r == 0 ? 641 : 0;
    slot[r] = 2 * (q < 2 ? lowr : q == 2 ? midr : topr);
  }
  const int hi8 = i - 8, big = hi8 == 0 ? 512 : hi8 == 1 ? 516 : hi8 << 9;
  float p1 = (float)(hi8 < 0 ? i : hi8 < 5 ? big : 0), p2 = (float)(8 * i);
  asm volatile("" : "+v"(slot[0]), "+v"(slot[1]), "+v"(slot[2]), "+v"(slot[3]), "+v"(p1), "+v"(p2));
  __builtin_amdgcn_sched_barrier(0);
  u32* tab = (u32*)(smem + w * 768);
  float* part = (float*)(smem + 4096);
  const float dt = rbf(fexp(bf1(h_dt)));
  const float ar = -rbf(fexp(bf1(h_la))) * dt * 1.44269504088896f;
  const float ai = bf1(h_ai) * dt * 0.159154943091895f;
  float cr = bf1(h_cr) * dt, ci = bf1(h_ci) * dt;
  u32 mx = __builtin_elementwise_max(as_u(cr) & 0x7fffffffu, as_u(ci) & 0x7fffffffu);
  mx = __builtin_elementwise_max(mx, (u32)__builtin_amdgcn_mov_dpp(mx, 0x128, 0xF, 0xF, true)); mx = __builtin_elementwise_max(mx, (u32)__builtin_amdgcn_mov_dpp(mx, 0x124, 0xF, 0xF, true));
  mx = __builtin_elementwise_max(mx, (u32)__builtin_amdgcn_mov_dpp(mx, 0x122, 0xF, 0xF, true)); mx = __builtin_elementwise_max(mx, (u32)__builtin_amdgcn_mov_dpp(mx, 0x121, 0xF, 0xF, true));
  { const auto sw = __builtin_amdgcn_permlane16_swap(mx, mx, false, false); mx = __builtin_elementwise_max((u32)sw[0], (u32)sw[1]); }
  const int ex = __builtin_amdgcn_frexp_expf(as_f(mx));
  cr = __builtin_amdgcn_ldexpf(cr, -ex); ci = __builtin_amdgcn_ldexpf(ci, -ex);
  {
    const float mg = __builtin_amdgcn_exp2f(ar * 128.f), ph = __builtin_amdgcn_fractf(ai * 128.f);
    const float er = mg * __builtin_amdgcn_cosf(ph), ei = mg * __builtin_amdgcn_sinf(ph);
    const bool lo = lane < 32;
    u32* t = tab + (lane & 31) + (lo ? 0 : 96);
    u32 h0 = pk_f16(ci, -ci), h1 = pk_f16(er, er);
    asm volatile("" : "+v"(h0), "+v"(h1));
    t[0] = lo ? as_u(ar) : h0; t[32] = lo ? as_u(ai) : h1; t[64] = pk_f16(lo ? cr : -ei, lo ? cr : ei);
  }
  __builtin_amdgcn_fence(__ATOMIC_RELEASE, "wavefront"); __builtin_amdgcn_wave_barrier(); __builtin_amdgcn_fence(__ATOMIC_ACQUIRE, "wavefront");
  f32x4 acc[4] = {};
#pragma unroll
  for (int s = 0; s < 2; ++s) {
    const u32* t = tab + 16 * s + 4 * q;
    const f32x4 tar = *(const f32x4*)t, tai = *(const f32x4*)(t + 32);
    const u32x4 tcr = *(const u32x4*)(t + 64), tci = *(const u32x4*)(t + 96), ter = *(const u32x4*)(t + 128), tei = *(const u32x4*)(t + 160);
    u32x4 xa, yb[4];
#pragma unroll
    for (int m = 0; m < 4; ++m) {
      const float mg = __builtin_amdgcn_exp2f(tar[m] * p1), ph = __builtin_amdgcn_fractf(tai[m] * p1);
      const f16x2 ec = __builtin_bit_cast(f16x2, pk_f16(mg * __builtin_amdgcn_cosf(ph), -mg * __builtin_amdgcn_sinf(ph)));
      xa[m] = __builtin_bit_cast(u32, cmul16(ec, tcr[m], tci[m]));
      const float mg2 = __builtin_amdgcn_exp2f(tar[m] * p2), ph2 = __builtin_amdgcn_fractf(tai[m] * p2);
      f16x2 y = __builtin_bit_cast(f16x2, pk_f16(mg2 * __builtin_amdgcn_cosf(ph2), mg2 * __builtin_amdgcn_sinf(ph2)));
      yb[0][m] = __builtin_bit_cast(u32, y);
#pragma unroll
      for (int ct = 1; ct < 4; ++ct) { y = cmul16(y, ter[m], tei[m]); yb[ct][m] = __builtin_bit_cast(u32, y); }
    }
#pragma unroll
    for (int ct = 0; ct < 4; ++ct) acc[ct] = mfma_f16(xa, yb[ct], acc[ct]);
  }
  const float unsc = __builtin_amdgcn_ldexpf(2.0f, ex);
#pragma unroll
  for (int ct = 0; ct < 4; ++ct) *(f32x4*)(part + ((w * 4 + ct) * 64 + lane) * 4) = acc[ct] * unsc;
  bar();
  const float* pp = part + tid * 4;
  const f32x4 sum = (*(const f32x4*)pp + *(const f32x4*)(pp + 1024)) + (*(const f32x4*)(pp + 2048) + *(const f32x4*)(pp + 3072));
  const u32 o01 = pk_bf16(sum[0], q == 3 ? 0.f : sum[1]), o23 = pk_bf16(sum[2], sum[3]);
  char* tb = smem + 2048;
  *(u16*)(tb + slot[0]) = (u16)o01; *(u16*)(tb + slot[1]) = (u16)(o01 >> 16); *(u16*)(tb + slot[2]) = (u16)o23; *(u16*)(tb + slot[3]) = (u16)(o23 >> 16);
  bar();
  if (tid < 81) *(GAS u32x4*)(ws + OFF_K + (size_t)d * 1408 + tid * 16) = *(const u32x4*)(tb + tid * 16);
}

template <int I> DI u32 centre2(u32 w, float mean) {
  return __builtin_amdgcn_perm(as_u(as_f(w) - mean), as_u(bf_lo(w) - mean) + ((I * 2 * 40503u + 20251u) & 0xffffu), 0x07060302u);
}
template <int J> DI u32x4 centre8(u32x2 a, u32x2 b, float mean) { return u32x4{centre2<4 * J>(a[0], mean), centre2<4 * J + 1>(a[1], mean), centre2<4 * J + 2>(b[0], mean), centre2<4 * J + 3>(b[1], mean)}; }
template <int J, int N, bool WT> DI void centre_store(const u32x2* f0, const u32x2* f1, float mean, char* dst) {
  if constexpr (J < N) { if constexpr (WT) st16_wt(dst + J * 2048, centre8<J>(f0[J], f1[J], mean)); else W1STORE(dst + J * 2048, centre8<J>(f0[J], f1[J], mean)); centre_store<J + 1, N, WT>(f0, f1, mean, dst); }
}

DI void role_w1(int u, const u16* __restrict__ wg, const u16* __restrict__ wu, char* ws, char* smem) {
  const int tid = tidx(), lane = tid & 63, w = __builtin_amdgcn_readfirstlane(tid >> 6);
  const int strip = u >> 1, gu = u & 1;
  {
    const int r = lane >> 3, ch = (lane & 7) ^ (r & 2);
    const u16* p = (gu ? wu : wg) + strip * 64 + (size_t)(w * 72 + r) * F_;
#pragma unroll
    for (int j = 0; j < 9; ++j) glds16(p + (size_t)j * 8 * F_ + (ch ^ (((w + j) & 1) << 2)) * 8, smem + (w * 9 + j) * 1024);
  }
  wait_vm0();
  bar();
  const int c = w & 3, kh = w >> 2, g = lane >> 4, a = (lane >> 2) & 3, p = lane & 3;
  const u32 ad = lds_off(smem) + (kh * 288 + 8 * g + a) * 128 + ((2 * c + (p >> 1)) ^ (a & 2) ^ ((g & 1) << 2)) * 16 + (p & 1) * 8;
  u32x2 f0[9], f1[9];
#pragma unroll
  for (int j = 0; j < 9; ++j) {
    asm volatile("ds_read_b64_tr_b16 %0, %1 offset:%2" : "=v"(f0[j]) : "v"(ad), "i"(j * 4096) : "memory");
    asm volatile("ds_read_b64_tr_b16 %0, %1 offset:%2" : "=v"(f1[j]) : "v"(ad), "i"(j * 4096 + 512) : "memory");
  }
  asm volatile("s_waitcnt lgkmcnt(0)" : "+v"(f0[0]), "+v"(f0[1]), "+v"(f0[2]), "+v"(f0[3]), "+v"(f0[4]), "+v"(f0[5]), "+v"(f0[6]), "+v"(f0[7]), "+v"(f0[8]),
               "+v"(f1[0]), "+v"(f1[1]), "+v"(f1[2]), "+v"(f1[3]), "+v"(f1[4]), "+v"(f1[5]), "+v"(f1[6]), "+v"(f1[7]), "+v"(f1[8]) :: "memory");
  f32x4 cs = {0.f, 0.f, 0.f, 0.f};
  const u32x4 ones = {0x3f803f80u, 0x3f803f80u, 0x3f803f80u, 0x3f803f80u};
#pragma unroll
  for (int j = 0; j < 9; ++j) cs = mfma_bf16(ones, u32x4{f0[j][0], f0[j][1], f1[j][0], f1[j][1]}, cs);
  float* ex = (float*)(smem + 73728);
  ex[tid] = cs[0];
  bar();
  const float mean = (kh ? ex[tid ^ 256] + cs[0] : cs[0] + ex[tid ^ 256]) * (1.0f / D_);
  char* dst = ws + OFF_W1 + ((size_t)((4 * strip + c) * 18 + kh * 9) * 2 + gu) * 1024 + lane * 16;
  centre_store<0, 9, false>(f0, f1, mean, dst);
}

DI void role_w1x4(int u, const u16* __restrict__ wg, const u16* __restrict__ wu, char* ws, char* smem) {
  const int tid = tidx(), lane = tid & 63, w = __builtin_amdgcn_readfirstlane(tid >> 6);
  const int strip = u >> 1, gu = u & 1;
  {
    const int r = lane >> 3, ch = (lane & 7) ^ (r & 2);
    const u16* p = (gu ? wu : wg) + strip * 64 + (size_t)(w * 144 + r) * F_;
#pragma unroll
    for (int j = 0; j < 18; ++j) glds16(p + (size_t)j * 8 * F_ + (ch ^ ((j & 1) << 2)) * 8, smem + (w * 18 + j) * 1024);
  }
  wait_vm0();
  bar();
  const int g = lane >> 4, a = (lane >> 2) & 3, p = lane & 3;
  const u32 ad = lds_off(smem) + (8 * g + a) * 128 + ((2 * w + (p >> 1)) ^ (a & 2) ^ ((g & 1) << 2)) * 16 + (p & 1) * 8;
  u32x2 f0[18], f1[18];
  f32x4 cs = {0.f, 0.f, 0.f, 0.f};
  const u32x4 ones = {0x3f803f80u, 0x3f803f80u, 0x3f803f80u, 0x3f803f80u};
#pragma unroll
  for (int h = 0; h < 18; h += 9) {
#pragma unroll
    for (int j = h; j < h + 9; ++j) {
      asm volatile("ds_read_b64_tr_b16 %0, %1 offset:%2" : "=v"(f0[j]) : "v"(ad + h * 4096), "i"((j - h) * 4096) : "memory");
      asm volatile("ds_read_b64_tr_b16 %0, %1 offset:%2" : "=v"(f1[j]) : "v"(ad + h * 4096), "i"((j - h) * 4096 + 512) : "memory");
    }
    asm volatile("s_waitcnt lgkmcnt(0)" : "+v"(f0[h]), "+v"(f0[h + 1]), "+v"(f0[h + 2]), "+v"(f0[h + 3]), "+v"(f0[h + 4]), "+v"(f0[h + 5]), "+v"(f0[h + 6]), "+v"(f0[h + 7]), "+v"(f0[h + 8]),
                 "+v"(f1[h]), "+v"(f1[h + 1]), "+v"(f1[h + 2]), "+v"(f1[h + 3]), "+v"(f1[h + 4]), "+v"(f1[h + 5]), "+v"(f1[h + 6]), "+v"(f1[h + 7]), "+v"(f1[h + 8]) :: "memory");
#pragma unroll
    for (int j = h; j < h + 9; ++j) cs = mfma_bf16(ones, u32x4{f0[j][0], f0[j][1], f1[j][0], f1[j][1]}, cs);
  }
  const float mean = cs[0] * (1.0f / D_);
  char* dst = ws + OFF_W1 + ((size_t)(4 * strip + w) * 36 + gu) * 1024 + lane * 16;
  centre_store<0, 18, true>(f0, f1, mean, dst);
}

DI void role_w2(int s2, const u16* __restrict__ wd, char* ws, char* smem) {
  const int tid = tidx(), lane = tid & 63, w = __builtin_amdgcn_readfirstlane(tid >> 6);
  const char* src = (const char*)wd + (size_t)s2 * 73728 + w * 18432 + lane * 16;
#pragma unroll
  for (int j = 0; j < 18; ++j) glds16(src + j * 1024, smem + (w * 18 + j) * 1024);
  wait_vm0();
  bar();
#pragma unroll 1
  for (int s = 2 * s2; s < 2 * s2 + 2; ++s, smem += 36864) {
  const int g = lane >> 4, a = (lane >> 2) & 3, p = lane & 3;
  const int kq = s / 36, sr = s - 36 * kq, a9 = sr / 9, r9 = sr - 9 * a9;
  const int u = r9 < 4 ? 8 * r9 + 2 * a9 : r9 == 4 ? 32 + a9 : 8 * (r9 - 5) + 2 * a9 + 1;
  const u32 row0 = lds_off(smem) + ((r9 == 4 ? 8 : 4) * g + a) * 1152 + w * 288, second = (r9 == 4 ? 4 : 16) * 1152;
  const u32 adp = row0 + p * 16, ads = row0 + 256 + p * 8;
  u32x2 f0[9], f1[9];
#pragma unroll
  for (int t = 0; t < 8; ++t) {
    asm volatile("ds_read_b64_tr_b16 %0, %1 offset:%2" : "=v"(f0[t]) : "v"(adp), "i"((t >> 1) * 64 + (t & 1) * 8) : "memory");
    asm volatile("ds_read_b64_tr_b16 %0, %1 offset:%2" : "=v"(f1[t]) : "v"(adp + second), "i"((t >> 1) * 64 + (t & 1) * 8) : "memory");
  }
  asm volatile("ds_read_b64_tr_b16 %0, %1" : "=v"(f0[8]) : "v"(ads) : "memory");
  asm volatile("ds_read_b64_tr_b16 %0, %1" : "=v"(f1[8]) : "v"(ads + second) : "memory");
  asm volatile("s_waitcnt lgkmcnt(0)" : "+v"(f0[0]), "+v"(f0[1]), "+v"(f0[2]), "+v"(f0[3]), "+v"(f0[4]), "+v"(f0[5]), "+v"(f0[6]), "+v"(f0[7]), "+v"(f0[8]),
               "+v"(f1[0]), "+v"(f1[1]), "+v"(f1[2]), "+v"(f1[3]), "+v"(f1[4]), "+v"(f1[5]), "+v"(f1[6]), "+v"(f1[7]), "+v"(f1[8]) :: "memory");
  char* dst = ws + OFF_W2 + (size_t)(w * 4 + kq) * 331776 + lane * 16;
#pragma unroll
  for (int t = 0; t < 9; ++t) {
    const int dd = ((t >> 1) - kq - 1) & 3;
    const int v = u - 24;
    const int pos = t < 8 && dd == 3 ? 252 + u * 2 + (t & 1) : v < 0 ? u * 7 + (t == 8 ? 6 : 2 * dd + (t & 1))
                  : t == 8 ? (6 + (v >> 2)) * 28 + 24 + (v & 3) : (6 + dd) * 28 + (v >> 1) * 4 + (t & 1) * 2 + (v & 1);
    st16_wt(dst + pos * 1024, u32x4{f0[t][0], f0[t][1], f1[t][0], f1[t][1]});
  }
  }
}

DI void role_codepf() {
  u32 lo, hi, elo;
  asm volatile("s_getpc_b64 s[40:41]\n\ts_add_u32 s42, s40, CODE_END_LABEL@rel32@lo+4\n\t"
               "s_getpc_b64 s[40:41]\n\ts_add_u32 s43, s40, CODE_END2@rel32@lo+4\n\t"
               "s_getpc_b64 s[40:41]\n\ts_add_u32 s44, s40, CODE_END3@rel32@lo+4\n\t"
               "s_getpc_b64 s[40:41]\n\ts_add_u32 s40, s40, k1_front@rel32@lo+4\n\ts_addc_u32 s41, s41, k1_front@rel32@hi+12\n\t"
               "s_sub_u32 s42, s42, s40\n\ts_sub_u32 s43, s43, s40\n\ts_sub_u32 s44, s44, s40\n\ts_max_u32 s42, s42, s43\n\ts_max_u32 s42, s42, s44\n\t"
               "s_mov_b32 %0, s40\n\ts_mov_b32 %1, s41\n\ts_mov_b32 %2, s42" : "=s"(lo), "=s"(hi), "=s"(elo) :: "s40", "s41", "s42", "s43", "s44", "scc");
  const GAS char* base = (const GAS char*)(((unsigned long long)hi << 32) | lo);
  const u32 len = elo, o = tidx() * 128;
  const u32 a0 = *(const GAS u32*)(base + (o < len ? o : 0));
  asm volatile("" :: "v"(a0));
}

constexpr int NPF = 8, K1_LDS = 75776;
extern "C" __global__ NOPK __launch_bounds__(512, 4) void k1_front(const u16* __restrict__ x, const u16* __restrict__ logA, const u16* __restrict__ Aim, const u16* __restrict__ Cre,
                                                const u16* __restrict__ Cim, const u16* __restrict__ logdt, const u16* __restrict__ dskip,
                                                const u16* __restrict__ wg, const u16* __restrict__ wu, char* ws, char* out) {
  extern __shared__ __attribute__((aligned(16))) char smem[];
  const int b = bidx_() - NPF;
  if (__builtin_expect((u32)(b - 64) < 288u, 1)) role_kgen(b - 64, logA, Aim, Cre, Cim, logdt, ws, smem);
  else if (__builtin_expect((u32)b < 64u, 1)) role_ln1(b, x, dskip, ws, out, smem);
  else if (b >= 0) {
    const int v = b - 352, t = v >> 1, q = t < 24 ? t / 6 : 4 + (t - 24) / 7, r = t < 24 ? t - 6 * q : t - 24 - 7 * (q - 4);
    role_w1(2 * (9 * q + r + (r > 0) + (q < 4 && r == 5)) + (v & 1), wg, wu, ws, smem);
  } else role_codepf();
}

constexpr int FFN_LDS = 163840;
__global__ NOPK __attribute__((aligned(4096))) __launch_bounds__(256, 1) void k2_main(const u16* __restrict__ wg, const u16* __restrict__ wu, const u16* __restrict__ wd, char* ws, char* out) {
  extern __shared__ __attribute__((aligned(16))) char smem[];
  const u32 t0 = (u32)__builtin_amdgcn_s_memrealtime();
  const int b = bidx_(), w = __builtin_amdgcn_readfirstlane(tidx() >> 6);
  if (__builtin_expect(b < 144, 1)) {
    asm volatile(
  "s_mov_b64 s[16:17], %0\n\t"
  "s_mov_b32 s18, %1\n\t"
  "s_mov_b32 s19, %2\n\t"
  "s_mov_b64 s[20:21], %3\n\t"
  "v_mbcnt_lo_u32_b32 v0, -1, 0\n\t"
  "v_mbcnt_hi_u32_b32 v0, -1, v0\n\t"
  "s_lshr_b32 s22, s18, 4\n\t"
  "s_lshl_b32 s22, s22, 3\n\t"
  "s_and_b32 s23, s18, 7\n\t"
  "s_add_u32 s31, s22, s23\n\t"
  "s_bfe_u32 s23, s18, 0x10003\n\t"
  "s_lshl_b32 s22, s31, 3\n\t"
  "s_lshl_b32 s30, s23, 2\n\t"
  "s_add_u32 s22, s22, s30\n\t"
  "s_add_u32 s27, s22, s19\n\t"
  "v_lshlrev_b32 v1, 4, v0\n\t"
  "s_mul_i32 s26, s19, 19712\n\t"
  "s_mul_i32 s22, s27, 1408\n\t"
  "s_add_u32 s22, s22, 4718592\n\t"
  "s_add_u32 m0, s26, 17664\n\t"
  "s_add_u32 s24, s16, s22\n\t"
  "s_addc_u32 s25, s17, 0\n\t"
  "global_load_lds_dwordx4 v1, s[24:25] offset:0\n\t"
  "global_load_lds_dwordx4 v1, s[24:25] offset:1024\n\t"
  "s_lshl_b32 s22, s27, 12\n\t"
  "s_add_u32 m0, s26, 9216\n\t"
  "s_add_u32 s24, s20, s22\n\t"
  "s_addc_u32 s25, s21, 0\n\t"
  "global_load_lds_dwordx4 v1, s[24:25] offset:0\n\t"
  "global_load_lds_dwordx4 v1, s[24:25] offset:1024\n\t"
  "global_load_lds_dwordx4 v1, s[24:25] offset:2048\n\t"
  "global_load_lds_dwordx4 v1, s[24:25] offset:3072\n\t"
  "s_add_u32 m0, s26, 13444\n\t"
  "s_nop 0\n\t"
  "global_load_lds_dwordx4 v1, s[24:25] offset:0\n\t"
  "global_load_lds_dwordx4 v1, s[24:25] offset:1024\n\t"
  "global_load_lds_dwordx4 v1, s[24:25] offset:2048\n\t"
  "global_load_lds_dwordx4 v1, s[24:25] offset:3072\n\t"
  "s_lshl_b32 s22, s31, 8\n\t"
  "s_lshl_b32 s23, s23, 3\n\t"
  "s_add_u32 s22, s22, s23\n\t"
  "s_mul_i32 s23, s19, 73728\n\t"
  "s_add_u32 s22, s22, s23\n\t"
  "s_add_u32 s22, s22, 2359296\n\t"
  "s_add_u32 s28, s16, s22\n\t"
  "s_addc_u32 s29, s17, 0\n\t"
  "v_and_b32 v10, 15, v0\n\t"
  "v_lshrrev_b32 v11, 4, v0\n\t"
  "v_mul_u32_u24 v5, 18432, v11\n\t"
  "v_lshl_add_u32 v5, v10, 4, v5\n\t"
  "v_mov_b32 v16, 0\n\t"
  "v_mov_b32 v17, 0\n\t"
  "v_mov_b32 v18, 0\n\t"
  "v_mov_b32 v19, 0\n\t"
  "v_mov_b32 v20, 0\n\t"
  "v_mov_b32 v21, 0\n\t"
  "v_mov_b32 v22, 0\n\t"
  "v_mov_b32 v23, 0\n\t"
  "v_mov_b32 v24, 0\n\t"
  "v_mov_b32 v25, 0\n\t"
  "v_mov_b32 v26, 0\n\t"
  "v_mov_b32 v27, 0\n\t"
  "v_mov_b32 v28, 0\n\t"
  "v_mov_b32 v29, 0\n\t"
  "v_mov_b32 v30, 0\n\t"
  "v_mov_b32 v31, 0\n\t"
  "v_mov_b32 v32, 0\n\t"
  "v_mov_b32 v33, 0\n\t"
  "v_mov_b32 v34, 0\n\t"
  "v_mov_b32 v35, 0\n\t"
  "v_mov_b32 v36, 0\n\t"
  "v_mov_b32 v37, 0\n\t"
  "v_mov_b32 v38, 0\n\t"
  "v_mov_b32 v39, 0\n\t"
  "v_mov_b32 v40, 0\n\t"
  "v_mov_b32 v41, 0\n\t"
  "v_mov_b32 v42, 0\n\t"
  "v_mov_b32 v43, 0\n\t"
  "v_mov_b32 v44, 0\n\t"
  "v_mov_b32 v45, 0\n\t"
  "v_mov_b32 v46, 0\n\t"
  "v_mov_b32 v47, 0\n\t"
  "v_and_b32 v12, 1, v11\n\t"
  "v_mul_u32_u24 v12, 4228, v12\n\t"
  "v_lshl_add_u32 v12, v10, 2, v12\n\t"
  "v_add_u32 v12, s26, v12\n\t"
  "ds_write_b32 v12, v16 offset:13312\n\t"
  "v_min_u32 v12, 55, v0\n\t"
  "v_cmp_lt_u32 vcc, 27, v12\n\t"
  "v_lshl_add_u32 v12, v12, 4, s26\n\t"
  "v_add_u32 v13, 4192, v12\n\t"
  "v_cndmask_b32 v12, v12, v13, vcc\n\t"
  "ds_write_b128 v12, v[16:19] offset:0\n\t"
  "v_and_b32 v12, 1, v10\n\t"
  "v_mul_u32_u24 v2, 4640, v12\n\t"
  "v_lshrrev_b32 v13, 1, v10\n\t"
  "v_lshl_add_u32 v2, v13, 6, v2\n\t"
  "v_lshl_add_u32 v2, v11, 4, v2\n\t"
  "v_add_u32 v2, s26, v2\n\t"
  "v_mul_u32_u24 v12, 4228, v12\n\t"
  "v_lshlrev_b32 v13, 2, v10\n\t"
  "v_add_u32 v12, v12, v13\n\t"
  "v_lshlrev_b32 v13, 4, v11\n\t"
  "v_sub_u32 v3, v13, v12\n\t"
  "s_add_u32 s22, s26, 13504\n\t"
  "v_add_u32 v3, s22, v3\n\t"
  "v_lshlrev_b32 v4, 8, v11\n\t"
  "v_lshl_add_u32 v4, v10, 3, v4\n\t"
  "v_add_u32 v4, s26, v4\n\t"
  "s_lshl_b32 s22, s19, 8\n\t"
  "v_lshl_add_u32 v6, v0, 2, s22\n\t"
  "v_add_u32 v7, 39424, v6\n\t"
  "v_lshlrev_b32 v9, 3, v0\n\t"
  "v_cvt_f32_u32 v8, v9\n\t"
  "s_add_u32 s22, s26, 17666\n\t"
  "s_sub_u32 s23, s22, 34048\n\t"
  "s_movk_i32 s31, 128\n\t"
  "v_add_f32 v112, 0xbf800000, v8\n\t"
  "v_add_f32 v113, 0x0, v8\n\t"
  "v_add_f32 v114, 0x3f800000, v8\n\t"
  "v_add_f32 v115, 0x40000000, v8\n\t"
  "v_add_f32 v116, 0x40400000, v8\n\t"
  "v_add_f32 v117, 0x40800000, v8\n\t"
  "v_add_f32 v118, 0x40a00000, v8\n\t"
  "v_add_f32 v119, 0x40c00000, v8\n\t"
  "v_add_f32 v120, 0x40e00000, v8\n\t"
  "v_max_f32 v112, 0, v112\n\t"
  "v_cvt_pk_bf16_f32 v12, v112, v113\n\t"
  "v_and_b32 v112, 0xffff, v12\n\t"
  "v_lshrrev_b32 v113, 16, v12\n\t"
  "v_cvt_pk_bf16_f32 v12, v114, v115\n\t"
  "v_and_b32 v114, 0xffff, v12\n\t"
  "v_lshrrev_b32 v115, 16, v12\n\t"
  "v_cvt_pk_bf16_f32 v12, v116, v117\n\t"
  "v_and_b32 v116, 0xffff, v12\n\t"
  "v_lshrrev_b32 v117, 16, v12\n\t"
  "v_cvt_pk_bf16_f32 v12, v118, v119\n\t"
  "v_and_b32 v118, 0xffff, v12\n\t"
  "v_lshrrev_b32 v119, 16, v12\n\t"
  "v_cvt_pk_bf16_f32 v12, v120, v120\n\t"
  "v_and_b32 v120, 0xffff, v12\n\t"
  "v_subrev_u32 v112, 0x4280, v112\n\t"
  "v_add_u32 v12, -1, v9\n\t"
  "v_med3_i32 v112, v112, v12, s31\n\t"
  "v_lshl_add_u32 v112, v112, 1, s22\n\t"
  "v_subrev_u32 v113, 0x4280, v113\n\t"
  "v_add_u32 v12, 0, v9\n\t"
  "v_med3_i32 v113, v113, v12, s31\n\t"
  "v_lshl_add_u32 v113, v113, 1, s22\n\t"
  "v_subrev_u32 v114, 0x4280, v114\n\t"
  "v_add_u32 v12, 1, v9\n\t"
  "v_med3_i32 v114, v114, v12, s31\n\t"
  "v_lshl_add_u32 v114, v114, 1, s22\n\t"
  "v_subrev_u32 v115, 0x4280, v115\n\t"
  "v_add_u32 v12, 2, v9\n\t"
  "v_med3_i32 v115, v115, v12, s31\n\t"
  "v_lshl_add_u32 v115, v115, 1, s22\n\t"
  "v_subrev_u32 v116, 0x4280, v116\n\t"
  "v_add_u32 v12, 3, v9\n\t"
  "v_med3_i32 v116, v116, v12, s31\n\t"
  "v_lshl_add_u32 v116, v116, 1, s22\n\t"
  "v_subrev_u32 v117, 0x4280, v117\n\t"
  "v_add_u32 v12, 4, v9\n\t"
  "v_med3_i32 v117, v117, v12, s31\n\t"
  "v_lshl_add_u32 v117, v117, 1, s22\n\t"
  "v_subrev_u32 v118, 0x4280, v118\n\t"
  "v_add_u32 v12, 5, v9\n\t"
  "v_med3_i32 v118, v118, v12, s31\n\t"
  "v_lshl_add_u32 v118, v118, 1, s22\n\t"
  "v_subrev_u32 v119, 0x4280, v119\n\t"
  "v_add_u32 v12, 6, v9\n\t"
  "v_med3_i32 v119, v119, v12, s31\n\t"
  "v_lshl_add_u32 v119, v119, 1, s22\n\t"
  "v_subrev_u32 v120, 0x4280, v120\n\t"
  "v_add_u32 v12, 7, v9\n\t"
  "v_med3_i32 v120, v120, v12, s31\n\t"
  "v_lshl_add_u32 v120, v120, 1, s22\n\t"
  "v_add_f32 v121, 0x43ff8000, v8\n\t"
  "v_add_f32 v122, 0x44000000, v8\n\t"
  "v_add_f32 v123, 0x44004000, v8\n\t"
  "v_add_f32 v124, 0x44008000, v8\n\t"
  "v_add_f32 v125, 0x4400c000, v8\n\t"
  "v_add_f32 v126, 0x44010000, v8\n\t"
  "v_add_f32 v127, 0x44014000, v8\n\t"
  "v_add_f32 v128, 0x44018000, v8\n\t"
  "v_add_f32 v129, 0x4401c000, v8\n\t"
  "v_cvt_pk_bf16_f32 v12, v121, v122\n\t"
  "v_and_b32 v121, 0xffff, v12\n\t"
  "v_lshrrev_b32 v122, 16, v12\n\t"
  "v_cvt_pk_bf16_f32 v12, v123, v124\n\t"
  "v_and_b32 v123, 0xffff, v12\n\t"
  "v_lshrrev_b32 v124, 16, v12\n\t"
  "v_cvt_pk_bf16_f32 v12, v125, v126\n\t"
  "v_and_b32 v125, 0xffff, v12\n\t"
  "v_lshrrev_b32 v126, 16, v12\n\t"
  "v_cvt_pk_bf16_f32 v12, v127, v128\n\t"
  "v_and_b32 v127, 0xffff, v12\n\t"
  "v_lshrrev_b32 v128, 16, v12\n\t"
  "v_cvt_pk_bf16_f32 v12, v129, v129\n\t"
  "v_and_b32 v129, 0xffff, v12\n\t"
  "v_lshl_add_u32 v121, v121, 1, s23\n\t"
  "v_lshl_add_u32 v122, v122, 1, s23\n\t"
  "v_lshl_add_u32 v123, v123, 1, s23\n\t"
  "v_lshl_add_u32 v124, v124, 1, s23\n\t"
  "v_lshl_add_u32 v125, v125, 1, s23\n\t"
  "v_lshl_add_u32 v126, v126, 1, s23\n\t"
  "v_lshl_add_u32 v127, v127, 1, s23\n\t"
  "v_lshl_add_u32 v128, v128, 1, s23\n\t"
  "v_lshl_add_u32 v129, v129, 1, s23\n\t"
  "v_add_f32 v130, 0x447fc000, v8\n\t"
  "v_add_f32 v131, 0x44800000, v8\n\t"
  "v_add_f32 v132, 0x44802000, v8\n\t"
  "v_add_f32 v133, 0x44804000, v8\n\t"
  "v_add_f32 v134, 0x44806000, v8\n\t"
  "v_add_f32 v135, 0x44808000, v8\n\t"
  "v_add_f32 v136, 0x4480a000, v8\n\t"
  "v_add_f32 v137, 0x4480c000, v8\n\t"
  "v_add_f32 v138, 0x4480e000, v8\n\t"
  "v_cvt_pk_bf16_f32 v12, v130, v131\n\t"
  "v_and_b32 v130, 0xffff, v12\n\t"
  "v_lshrrev_b32 v131, 16, v12\n\t"
  "v_cvt_pk_bf16_f32 v12, v132, v133\n\t"
  "v_and_b32 v132, 0xffff, v12\n\t"
  "v_lshrrev_b32 v133, 16, v12\n\t"
  "v_cvt_pk_bf16_f32 v12, v134, v135\n\t"
  "v_and_b32 v134, 0xffff, v12\n\t"
  "v_lshrrev_b32 v135, 16, v12\n\t"
  "v_cvt_pk_bf16_f32 v12, v136, v137\n\t"
  "v_and_b32 v136, 0xffff, v12\n\t"
  "v_lshrrev_b32 v137, 16, v12\n\t"
  "v_cvt_pk_bf16_f32 v12, v138, v138\n\t"
  "v_and_b32 v138, 0xffff, v12\n\t"
  "v_lshl_add_u32 v130, v130, 1, s23\n\t"
  "v_lshl_add_u32 v131, v131, 1, s23\n\t"
  "v_lshl_add_u32 v132, v132, 1, s23\n\t"
  "v_lshl_add_u32 v133, v133, 1, s23\n\t"
  "v_lshl_add_u32 v134, v134, 1, s23\n\t"
  "v_lshl_add_u32 v135, v135, 1, s23\n\t"
  "v_lshl_add_u32 v136, v136, 1, s23\n\t"
  "v_lshl_add_u32 v137, v137, 1, s23\n\t"
  "v_lshl_add_u32 v138, v138, 1, s23\n\t"
  "v_add_f32 v139, 0x44bfe000, v8\n\t"
  "v_add_f32 v140, 0x44c00000, v8\n\t"
  "v_add_f32 v141, 0x44c02000, v8\n\t"
  "v_add_f32 v142, 0x44c04000, v8\n\t"
  "v_add_f32 v143, 0x44c06000, v8\n\t"
  "v_add_f32 v144, 0x44c08000, v8\n\t"
  "v_add_f32 v145, 0x44c0a000, v8\n\t"
  "v_add_f32 v146, 0x44c0c000, v8\n\t"
  "v_add_f32 v147, 0x44c0e000, v8\n\t"
  "v_cvt_pk_bf16_f32 v12, v139, v140\n\t"
  "v_and_b32 v139, 0xffff, v12\n\t"
  "v_lshrrev_b32 v140, 16, v12\n\t"
  "v_cvt_pk_bf16_f32 v12, v141, v142\n\t"
  "v_and_b32 v141, 0xffff, v12\n\t"
  "v_lshrrev_b32 v142, 16, v12\n\t"
  "v_cvt_pk_bf16_f32 v12, v143, v144\n\t"
  "v_and_b32 v143, 0xffff, v12\n\t"
  "v_lshrrev_b32 v144, 16, v12\n\t"
  "v_cvt_pk_bf16_f32 v12, v145, v146\n\t"
  "v_and_b32 v145, 0xffff, v12\n\t"
  "v_lshrrev_b32 v146, 16, v12\n\t"
  "v_cvt_pk_bf16_f32 v12, v147, v147\n\t"
  "v_and_b32 v147, 0xffff, v12\n\t"
  "v_lshl_add_u32 v139, v139, 1, s23\n\t"
  "v_lshl_add_u32 v140, v140, 1, s23\n\t"
  "v_lshl_add_u32 v141, v141, 1, s23\n\t"
  "v_lshl_add_u32 v142, v142, 1, s23\n\t"
  "v_lshl_add_u32 v143, v143, 1, s23\n\t"
  "v_lshl_add_u32 v144, v144, 1, s23\n\t"
  "v_lshl_add_u32 v145, v145, 1, s23\n\t"
  "v_lshl_add_u32 v146, v146, 1, s23\n\t"
  "v_lshl_add_u32 v147, v147, 1, s23\n\t"
  "v_add_u32 v13, s26, v1\n\t"
  "s_waitcnt vmcnt(8)\n\t"
  "ds_read_u16 v139, v139\n\t"
  "ds_read_u16 v140, v140\n\t"
  "ds_read_u16 v141, v141\n\t"
  "ds_read_u16 v142, v142\n\t"
  "ds_read_u16 v143, v143\n\t"
  "ds_read_u16 v144, v144\n\t"
  "ds_read_u16 v145, v145\n\t"
  "ds_read_u16 v146, v146\n\t"
  "ds_read_u16 v147, v147\n\t"
  "ds_read_u16 v130, v130\n\t"
  "ds_read_u16 v131, v131\n\t"
  "ds_read_u16 v132, v132\n\t"
  "ds_read_u16 v133, v133\n\t"
  "ds_read_u16 v134, v134\n\t"
  "ds_read_u16 v135, v135\n\t"
  "ds_read_u16 v136, v136\n\t"
  "ds_read_u16 v137, v137\n\t"
  "ds_read_u16 v138, v138\n\t"
  "ds_read_u16 v121, v121\n\t"
  "ds_read_u16 v122, v122\n\t"
  "ds_read_u16 v123, v123\n\t"
  "ds_read_u16 v124, v124\n\t"
  "ds_read_u16 v125, v125\n\t"
  "ds_read_u16 v126, v126\n\t"
  "ds_read_u16 v127, v127\n\t"
  "ds_read_u16 v128, v128\n\t"
  "ds_read_u16 v129, v129\n\t"
  "s_waitcnt lgkmcnt(15)\n\t"
  "v_lshl_or_b32 v72, v140, 16, v139\n\t"
  "v_lshl_or_b32 v73, v142, 16, v141\n\t"
  "v_lshl_or_b32 v74, v144, 16, v143\n\t"
  "v_lshl_or_b32 v75, v146, 16, v145\n\t"
  "ds_write_b128 v13, v[72:75] offset:3520\n\t"
  "v_lshl_or_b32 v76, v141, 16, v140\n\t"
  "v_lshl_or_b32 v77, v143, 16, v142\n\t"
  "v_lshl_or_b32 v78, v145, 16, v144\n\t"
  "v_lshl_or_b32 v79, v147, 16, v146\n\t"
  "ds_write_b128 v13, v[76:79] offset:8160\n\t"
  "ds_read_u16 v112, v112\n\t"
  "ds_read_u16 v113, v113\n\t"
  "ds_read_u16 v114, v114\n\t"
  "ds_read_u16 v115, v115\n\t"
  "ds_read_u16 v116, v116\n\t"
  "ds_read_u16 v117, v117\n\t"
  "ds_read_u16 v118, v118\n\t"
  "ds_read_u16 v119, v119\n\t"
  "ds_read_u16 v120, v120\n\t"
  "s_waitcnt lgkmcnt(15)\n\t"
  "v_lshl_or_b32 v64, v131, 16, v130\n\t"
  "v_lshl_or_b32 v65, v133, 16, v132\n\t"
  "v_lshl_or_b32 v66, v135, 16, v134\n\t"
  "v_lshl_or_b32 v67, v137, 16, v136\n\t"
  "ds_write_b128 v13, v[64:67] offset:2496\n\t"
  "v_lshl_or_b32 v68, v132, 16, v131\n\t"
  "v_lshl_or_b32 v69, v134, 16, v133\n\t"
  "v_lshl_or_b32 v70, v136, 16, v135\n\t"
  "v_lshl_or_b32 v71, v138, 16, v137\n\t"
  "ds_write_b128 v13, v[68:71] offset:7136\n\t"
  "s_waitcnt lgkmcnt(13)\n\t"
  "v_lshl_or_b32 v56, v122, 16, v121\n\t"
  "v_lshl_or_b32 v57, v124, 16, v123\n\t"
  "v_lshl_or_b32 v58, v126, 16, v125\n\t"
  "v_lshl_or_b32 v59, v128, 16, v127\n\t"
  "ds_write_b128 v13, v[56:59] offset:1472\n\t"
  "v_lshl_or_b32 v60, v123, 16, v122\n\t"
  "v_lshl_or_b32 v61, v125, 16, v124\n\t"
  "v_lshl_or_b32 v62, v127, 16, v126\n\t"
  "v_lshl_or_b32 v63, v129, 16, v128\n\t"
  "ds_write_b128 v13, v[60:63] offset:6112\n\t"
  "s_waitcnt lgkmcnt(4)\n\t"
  "v_lshl_or_b32 v48, v113, 16, v112\n\t"
  "v_lshl_or_b32 v49, v115, 16, v114\n\t"
  "v_lshl_or_b32 v50, v117, 16, v116\n\t"
  "v_lshl_or_b32 v51, v119, 16, v118\n\t"
  "ds_write_b128 v13, v[48:51] offset:448\n\t"
  "v_lshl_or_b32 v52, v114, 16, v113\n\t"
  "v_lshl_or_b32 v53, v116, 16, v115\n\t"
  "v_lshl_or_b32 v54, v118, 16, v117\n\t"
  "v_lshl_or_b32 v55, v120, 16, v119\n\t"
  "ds_write_b128 v13, v[52:55] offset:5088\n\t"
  "s_waitcnt vmcnt(0) lgkmcnt(0)\n\t"
  "ds_read_b128 v[48:51], v2 offset:0\n\t"
  "ds_read_b64 v[80:81], v3 offset:3584\n\t"
  "ds_read_b64 v[82:83], v3 offset:3592\n\t"
  "ds_read_b128 v[52:55], v2 offset:512\n\t"
  "ds_read_b64 v[84:85], v3 offset:3072\n\t"
  "ds_read_b64 v[86:87], v3 offset:3080\n\t"
  "ds_read_b128 v[56:59], v2 offset:1024\n\t"
  "ds_read_b64 v[88:89], v3 offset:2560\n\t"
  "ds_read_b64 v[90:91], v3 offset:2568\n\t"
  "ds_read_b128 v[60:63], v2 offset:1536\n\t"
  "ds_read_b64 v[92:93], v3 offset:2048\n\t"
  "ds_read_b64 v[94:95], v3 offset:2056\n\t"
  "ds_read_b128 v[64:67], v2 offset:2048\n\t"
  "ds_read_b64 v[96:97], v3 offset:1536\n\t"
  "ds_read_b64 v[98:99], v3 offset:1544\n\t"
  "ds_read_b128 v[68:71], v2 offset:2560\n\t"
  "ds_read_b64 v[100:101], v3 offset:1024\n\t"
  "ds_read_b64 v[102:103], v3 offset:1032\n\t"
  "ds_read_b128 v[72:75], v2 offset:3072\n\t"
  "ds_read_b64 v[104:105], v3 offset:512\n\t"
  "ds_read_b64 v[106:107], v3 offset:520\n\t"
  "ds_read_b128 v[76:79], v2 offset:3584\n\t"
  "ds_read_b64 v[108:109], v3 offset:0\n\t"
  "ds_read_b64 v[110:111], v3 offset:8\n\t"
  "s_waitcnt lgkmcnt(15)\n\t"
  "v_mfma_f32_16x16x32_bf16 v[16:19], v[48:51], v[80:83], v[16:19]\n\t"
  "s_waitcnt lgkmcnt(15)\n\t"
  "v_mfma_f32_16x16x32_bf16 v[20:23], v[48:51], v[84:87], v[20:23]\n\t"
  "v_mfma_f32_16x16x32_bf16 v[20:23], v[52:55], v[80:83], v[20:23]\n\t"
  "s_waitcnt lgkmcnt(15)\n\t"
  "v_mfma_f32_16x16x32_bf16 v[24:27], v[48:51], v[88:91], v[24:27]\n\t"
  "v_mfma_f32_16x16x32_bf16 v[24:27], v[52:55], v[84:87], v[24:27]\n\t"
  "global_load_dwordx2 v[176:177], v5, s[28:29] offset:0\n\t"
  "v_mfma_f32_16x16x32_bf16 v[24:27], v[56:59], v[80:83], v[24:27]\n\t"
  "s_add_u32 s28, s28, 294912\n\t"
  "s_addc_u32 s29, s29, 0\n\t"
  "s_waitcnt lgkmcnt(12)\n\t"
  "v_mfma_f32_16x16x32_bf16 v[28:31], v[48:51], v[92:95], v[28:31]\n\t"
  "v_mfma_f32_16x16x32_bf16 v[28:31], v[52:55], v[88:91], v[28:31]\n\t"
  "v_mfma_f32_16x16x32_bf16 v[28:31], v[56:59], v[84:87], v[28:31]\n\t"
  "ds_read_b128 v[112:115], v2 offset:64\n\t"
  "v_mfma_f32_16x16x32_bf16 v[28:31], v[60:63], v[80:83], v[28:31]\n\t"
  "ds_read_b64 v[144:145], v3 offset:3648\n\t"
  "s_waitcnt lgkmcnt(11)\n\t"
  "v_mfma_f32_16x16x32_bf16 v[32:35], v[48:51], v[96:99], v[32:35]\n\t"
  "ds_read_b64 v[146:147], v3 offset:3656\n\t"
  "v_mfma_f32_16x16x32_bf16 v[32:35], v[52:55], v[92:95], v[32:35]\n\t"
  "ds_read_b128 v[116:119], v2 offset:576\n\t"
  "v_mfma_f32_16x16x32_bf16 v[32:35], v[56:59], v[88:91], v[32:35]\n\t"
  "ds_read_b64 v[148:149], v3 offset:3136\n\t"
  "v_mfma_f32_16x16x32_bf16 v[32:35], v[60:63], v[84:87], v[32:35]\n\t"
  "ds_read_b64 v[150:151], v3 offset:3144\n\t"
  "v_mfma_f32_16x16x32_bf16 v[32:35], v[64:67], v[80:83], v[32:35]\n\t"
  "ds_read_b128 v[120:123], v2 offset:1088\n\t"
  "s_waitcnt lgkmcnt(13)\n\t"
  "v_mfma_f32_16x16x32_bf16 v[36:39], v[48:51], v[100:103], v[36:39]\n\t"
  "ds_read_b64 v[152:153], v3 offset:2624\n\t"
  "global_load_dwordx2 v[178:179], v5, s[28:29] offset:0\n\t"
  "v_mfma_f32_16x16x32_bf16 v[36:39], v[52:55], v[96:99], v[36:39]\n\t"
  "ds_read_b64 v[154:155], v3 offset:2632\n\t"
  "s_add_u32 s28, s28, 294912\n\t"
  "s_addc_u32 s29, s29, 0\n\t"
  "v_mfma_f32_16x16x32_bf16 v[36:39], v[56:59], v[92:95], v[36:39]\n\t"
  "ds_read_b128 v[124:127], v2 offset:1600\n\t"
  "v_mfma_f32_16x16x32_bf16 v[36:39], v[60:63], v[88:91], v[36:39]\n\t"
  "ds_read_b64 v[156:157], v3 offset:2112\n\t"
  "v_mfma_f32_16x16x32_bf16 v[36:39], v[64:67], v[84:87], v[36:39]\n\t"
  "ds_read_b64 v[158:159], v3 offset:2120\n\t"
  "v_mfma_f32_16x16x32_bf16 v[36:39], v[68:71], v[80:83], v[36:39]\n\t"
  "ds_read_b128 v[128:131], v2 offset:2112\n\t"
  "s_waitcnt lgkmcnt(15)\n\t"
  "v_mfma_f32_16x16x32_bf16 v[40:43], v[48:51], v[104:107], v[40:43]\n\t"
  "ds_read_b64 v[160:161], v3 offset:1600\n\t"
  "v_mfma_f32_16x16x32_bf16 v[40:43], v[52:55], v[100:103], v[40:43]\n\t"
  "ds_read_b64 v[162:163], v3 offset:1608\n\t"
  "v_mfma_f32_16x16x32_bf16 v[40:43], v[56:59], v[96:99], v[40:43]\n\t"
  "ds_read_b128 v[132:135], v2 offset:2624\n\t"
  "v_mfma_f32_16x16x32_bf16 v[40:43], v[60:63], v[92:95], v[40:43]\n\t"
  "ds_read_b64 v[164:165], v3 offset:1088\n\t"
  "global_load_dwordx2 v[180:181], v5, s[28:29] offset:0\n\t"
  "v_mfma_f32_16x16x32_bf16 v[40:43], v[64:67], v[88:91], v[40:43]\n\t"
  "ds_read_b64 v[166:167], v3 offset:1096\n\t"
  "s_add_u32 s28, s28, 294912\n\t"
  "s_addc_u32 s29, s29, 0\n\t"
  "v_mfma_f32_16x16x32_bf16 v[40:43], v[68:71], v[84:87], v[40:43]\n\t"
  "ds_read_b128 v[136:139], v2 offset:3136\n\t"
  "v_mfma_f32_16x16x32_bf16 v[40:43], v[72:75], v[80:83], v[40:43]\n\t"
  "ds_read_b64 v[168:169], v3 offset:576\n\t"
  "s_waitcnt lgkmcnt(15)\n\t"
  "v_mfma_f32_16x16x32_bf16 v[44:47], v[48:51], v[108:111], v[44:47]\n\t"
  "ds_read_b64 v[170:171], v3 offset:584\n\t"
  "v_mfma_f32_16x16x32_bf16 v[44:47], v[52:55], v[104:107], v[44:47]\n\t"
  "ds_read_b128 v[140:143], v2 offset:3648\n\t"
  "v_mfma_f32_16x16x32_bf16 v[44:47], v[56:59], v[100:103], v[44:47]\n\t"
  "ds_read_b64 v[172:173], v3 offset:64\n\t"
  "v_mfma_f32_16x16x32_bf16 v[44:47], v[60:63], v[96:99], v[44:47]\n\t"
  "ds_read_b64 v[174:175], v3 offset:72\n\t"
  "v_mfma_f32_16x16x32_bf16 v[44:47], v[64:67], v[92:95], v[44:47]\n\t"
  "v_mfma_f32_16x16x32_bf16 v[44:47], v[68:71], v[88:91], v[44:47]\n\t"
  "v_mfma_f32_16x16x32_bf16 v[44:47], v[72:75], v[84:87], v[44:47]\n\t"
  "v_mfma_f32_16x16x32_bf16 v[44:47], v[76:79], v[80:83], v[44:47]\n\t"
  "global_load_dwordx2 v[182:183], v5, s[28:29] offset:0\n\t"
  "s_waitcnt lgkmcnt(0)\n\t"
  "v_mfma_f32_16x16x32_bf16 v[16:19], v[112:115], v[144:147], v[16:19]\n\t"
  "ds_read_b128 v[48:51], v2 offset:128\n\t"
  "s_add_u32 s28, s28, 294912\n\t"
  "s_addc_u32 s29, s29, 0\n\t"
  "v_mfma_f32_16x16x32_bf16 v[20:23], v[112:115], v[148:151], v[20:23]\n\t"
  "ds_read_b64 v[80:81], v3 offset:3712\n\t"
  "v_mfma_f32_16x16x32_bf16 v[20:23], v[116:119], v[144:147], v[20:23]\n\t"
  "ds_read_b64 v[82:83], v3 offset:3720\n\t"
  "v_mfma_f32_16x16x32_bf16 v[24:27], v[112:115], v[152:155], v[24:27]\n\t"
  "ds_read_b128 v[52:55], v2 offset:640\n\t"
  "v_mfma_f32_16x16x32_bf16 v[24:27], v[116:119], v[148:151], v[24:27]\n\t"
  "ds_read_b64 v[84:85], v3 offset:3200\n\t"
  "v_mfma_f32_16x16x32_bf16 v[24:27], v[120:123], v[144:147], v[24:27]\n\t"
  "ds_read_b64 v[86:87], v3 offset:3208\n\t"
  "v_mfma_f32_16x16x32_bf16 v[28:31], v[112:115], v[156:159], v[28:31]\n\t"
  "ds_read_b128 v[56:59], v2 offset:1152\n\t"
  "v_mfma_f32_16x16x32_bf16 v[28:31], v[116:119], v[152:155], v[28:31]\n\t"
  "ds_read_b64 v[88:89], v3 offset:2688\n\t"
  "v_mfma_f32_16x16x32_bf16 v[28:31], v[120:123], v[148:151], v[28:31]\n\t"
  "ds_read_b64 v[90:91], v3 offset:2696\n\t"
  "v_mfma_f32_16x16x32_bf16 v[28:31], v[124:127], v[144:147], v[28:31]\n\t"
  "ds_read_b128 v[60:63], v2 offset:1664\n\t"
  "global_load_dwordx2 v[184:185], v5, s[28:29] offset:0\n\t"
  "v_mfma_f32_16x16x32_bf16 v[32:35], v[112:115], v[160:163], v[32:35]\n\t"
  "ds_read_b64 v[92:93], v3 offset:2176\n\t"
  "s_add_u32 s28, s28, 294912\n\t"
  "s_addc_u32 s29, s29, 0\n\t"
  "v_mfma_f32_16x16x32_bf16 v[32:35], v[116:119], v[156:159], v[32:35]\n\t"
  "ds_read_b64 v[94:95], v3 offset:2184\n\t"
  "v_mfma_f32_16x16x32_bf16 v[32:35], v[120:123], v[152:155], v[32:35]\n\t"
  "ds_read_b128 v[64:67], v2 offset:2176\n\t"
  "v_mfma_f32_16x16x32_bf16 v[32:35], v[124:127], v[148:151], v[32:35]\n\t"
  "ds_read_b64 v[96:97], v3 offset:1664\n\t"
  "v_mfma_f32_16x16x32_bf16 v[32:35], v[128:131], v[144:147], v[32:35]\n\t"
  "ds_read_b64 v[98:99], v3 offset:1672\n\t"
  "v_mfma_f32_16x16x32_bf16 v[36:39], v[112:115], v[164:167], v[36:39]\n\t"
  "ds_read_b128 v[68:71], v2 offset:2688\n\t"
  "v_mfma_f32_16x16x32_bf16 v[36:39], v[116:119], v[160:163], v[36:39]\n\t"
  "ds_read_b64 v[100:101], v3 offset:1152\n\t"
  "v_mfma_f32_16x16x32_bf16 v[36:39], v[120:123], v[156:159], v[36:39]\n\t"
  "ds_read_b64 v[102:103], v3 offset:1160\n\t"
  "v_mfma_f32_16x16x32_bf16 v[36:39], v[124:127], v[152:155], v[36:39]\n\t"
  "ds_read_b128 v[72:75], v2 offset:3200\n\t"
  "v_mfma_f32_16x16x32_bf16 v[36:39], v[128:131], v[148:151], v[36:39]\n\t"
  "ds_read_b64 v[104:105], v3 offset:640\n\t"
  "global_load_dwordx2 v[186:187], v5, s[28:29] offset:0\n\t"
  "v_mfma_f32_16x16x32_bf16 v[36:39], v[132:135], v[144:147], v[36:39]\n\t"
  "ds_read_b64 v[106:107], v3 offset:648\n\t"
  "s_add_u32 s28, s28, 294912\n\t"
  "s_addc_u32 s29, s29, 0\n\t"
  "v_mfma_f32_16x16x32_bf16 v[40:43], v[112:115], v[168:171], v[40:43]\n\t"
  "ds_read_b128 v[76:79], v2 offset:3712\n\t"
  "v_mfma_f32_16x16x32_bf16 v[40:43], v[116:119], v[164:167], v[40:43]\n\t"
  "ds_read_b64 v[108:109], v3 offset:128\n\t"
  "v_mfma_f32_16x16x32_bf16 v[40:43], v[120:123], v[160:163], v[40:43]\n\t"
  "ds_read_b64 v[110:111], v3 offset:136\n\t"
  "v_mfma_f32_16x16x32_bf16 v[40:43], v[124:127], v[156:159], v[40:43]\n\t"
  "v_mfma_f32_16x16x32_bf16 v[40:43], v[128:131], v[152:155], v[40:43]\n\t"
  "v_mfma_f32_16x16x32_bf16 v[40:43], v[132:135], v[148:151], v[40:43]\n\t"
  "v_mfma_f32_16x16x32_bf16 v[40:43], v[136:139], v[144:147], v[40:43]\n\t"
  "v_mfma_f32_16x16x32_bf16 v[44:47], v[112:115], v[172:175], v[44:47]\n\t"
  "v_mfma_f32_16x16x32_bf16 v[44:47], v[116:119], v[168:171], v[44:47]\n\t"
  "v_mfma_f32_16x16x32_bf16 v[44:47], v[120:123], v[164:167], v[44:47]\n\t"
  "global_load_dwordx2 v[188:189], v5, s[28:29] offset:0\n\t"
  "v_mfma_f32_16x16x32_bf16 v[44:47], v[124:127], v[160:163], v[44:47]\n\t"
  "s_add_u32 s28, s28, 294912\n\t"
  "s_addc_u32 s29, s29, 0\n\t"
  "v_mfma_f32_16x16x32_bf16 v[44:47], v[128:131], v[156:159], v[44:47]\n\t"
  "v_mfma_f32_16x16x32_bf16 v[44:47], v[132:135], v[152:155], v[44:47]\n\t"
  "v_mfma_f32_16x16x32_bf16 v[44:47], v[136:139], v[148:151], v[44:47]\n\t"
  "v_mfma_f32_16x16x32_bf16 v[44:47], v[140:143], v[144:147], v[44:47]\n\t"
  "s_waitcnt lgkmcnt(0)\n\t"
  "v_mfma_f32_16x16x32_bf16 v[16:19], v[48:51], v[80:83], v[16:19]\n\t"
  "ds_read_b128 v[112:115], v2 offset:192\n\t"
  "v_mfma_f32_16x16x32_bf16 v[20:23], v[48:51], v[84:87], v[20:23]\n\t"
  "ds_read_b64 v[144:145], v3 offset:3776\n\t"
  "v_mfma_f32_16x16x32_bf16 v[20:23], v[52:55], v[80:83], v[20:23]\n\t"
  "ds_read_b64 v[146:147], v3 offset:3784\n\t"
  "v_mfma_f32_16x16x32_bf16 v[24:27], v[48:51], v[88:91], v[24:27]\n\t"
  "ds_read_b128 v[116:119], v2 offset:704\n\t"
  "v_mfma_f32_16x16x32_bf16 v[24:27], v[52:55], v[84:87], v[24:27]\n\t"
  "ds_read_b64 v[148:149], v3 offset:3264\n\t"
  "global_load_dwordx2 v[190:191], v5, s[28:29] offset:0\n\t"
  "v_mfma_f32_16x16x32_bf16 v[24:27], v[56:59], v[80:83], v[24:27]\n\t"
  "ds_read_b64 v[150:151], v3 offset:3272\n\t"
  "s_add_u32 s28, s28, 294912\n\t"
  "s_addc_u32 s29, s29, 0\n\t"
  "v_mfma_f32_16x16x32_bf16 v[28:31], v[48:51], v[92:95], v[28:31]\n\t"
  "ds_read_b128 v[120:123], v2 offset:1216\n\t"
  "s_sub_u32 s28, s28, 2359296\n\t"
  "s_subb_u32 s29, s29, 0\n\t"
  "v_mfma_f32_16x16x32_bf16 v[28:31], v[52:55], v[88:91], v[28:31]\n\t"
  "ds_read_b64 v[152:153], v3 offset:2752\n\t"
  "s_bfe_u32 s22, s18, 0x20001\n\t"
  "v_mfma_f32_16x16x32_bf16 v[28:31], v[56:59], v[84:87], v[28:31]\n\t"
  "ds_read_b64 v[154:155], v3 offset:2760\n\t"
  "s_lshl_b32 s22, s22, 3\n\t"
  "v_mfma_f32_16x16x32_bf16 v[28:31], v[60:63], v[80:83], v[28:31]\n\t"
  "ds_read_b128 v[124:127], v2 offset:1728\n\t"
  "s_lshr_b32 s23, s18, 5\n\t"
  "v_mfma_f32_16x16x32_bf16 v[32:35], v[48:51], v[96:99], v[32:35]\n\t"
  "ds_read_b64 v[156:157], v3 offset:2240\n\t"
  "s_add_u32 s22, s22, s23\n\t"
  "v_mfma_f32_16x16x32_bf16 v[32:35], v[52:55], v[92:95], v[32:35]\n\t"
  "ds_read_b64 v[158:159], v3 offset:2248\n\t"
  "s_and_b32 s23, s22, 1\n\t"
  "v_mfma_f32_16x16x32_bf16 v[32:35], v[56:59], v[88:91], v[32:35]\n\t"
  "ds_read_b128 v[128:131], v2 offset:2240\n\t"
  "s_mul_i32 s22, s22, 9\n\t"
  "v_mfma_f32_16x16x32_bf16 v[32:35], v[60:63], v[84:87], v[32:35]\n\t"
  "ds_read_b64 v[160:161], v3 offset:1728\n\t"
  "s_add_u32 s22, s22, s23\n\t"
  "v_mfma_f32_16x16x32_bf16 v[32:35], v[64:67], v[80:83], v[32:35]\n\t"
  "ds_read_b64 v[162:163], v3 offset:1736\n\t"
  "s_mul_i32 s22, s22, 36864\n\t"
  "v_mfma_f32_16x16x32_bf16 v[36:39], v[48:51], v[100:103], v[36:39]\n\t"
  "ds_read_b128 v[132:135], v2 offset:2752\n\t"
  "s_add_u32 s22, s22, 10027008\n\t"
  "v_mfma_f32_16x16x32_bf16 v[36:39], v[52:55], v[96:99], v[36:39]\n\t"
  "ds_read_b64 v[164:165], v3 offset:1216\n\t"
  "s_add_u32 s34, s16, s22\n\t"
  "s_addc_u32 s35, s17, 0\n\t"
  "v_mfma_f32_16x16x32_bf16 v[36:39], v[56:59], v[92:95], v[36:39]\n\t"
  "ds_read_b64 v[166:167], v3 offset:1224\n\t"
  "s_mul_i32 s23, s19, 3072\n\t"
  "v_mfma_f32_16x16x32_bf16 v[36:39], v[60:63], v[88:91], v[36:39]\n\t"
  "ds_read_b128 v[136:139], v2 offset:3264\n\t"
  "s_mul_i32 s22, s19, 3\n\t"
  "v_mfma_f32_16x16x32_bf16 v[36:39], v[64:67], v[84:87], v[36:39]\n\t"
  "ds_read_b64 v[168:169], v3 offset:704\n\t"
  "s_add_u32 s22, s22, 0\n\t"
  "v_mfma_f32_16x16x32_bf16 v[36:39], v[68:71], v[80:83], v[36:39]\n\t"
  "ds_read_b64 v[170:171], v3 offset:712\n\t"
  "s_lshr_b32 s22, s22, 2\n\t"
  "v_mfma_f32_16x16x32_bf16 v[40:43], v[48:51], v[104:107], v[40:43]\n\t"
  "ds_read_b128 v[140:143], v2 offset:3776\n\t"
  "s_lshl_b32 s22, s22, 15\n\t"
  "v_mfma_f32_16x16x32_bf16 v[40:43], v[52:55], v[100:103], v[40:43]\n\t"
  "ds_read_b64 v[172:173], v3 offset:192\n\t"
  "s_add_u32 s22, s22, s23\n\t"
  "v_mfma_f32_16x16x32_bf16 v[40:43], v[56:59], v[96:99], v[40:43]\n\t"
  "ds_read_b64 v[174:175], v3 offset:200\n\t"
  "v_add_u32 v196, s22, v1\n\t"
  "v_mfma_f32_16x16x32_bf16 v[40:43], v[60:63], v[92:95], v[40:43]\n\t"
  "s_mul_i32 s22, s19, 3\n\t"
  "v_mfma_f32_16x16x32_bf16 v[40:43], v[64:67], v[88:91], v[40:43]\n\t"
  "s_add_u32 s22, s22, 1\n\t"
  "v_mfma_f32_16x16x32_bf16 v[40:43], v[68:71], v[84:87], v[40:43]\n\t"
  "s_lshr_b32 s22, s22, 2\n\t"
  "v_mfma_f32_16x16x32_bf16 v[40:43], v[72:75], v[80:83], v[40:43]\n\t"
  "s_lshl_b32 s22, s22, 15\n\t"
  "v_mfma_f32_16x16x32_bf16 v[44:47], v[48:51], v[108:111], v[44:47]\n\t"
  "s_add_u32 s22, s22, s23\n\t"
  "v_mfma_f32_16x16x32_bf16 v[44:47], v[52:55], v[104:107], v[44:47]\n\t"
  "v_add_u32 v197, s22, v1\n\t"
  "v_mfma_f32_16x16x32_bf16 v[44:47], v[56:59], v[100:103], v[44:47]\n\t"
  "s_mul_i32 s22, s19, 3\n\t"
  "v_mfma_f32_16x16x32_bf16 v[44:47], v[60:63], v[96:99], v[44:47]\n\t"
  "s_add_u32 s22, s22, 2\n\t"
  "v_mfma_f32_16x16x32_bf16 v[44:47], v[64:67], v[92:95], v[44:47]\n\t"
  "s_lshr_b32 s22, s22, 2\n\t"
  "v_mfma_f32_16x16x32_bf16 v[44:47], v[68:71], v[88:91], v[44:47]\n\t"
  "s_lshl_b32 s22, s22, 15\n\t"
  "v_mfma_f32_16x16x32_bf16 v[44:47], v[72:75], v[84:87], v[44:47]\n\t"
  "s_add_u32 s22, s22, s23\n\t"
  "v_mfma_f32_16x16x32_bf16 v[44:47], v[76:79], v[80:83], v[44:47]\n\t"
  "v_add_u32 v198, s22, v1\n\t"
  "s_waitcnt lgkmcnt(0)\n\t"
  "v_mfma_f32_16x16x32_bf16 v[16:19], v[112:115], v[144:147], v[16:19]\n\t"
  "ds_read_b128 v[48:51], v2 offset:256\n\t"
  "s_add_u32 m0, s23, 126976\n\t"
  "v_mfma_f32_16x16x32_bf16 v[20:23], v[112:115], v[148:151], v[20:23]\n\t"
  "ds_read_b64 v[80:81], v3 offset:3840\n\t"
  "s_add_u32 s24, s34, 0\n\t"
  "s_addc_u32 s25, s35, 0\n\t"
  "v_mfma_f32_16x16x32_bf16 v[20:23], v[116:119], v[144:147], v[20:23]\n\t"
  "ds_read_b64 v[82:83], v3 offset:3848\n\t"
  "global_load_lds_dwordx4 v196, s[24:25] offset:0\n\t"
  "v_mfma_f32_16x16x32_bf16 v[24:27], v[112:115], v[152:155], v[24:27]\n\t"
  "ds_read_b128 v[52:55], v2 offset:768\n\t"
  "global_load_lds_dwordx4 v197, s[24:25] offset:1024\n\t"
  "v_mfma_f32_16x16x32_bf16 v[24:27], v[116:119], v[148:151], v[24:27]\n\t"
  "ds_read_b64 v[84:85], v3 offset:3328\n\t"
  "global_load_lds_dwordx4 v198, s[24:25] offset:2048\n\t"
  "v_mfma_f32_16x16x32_bf16 v[24:27], v[120:123], v[144:147], v[24:27]\n\t"
  "ds_read_b64 v[86:87], v3 offset:3336\n\t"
  "s_add_u32 m0, s23, 139264\n\t"
  "v_mfma_f32_16x16x32_bf16 v[28:31], v[112:115], v[156:159], v[28:31]\n\t"
  "ds_read_b128 v[56:59], v2 offset:1280\n\t"
  "s_add_u32 s24, s34, 4096\n\t"
  "s_addc_u32 s25, s35, 0\n\t"
  "v_mfma_f32_16x16x32_bf16 v[28:31], v[116:119], v[152:155], v[28:31]\n\t"
  "ds_read_b64 v[88:89], v3 offset:2816\n\t"
  "v_mfma_f32_16x16x32_bf16 v[28:31], v[120:123], v[148:151], v[28:31]\n\t"
  "ds_read_b64 v[90:91], v3 offset:2824\n\t"
  "v_mfma_f32_16x16x32_bf16 v[28:31], v[124:127], v[144:147], v[28:31]\n\t"
  "ds_read_b128 v[60:63], v2 offset:1792\n\t"
  "v_mfma_f32_16x16x32_bf16 v[32:35], v[112:115], v[160:163], v[32:35]\n\t"
  "ds_read_b64 v[92:93], v3 offset:2304\n\t"
  "global_load_lds_dwordx4 v196, s[24:25] offset:0\n\t"
  "v_mfma_f32_16x16x32_bf16 v[32:35], v[116:119], v[156:159], v[32:35]\n\t"
  "ds_read_b64 v[94:95], v3 offset:2312\n\t"
  "v_mfma_f32_16x16x32_bf16 v[32:35], v[120:123], v[152:155], v[32:35]\n\t"
  "ds_read_b128 v[64:67], v2 offset:2304\n\t"
  "v_mfma_f32_16x16x32_bf16 v[32:35], v[124:127], v[148:151], v[32:35]\n\t"
  "ds_read_b64 v[96:97], v3 offset:1792\n\t"
  "v_mfma_f32_16x16x32_bf16 v[32:35], v[128:131], v[144:147], v[32:35]\n\t"
  "ds_read_b64 v[98:99], v3 offset:1800\n\t"
  "v_mfma_f32_16x16x32_bf16 v[36:39], v[112:115], v[164:167], v[36:39]\n\t"
  "ds_read_b128 v[68:71], v2 offset:2816\n\t"
  "v_mfma_f32_16x16x32_bf16 v[36:39], v[116:119], v[160:163], v[36:39]\n\t"
  "ds_read_b64 v[100:101], v3 offset:1280\n\t"
  "v_mfma_f32_16x16x32_bf16 v[36:39], v[120:123], v[156:159], v[36:39]\n\t"
  "ds_read_b64 v[102:103], v3 offset:1288\n\t"
  "v_mfma_f32_16x16x32_bf16 v[36:39], v[124:127], v[152:155], v[36:39]\n\t"
  "ds_read_b128 v[72:75], v2 offset:3328\n\t"
  "v_mfma_f32_16x16x32_bf16 v[36:39], v[128:131], v[148:151], v[36:39]\n\t"
  "ds_read_b64 v[104:105], v3 offset:768\n\t"
  "v_mfma_f32_16x16x32_bf16 v[36:39], v[132:135], v[144:147], v[36:39]\n\t"
  "ds_read_b64 v[106:107], v3 offset:776\n\t"
  "global_load_lds_dwordx4 v197, s[24:25] offset:1024\n\t"
  "v_mfma_f32_16x16x32_bf16 v[40:43], v[112:115], v[168:171], v[40:43]\n\t"
  "ds_read_b128 v[76:79], v2 offset:3840\n\t"
  "v_mfma_f32_16x16x32_bf16 v[40:43], v[116:119], v[164:167], v[40:43]\n\t"
  "ds_read_b64 v[108:109], v3 offset:256\n\t"
  "v_mfma_f32_16x16x32_bf16 v[40:43], v[120:123], v[160:163], v[40:43]\n\t"
  "ds_read_b64 v[110:111], v3 offset:264\n\t"
  "v_mfma_f32_16x16x32_bf16 v[40:43], v[124:127], v[156:159], v[40:43]\n\t"
  "v_mfma_f32_16x16x32_bf16 v[40:43], v[128:131], v[152:155], v[40:43]\n\t"
  "v_mfma_f32_16x16x32_bf16 v[40:43], v[132:135], v[148:151], v[40:43]\n\t"
  "v_mfma_f32_16x16x32_bf16 v[40:43], v[136:139], v[144:147], v[40:43]\n\t"
  "v_mfma_f32_16x16x32_bf16 v[44:47], v[112:115], v[172:175], v[44:47]\n\t"
  "v_mfma_f32_16x16x32_bf16 v[44:47], v[116:119], v[168:171], v[44:47]\n\t"
  "v_mfma_f32_16x16x32_bf16 v[44:47], v[120:123], v[164:167], v[44:47]\n\t"
  "v_mfma_f32_16x16x32_bf16 v[44:47], v[124:127], v[160:163], v[44:47]\n\t"
  "global_load_lds_dwordx4 v198, s[24:25] offset:2048\n\t"
  "v_mfma_f32_16x16x32_bf16 v[44:47], v[128:131], v[156:159], v[44:47]\n\t"
  "s_add_u32 m0, s23, 151552\n\t"
  "v_mfma_f32_16x16x32_bf16 v[44:47], v[132:135], v[152:155], v[44:47]\n\t"
  "s_add_u32 s24, s34, 8192\n\t"
  "s_addc_u32 s25, s35, 0\n\t"
  "v_mfma_f32_16x16x32_bf16 v[44:47], v[136:139], v[148:151], v[44:47]\n\t"
  "v_mfma_f32_16x16x32_bf16 v[44:47], v[140:143], v[144:147], v[44:47]\n\t"
  "s_waitcnt lgkmcnt(0)\n\t"
  "v_mfma_f32_16x16x32_bf16 v[16:19], v[48:51], v[80:83], v[16:19]\n\t"
  "ds_read_b128 v[112:115], v2 offset:320\n\t"
  "v_mfma_f32_16x16x32_bf16 v[20:23], v[48:51], v[84:87], v[20:23]\n\t"
  "ds_read_b64 v[144:145], v3 offset:3904\n\t"
  "v_mfma_f32_16x16x32_bf16 v[20:23], v[52:55], v[80:83], v[20:23]\n\t"
  "ds_read_b64 v[146:147], v3 offset:3912\n\t"
  "v_mfma_f32_16x16x32_bf16 v[24:27], v[48:51], v[88:91], v[24:27]\n\t"
  "ds_read_b128 v[116:119], v2 offset:832\n\t"
  "v_mfma_f32_16x16x32_bf16 v[24:27], v[52:55], v[84:87], v[24:27]\n\t"
  "ds_read_b64 v[148:149], v3 offset:3392\n\t"
  "v_mfma_f32_16x16x32_bf16 v[24:27], v[56:59], v[80:83], v[24:27]\n\t"
  "ds_read_b64 v[150:151], v3 offset:3400\n\t"
  "global_load_lds_dwordx4 v196, s[24:25] offset:0\n\t"
  "v_mfma_f32_16x16x32_bf16 v[28:31], v[48:51], v[92:95], v[28:31]\n\t"
  "ds_read_b128 v[120:123], v2 offset:1344\n\t"
  "v_mfma_f32_16x16x32_bf16 v[28:31], v[52:55], v[88:91], v[28:31]\n\t"
  "ds_read_b64 v[152:153], v3 offset:2880\n\t"
  "v_mfma_f32_16x16x32_bf16 v[28:31], v[56:59], v[84:87], v[28:31]\n\t"
  "ds_read_b64 v[154:155], v3 offset:2888\n\t"
  "v_mfma_f32_16x16x32_bf16 v[28:31], v[60:63], v[80:83], v[28:31]\n\t"
  "ds_read_b128 v[124:127], v2 offset:1856\n\t"
  "v_mfma_f32_16x16x32_bf16 v[32:35], v[48:51], v[96:99], v[32:35]\n\t"
  "ds_read_b64 v[156:157], v3 offset:2368\n\t"
  "v_mfma_f32_16x16x32_bf16 v[32:35], v[52:55], v[92:95], v[32:35]\n\t"
  "ds_read_b64 v[158:159], v3 offset:2376\n\t"
  "v_mfma_f32_16x16x32_bf16 v[32:35], v[56:59], v[88:91], v[32:35]\n\t"
  "ds_read_b128 v[128:131], v2 offset:2368\n\t"
  "v_mfma_f32_16x16x32_bf16 v[32:35], v[60:63], v[84:87], v[32:35]\n\t"
  "ds_read_b64 v[160:161], v3 offset:1856\n\t"
  "v_mfma_f32_16x16x32_bf16 v[32:35], v[64:67], v[80:83], v[32:35]\n\t"
  "ds_read_b64 v[162:163], v3 offset:1864\n\t"
  "v_mfma_f32_16x16x32_bf16 v[36:39], v[48:51], v[100:103], v[36:39]\n\t"
  "ds_read_b128 v[132:135], v2 offset:2880\n\t"
  "global_load_lds_dwordx4 v197, s[24:25] offset:1024\n\t"
  "v_mfma_f32_16x16x32_bf16 v[36:39], v[52:55], v[96:99], v[36:39]\n\t"
  "ds_read_b64 v[164:165], v3 offset:1344\n\t"
  "v_mfma_f32_16x16x32_bf16 v[36:39], v[56:59], v[92:95], v[36:39]\n\t"
  "ds_read_b64 v[166:167], v3 offset:1352\n\t"
  "v_mfma_f32_16x16x32_bf16 v[36:39], v[60:63], v[88:91], v[36:39]\n\t"
  "ds_read_b128 v[136:139], v2 offset:3392\n\t"
  "v_mfma_f32_16x16x32_bf16 v[36:39], v[64:67], v[84:87], v[36:39]\n\t"
  "ds_read_b64 v[168:169], v3 offset:832\n\t"
  "v_mfma_f32_16x16x32_bf16 v[36:39], v[68:71], v[80:83], v[36:39]\n\t"
  "ds_read_b64 v[170:171], v3 offset:840\n\t"
  "v_mfma_f32_16x16x32_bf16 v[40:43], v[48:51], v[104:107], v[40:43]\n\t"
  "ds_read_b128 v[140:143], v2 offset:3904\n\t"
  "v_mfma_f32_16x16x32_bf16 v[40:43], v[52:55], v[100:103], v[40:43]\n\t"
  "ds_read_b64 v[172:173], v3 offset:320\n\t"
  "v_mfma_f32_16x16x32_bf16 v[40:43], v[56:59], v[96:99], v[40:43]\n\t"
  "ds_read_b64 v[174:175], v3 offset:328\n\t"
  "v_mfma_f32_16x16x32_bf16 v[40:43], v[60:63], v[92:95], v[40:43]\n\t"
  "v_mfma_f32_16x16x32_bf16 v[40:43], v[64:67], v[88:91], v[40:43]\n\t"
  "v_mfma_f32_16x16x32_bf16 v[40:43], v[68:71], v[84:87], v[40:43]\n\t"
  "global_load_lds_dwordx4 v198, s[24:25] offset:2048\n\t"
  "v_mfma_f32_16x16x32_bf16 v[40:43], v[72:75], v[80:83], v[40:43]\n\t"
  "s_add_u32 m0, s23, 90112\n\t"
  "v_mfma_f32_16x16x32_bf16 v[44:47], v[48:51], v[108:111], v[44:47]\n\t"
  "s_add_u32 s24, s34, 12288\n\t"
  "s_addc_u32 s25, s35, 0\n\t"
  "v_mfma_f32_16x16x32_bf16 v[44:47], v[52:55], v[104:107], v[44:47]\n\t"
  "v_mfma_f32_16x16x32_bf16 v[44:47], v[56:59], v[100:103], v[44:47]\n\t"
  "v_mfma_f32_16x16x32_bf16 v[44:47], v[60:63], v[96:99], v[44:47]\n\t"
  "v_mfma_f32_16x16x32_bf16 v[44:47], v[64:67], v[92:95], v[44:47]\n\t"
  "v_mfma_f32_16x16x32_bf16 v[44:47], v[68:71], v[88:91], v[44:47]\n\t"
  "v_mfma_f32_16x16x32_bf16 v[44:47], v[72:75], v[84:87], v[44:47]\n\t"
  "v_mfma_f32_16x16x32_bf16 v[44:47], v[76:79], v[80:83], v[44:47]\n\t"
  "s_waitcnt lgkmcnt(0)\n\t"
  "v_mfma_f32_16x16x32_bf16 v[16:19], v[112:115], v[144:147], v[16:19]\n\t"
  "ds_read_b128 v[48:51], v2 offset:384\n\t"
  "global_load_lds_dwordx4 v196, s[24:25] offset:0\n\t"
  "v_mfma_f32_16x16x32_bf16 v[20:23], v[112:115], v[148:151], v[20:23]\n\t"
  "ds_read_b64 v[80:81], v3 offset:3968\n\t"
  "v_mfma_f32_16x16x32_bf16 v[20:23], v[116:119], v[144:147], v[20:23]\n\t"
  "ds_read_b64 v[82:83], v3 offset:3976\n\t"
  "v_mfma_f32_16x16x32_bf16 v[24:27], v[112:115], v[152:155], v[24:27]\n\t"
  "ds_read_b128 v[52:55], v2 offset:896\n\t"
  "v_mfma_f32_16x16x32_bf16 v[24:27], v[116:119], v[148:151], v[24:27]\n\t"
  "ds_read_b64 v[84:85], v3 offset:3456\n\t"
  "v_mfma_f32_16x16x32_bf16 v[24:27], v[120:123], v[144:147], v[24:27]\n\t"
  "ds_read_b64 v[86:87], v3 offset:3464\n\t"
  "v_mfma_f32_16x16x32_bf16 v[28:31], v[112:115], v[156:159], v[28:31]\n\t"
  "ds_read_b128 v[56:59], v2 offset:1408\n\t"
  "v_mfma_f32_16x16x32_bf16 v[28:31], v[116:119], v[152:155], v[28:31]\n\t"
  "ds_read_b64 v[88:89], v3 offset:2944\n\t"
  "v_mfma_f32_16x16x32_bf16 v[28:31], v[120:123], v[148:151], v[28:31]\n\t"
  "ds_read_b64 v[90:91], v3 offset:2952\n\t"
  "v_mfma_f32_16x16x32_bf16 v[28:31], v[124:127], v[144:147], v[28:31]\n\t"
  "ds_read_b128 v[60:63], v2 offset:1920\n\t"
  "v_mfma_f32_16x16x32_bf16 v[32:35], v[112:115], v[160:163], v[32:35]\n\t"
  "ds_read_b64 v[92:93], v3 offset:2432\n\t"
  "global_load_lds_dwordx4 v197, s[24:25] offset:1024\n\t"
  "v_mfma_f32_16x16x32_bf16 v[32:35], v[116:119], v[156:159], v[32:35]\n\t"
  "ds_read_b64 v[94:95], v3 offset:2440\n\t"
  "v_mfma_f32_16x16x32_bf16 v[32:35], v[120:123], v[152:155], v[32:35]\n\t"
  "ds_read_b128 v[64:67], v2 offset:2432\n\t"
  "v_mfma_f32_16x16x32_bf16 v[32:35], v[124:127], v[148:151], v[32:35]\n\t"
  "ds_read_b64 v[96:97], v3 offset:1920\n\t"
  "v_mfma_f32_16x16x32_bf16 v[32:35], v[128:131], v[144:147], v[32:35]\n\t"
  "ds_read_b64 v[98:99], v3 offset:1928\n\t"
  "v_mfma_f32_16x16x32_bf16 v[36:39], v[112:115], v[164:167], v[36:39]\n\t"
  "ds_read_b128 v[68:71], v2 offset:2944\n\t"
  "v_mfma_f32_16x16x32_bf16 v[36:39], v[116:119], v[160:163], v[36:39]\n\t"
  "ds_read_b64 v[100:101], v3 offset:1408\n\t"
  "v_mfma_f32_16x16x32_bf16 v[36:39], v[120:123], v[156:159], v[36:39]\n\t"
  "ds_read_b64 v[102:103], v3 offset:1416\n\t"
  "v_mfma_f32_16x16x32_bf16 v[36:39], v[124:127], v[152:155], v[36:39]\n\t"
  "ds_read_b128 v[72:75], v2 offset:3456\n\t"
  "v_mfma_f32_16x16x32_bf16 v[36:39], v[128:131], v[148:151], v[36:39]\n\t"
  "ds_read_b64 v[104:105], v3 offset:896\n\t"
  "v_mfma_f32_16x16x32_bf16 v[36:39], v[132:135], v[144:147], v[36:39]\n\t"
  "ds_read_b64 v[106:107], v3 offset:904\n\t"
  "v_mfma_f32_16x16x32_bf16 v[40:43], v[112:115], v[168:171], v[40:43]\n\t"
  "ds_read_b128 v[76:79], v2 offset:3968\n\t"
  "global_load_lds_dwordx4 v198, s[24:25] offset:2048\n\t"
  "v_mfma_f32_16x16x32_bf16 v[40:43], v[116:119], v[164:167], v[40:43]\n\t"
  "ds_read_b64 v[108:109], v3 offset:384\n\t"
  "s_add_u32 m0, s23, 102400\n\t"
  "v_mfma_f32_16x16x32_bf16 v[40:43], v[120:123], v[160:163], v[40:43]\n\t"
  "ds_read_b64 v[110:111], v3 offset:392\n\t"
  "s_add_u32 s24, s34, 16384\n\t"
  "s_addc_u32 s25, s35, 0\n\t"
  "v_mfma_f32_16x16x32_bf16 v[40:43], v[124:127], v[156:159], v[40:43]\n\t"
  "v_mfma_f32_16x16x32_bf16 v[40:43], v[128:131], v[152:155], v[40:43]\n\t"
  "v_mfma_f32_16x16x32_bf16 v[40:43], v[132:135], v[148:151], v[40:43]\n\t"
  "v_mfma_f32_16x16x32_bf16 v[40:43], v[136:139], v[144:147], v[40:43]\n\t"
  "v_mfma_f32_16x16x32_bf16 v[44:47], v[112:115], v[172:175], v[44:47]\n\t"
  "v_mfma_f32_16x16x32_bf16 v[44:47], v[116:119], v[168:171], v[44:47]\n\t"
  "v_mfma_f32_16x16x32_bf16 v[44:47], v[120:123], v[164:167], v[44:47]\n\t"
  "v_mfma_f32_16x16x32_bf16 v[44:47], v[124:127], v[160:163], v[44:47]\n\t"
  "global_load_lds_dwordx4 v196, s[24:25] offset:0\n\t"
  "v_mfma_f32_16x16x32_bf16 v[44:47], v[128:131], v[156:159], v[44:47]\n\t"
  "v_mfma_f32_16x16x32_bf16 v[44:47], v[132:135], v[152:155], v[44:47]\n\t"
  "v_mfma_f32_16x16x32_bf16 v[44:47], v[136:139], v[148:151], v[44:47]\n\t"
  "v_mfma_f32_16x16x32_bf16 v[44:47], v[140:143], v[144:147], v[44:47]\n\t"
  "s_waitcnt lgkmcnt(0)\n\t"
  "v_mfma_f32_16x16x32_bf16 v[16:19], v[48:51], v[80:83], v[16:19]\n\t"
  "ds_read_b128 v[112:115], v2 offset:448\n\t"
  "v_mfma_f32_16x16x32_bf16 v[20:23], v[48:51], v[84:87], v[20:23]\n\t"
  "ds_read_b64 v[144:145], v3 offset:4032\n\t"
  "v_mfma_f32_16x16x32_bf16 v[20:23], v[52:55], v[80:83], v[20:23]\n\t"
  "ds_read_b64 v[146:147], v3 offset:4040\n\t"
  "v_mfma_f32_16x16x32_bf16 v[24:27], v[48:51], v[88:91], v[24:27]\n\t"
  "ds_read_b128 v[116:119], v2 offset:960\n\t"
  "v_mfma_f32_16x16x32_bf16 v[24:27], v[52:55], v[84:87], v[24:27]\n\t"
  "ds_read_b64 v[148:149], v3 offset:3520\n\t"
  "v_mfma_f32_16x16x32_bf16 v[24:27], v[56:59], v[80:83], v[24:27]\n\t"
  "ds_read_b64 v[150:151], v3 offset:3528\n\t"
  "v_mfma_f32_16x16x32_bf16 v[28:31], v[48:51], v[92:95], v[28:31]\n\t"
  "ds_read_b128 v[120:123], v2 offset:1472\n\t"
  "global_load_lds_dwordx4 v197, s[24:25] offset:1024\n\t"
  "v_mfma_f32_16x16x32_bf16 v[28:31], v[52:55], v[88:91], v[28:31]\n\t"
  "ds_read_b64 v[152:153], v3 offset:3008\n\t"
  "v_mfma_f32_16x16x32_bf16 v[28:31], v[56:59], v[84:87], v[28:31]\n\t"
  "ds_read_b64 v[154:155], v3 offset:3016\n\t"
  "v_mfma_f32_16x16x32_bf16 v[28:31], v[60:63], v[80:83], v[28:31]\n\t"
  "ds_read_b128 v[124:127], v2 offset:1984\n\t"
  "v_mfma_f32_16x16x32_bf16 v[32:35], v[48:51], v[96:99], v[32:35]\n\t"
  "ds_read_b64 v[156:157], v3 offset:2496\n\t"
  "v_mfma_f32_16x16x32_bf16 v[32:35], v[52:55], v[92:95], v[32:35]\n\t"
  "ds_read_b64 v[158:159], v3 offset:2504\n\t"
  "v_mfma_f32_16x16x32_bf16 v[32:35], v[56:59], v[88:91], v[32:35]\n\t"
  "ds_read_b128 v[128:131], v2 offset:2496\n\t"
  "v_mfma_f32_16x16x32_bf16 v[32:35], v[60:63], v[84:87], v[32:35]\n\t"
  "ds_read_b64 v[160:161], v3 offset:1984\n\t"
  "v_mfma_f32_16x16x32_bf16 v[32:35], v[64:67], v[80:83], v[32:35]\n\t"
  "ds_read_b64 v[162:163], v3 offset:1992\n\t"
  "v_mfma_f32_16x16x32_bf16 v[36:39], v[48:51], v[100:103], v[36:39]\n\t"
  "ds_read_b128 v[132:135], v2 offset:3008\n\t"
  "v_mfma_f32_16x16x32_bf16 v[36:39], v[52:55], v[96:99], v[36:39]\n\t"
  "ds_read_b64 v[164:165], v3 offset:1472\n\t"
  "global_load_lds_dwordx4 v198, s[24:25] offset:2048\n\t"
  "v_mfma_f32_16x16x32_bf16 v[36:39], v[56:59], v[92:95], v[36:39]\n\t"
  "ds_read_b64 v[166:167], v3 offset:1480\n\t"
  "s_add_u32 m0, s23, 114688\n\t"
  "v_mfma_f32_16x16x32_bf16 v[36:39], v[60:63], v[88:91], v[36:39]\n\t"
  "ds_read_b128 v[136:139], v2 offset:3520\n\t"
  "s_add_u32 s24, s34, 20480\n\t"
  "s_addc_u32 s25, s35, 0\n\t"
  "v_mfma_f32_16x16x32_bf16 v[36:39], v[64:67], v[84:87], v[36:39]\n\t"
  "ds_read_b64 v[168:169], v3 offset:960\n\t"
  "v_mfma_f32_16x16x32_bf16 v[36:39], v[68:71], v[80:83], v[36:39]\n\t"
  "ds_read_b64 v[170:171], v3 offset:968\n\t"
  "v_mfma_f32_16x16x32_bf16 v[40:43], v[48:51], v[104:107], v[40:43]\n\t"
  "ds_read_b128 v[140:143], v2 offset:4032\n\t"
  "v_mfma_f32_16x16x32_bf16 v[40:43], v[52:55], v[100:103], v[40:43]\n\t"
  "ds_read_b64 v[172:173], v3 offset:448\n\t"
  "v_mfma_f32_16x16x32_bf16 v[40:43], v[56:59], v[96:99], v[40:43]\n\t"
  "ds_read_b64 v[174:175], v3 offset:456\n\t"
  "v_mfma_f32_16x16x32_bf16 v[40:43], v[60:63], v[92:95], v[40:43]\n\t"
  "v_mfma_f32_16x16x32_bf16 v[40:43], v[64:67], v[88:91], v[40:43]\n\t"
  "v_mfma_f32_16x16x32_bf16 v[40:43], v[68:71], v[84:87], v[40:43]\n\t"
  "global_load_lds_dwordx4 v196, s[24:25] offset:0\n\t"
  "v_mfma_f32_16x16x32_bf16 v[40:43], v[72:75], v[80:83], v[40:43]\n\t"
  "v_mfma_f32_16x16x32_bf16 v[44:47], v[48:51], v[108:111], v[44:47]\n\t"
  "v_mfma_f32_16x16x32_bf16 v[44:47], v[52:55], v[104:107], v[44:47]\n\t"
  "v_mfma_f32_16x16x32_bf16 v[44:47], v[56:59], v[100:103], v[44:47]\n\t"
  "v_mfma_f32_16x16x32_bf16 v[44:47], v[60:63], v[96:99], v[44:47]\n\t"
  "v_mfma_f32_16x16x32_bf16 v[44:47], v[64:67], v[92:95], v[44:47]\n\t"
  "v_mfma_f32_16x16x32_bf16 v[44:47], v[68:71], v[88:91], v[44:47]\n\t"
  "v_mfma_f32_16x16x32_bf16 v[44:47], v[72:75], v[84:87], v[44:47]\n\t"
  "v_mfma_f32_16x16x32_bf16 v[44:47], v[76:79], v[80:83], v[44:47]\n\t"
  "s_waitcnt lgkmcnt(0)\n\t"
  "v_mfma_f32_16x16x32_bf16 v[16:19], v[112:115], v[144:147], v[16:19]\n\t"
  "v_mfma_f32_16x16x32_bf16 v[20:23], v[112:115], v[148:151], v[20:23]\n\t"
  "global_load_lds_dwordx4 v197, s[24:25] offset:1024\n\t"
  "v_mfma_f32_16x16x32_bf16 v[20:23], v[116:119], v[144:147], v[20:23]\n\t"
  "v_mfma_f32_16x16x32_bf16 v[24:27], v[112:115], v[152:155], v[24:27]\n\t"
  "v_mfma_f32_16x16x32_bf16 v[24:27], v[116:119], v[148:151], v[24:27]\n\t"
  "v_mfma_f32_16x16x32_bf16 v[24:27], v[120:123], v[144:147], v[24:27]\n\t"
  "s_nop 1\n\t"
  "ds_write_b64 v4, v[16:17] offset:0\n\t"
  "v_mfma_f32_16x16x32_bf16 v[28:31], v[112:115], v[156:159], v[28:31]\n\t"
  "ds_write_b64 v4, v[18:19] offset:128\n\t"
  "v_mfma_f32_16x16x32_bf16 v[28:31], v[116:119], v[152:155], v[28:31]\n\t"
  "ds_write_b64 v4, v[20:21] offset:1024\n\t"
  "v_mfma_f32_16x16x32_bf16 v[28:31], v[120:123], v[148:151], v[28:31]\n\t"
  "ds_write_b64 v4, v[22:23] offset:1152\n\t"
  "v_mfma_f32_16x16x32_bf16 v[28:31], v[124:127], v[144:147], v[28:31]\n\t"
  "v_mfma_f32_16x16x32_bf16 v[32:35], v[112:115], v[160:163], v[32:35]\n\t"
  "ds_write_b64 v4, v[24:25] offset:2048\n\t"
  "v_mfma_f32_16x16x32_bf16 v[32:35], v[116:119], v[156:159], v[32:35]\n\t"
  "global_load_lds_dwordx4 v198, s[24:25] offset:2048\n\t"
  "ds_write_b64 v4, v[26:27] offset:2176\n\t"
  "v_mfma_f32_16x16x32_bf16 v[32:35], v[120:123], v[152:155], v[32:35]\n\t"
  "v_mfma_f32_16x16x32_bf16 v[32:35], v[124:127], v[148:151], v[32:35]\n\t"
  "v_mfma_f32_16x16x32_bf16 v[32:35], v[128:131], v[144:147], v[32:35]\n\t"
  "ds_write_b64 v4, v[28:29] offset:3072\n\t"
  "v_mfma_f32_16x16x32_bf16 v[36:39], v[112:115], v[164:167], v[36:39]\n\t"
  "ds_write_b64 v4, v[30:31] offset:3200\n\t"
  "v_mfma_f32_16x16x32_bf16 v[36:39], v[116:119], v[160:163], v[36:39]\n\t"
  "v_mfma_f32_16x16x32_bf16 v[36:39], v[120:123], v[156:159], v[36:39]\n\t"
  "v_mfma_f32_16x16x32_bf16 v[36:39], v[124:127], v[152:155], v[36:39]\n\t"
  "v_mfma_f32_16x16x32_bf16 v[36:39], v[128:131], v[148:151], v[36:39]\n\t"
  "s_nop 0\n\t"
  "ds_write_b64 v4, v[32:33] offset:4096\n\t"
  "v_mfma_f32_16x16x32_bf16 v[36:39], v[132:135], v[144:147], v[36:39]\n\t"
  "ds_write_b64 v4, v[34:35] offset:4224\n\t"
  "v_mfma_f32_16x16x32_bf16 v[40:43], v[112:115], v[168:171], v[40:43]\n\t"
  "v_mfma_f32_16x16x32_bf16 v[40:43], v[116:119], v[164:167], v[40:43]\n\t"
  "v_mfma_f32_16x16x32_bf16 v[40:43], v[120:123], v[160:163], v[40:43]\n\t"
  "v_mfma_f32_16x16x32_bf16 v[40:43], v[124:127], v[156:159], v[40:43]\n\t"
  "v_mfma_f32_16x16x32_bf16 v[40:43], v[128:131], v[152:155], v[40:43]\n\t"
  "s_nop 1\n\t"
  "ds_write_b64 v4, v[36:37] offset:5120\n\t"
  "v_mfma_f32_16x16x32_bf16 v[40:43], v[132:135], v[148:151], v[40:43]\n\t"
  "ds_write_b64 v4, v[38:39] offset:5248\n\t"
  "v_mfma_f32_16x16x32_bf16 v[40:43], v[136:139], v[144:147], v[40:43]\n\t"
  "v_mfma_f32_16x16x32_bf16 v[44:47], v[112:115], v[172:175], v[44:47]\n\t"
  "v_mfma_f32_16x16x32_bf16 v[44:47], v[116:119], v[168:171], v[44:47]\n\t"
  "v_mfma_f32_16x16x32_bf16 v[44:47], v[120:123], v[164:167], v[44:47]\n\t"
  "v_mfma_f32_16x16x32_bf16 v[44:47], v[124:127], v[160:163], v[44:47]\n\t"
  "v_mfma_f32_16x16x32_bf16 v[44:47], v[128:131], v[156:159], v[44:47]\n\t"
  "s_nop 2\n\t"
  "ds_write_b64 v4, v[40:41] offset:6144\n\t"
  "v_mfma_f32_16x16x32_bf16 v[44:47], v[132:135], v[152:155], v[44:47]\n\t"
  "ds_write_b64 v4, v[42:43] offset:6272\n\t"
  "v_mfma_f32_16x16x32_bf16 v[44:47], v[136:139], v[148:151], v[44:47]\n\t"
  "v_mfma_f32_16x16x32_bf16 v[44:47], v[140:143], v[144:147], v[44:47]\n\t"
  "s_nop 7\n\t"
  "ds_write_b64 v4, v[44:45] offset:7168\n\t"
  "ds_write_b64 v4, v[46:47] offset:7296\n\t"
  "s_waitcnt vmcnt(18)\n\t"
  "s_waitcnt lgkmcnt(0)\n\t"
  "s_barrier\n\t"
  "ds_read_b32 v48, v6 offset:0\n\t"
  "ds_read_b32 v49, v6 offset:19712\n\t"
  "ds_read_b32 v50, v7 offset:0\n\t"
  "ds_read_b32 v51, v7 offset:19712\n\t"
  "ds_read_b32 v52, v6 offset:1024\n\t"
  "ds_read_b32 v53, v6 offset:20736\n\t"
  "ds_read_b32 v54, v7 offset:1024\n\t"
  "ds_read_b32 v55, v7 offset:20736\n\t"
  "ds_read_b32 v56, v6 offset:2048\n\t"
  "ds_read_b32 v57, v6 offset:21760\n\t"
  "ds_read_b32 v58, v7 offset:2048\n\t"
  "ds_read_b32 v59, v7 offset:21760\n\t"
  "ds_read_b32 v60, v6 offset:3072\n\t"
  "ds_read_b32 v61, v6 offset:22784\n\t"
  "ds_read_b32 v62, v7 offset:3072\n\t"
  "ds_read_b32 v63, v7 offset:22784\n\t"
  "s_waitcnt lgkmcnt(12)\n\t"
  "v_lshlrev_b32 v10, 16, v176\n\t"
  "v_and_b32 v11, 0xffff0000, v176\n\t"
  "v_lshlrev_b32 v12, 16, v177\n\t"
  "v_and_b32 v13, 0xffff0000, v177\n\t"
  "v_add_f32 v10, v10, v48\n\t"
  "v_add_f32 v11, v11, v49\n\t"
  "v_add_f32 v12, v12, v50\n\t"
  "v_add_f32 v13, v13, v51\n\t"
  "v_cvt_pk_bf16_f32 v176, v10, v11\n\t"
  "v_cvt_pk_bf16_f32 v177, v12, v13\n\t"
  "global_store_dwordx2 v5, v[176:177], s[28:29] offset:0 sc1\n\t"
  "s_add_u32 s28, s28, 294912\n\t"
  "s_addc_u32 s29, s29, 0\n\t"
  "ds_read_b32 v64, v6 offset:4096\n\t"
  "ds_read_b32 v65, v6 offset:23808\n\t"
  "ds_read_b32 v66, v7 offset:4096\n\t"
  "ds_read_b32 v67, v7 offset:23808\n\t"
  "s_waitcnt lgkmcnt(12)\n\t"
  "v_lshlrev_b32 v10, 16, v178\n\t"
  "v_and_b32 v11, 0xffff0000, v178\n\t"
  "v_lshlrev_b32 v12, 16, v179\n\t"
  "v_and_b32 v13, 0xffff0000, v179\n\t"
  "v_add_f32 v10, v10, v52\n\t"
  "v_add_f32 v11, v11, v53\n\t"
  "v_add_f32 v12, v12, v54\n\t"
  "v_add_f32 v13, v13, v55\n\t"
  "v_cvt_pk_bf16_f32 v178, v10, v11\n\t"
  "v_cvt_pk_bf16_f32 v179, v12, v13\n\t"
  "global_store_dwordx2 v5, v[178:179], s[28:29] offset:0 sc1\n\t"
  "s_add_u32 s28, s28, 294912\n\t"
  "s_addc_u32 s29, s29, 0\n\t"
  "ds_read_b32 v68, v6 offset:5120\n\t"
  "ds_read_b32 v69, v6 offset:24832\n\t"
  "ds_read_b32 v70, v7 offset:5120\n\t"
  "ds_read_b32 v71, v7 offset:24832\n\t"
  "s_waitcnt lgkmcnt(12)\n\t"
  "v_lshlrev_b32 v10, 16, v180\n\t"
  "v_and_b32 v11, 0xffff0000, v180\n\t"
  "v_lshlrev_b32 v12, 16, v181\n\t"
  "v_and_b32 v13, 0xffff0000, v181\n\t"
  "v_add_f32 v10, v10, v56\n\t"
  "v_add_f32 v11, v11, v57\n\t"
  "v_add_f32 v12, v12, v58\n\t"
  "v_add_f32 v13, v13, v59\n\t"
  "v_cvt_pk_bf16_f32 v180, v10, v11\n\t"
  "v_cvt_pk_bf16_f32 v181, v12, v13\n\t"
  "global_store_dwordx2 v5, v[180:181], s[28:29] offset:0 sc1\n\t"
  "s_add_u32 s28, s28, 294912\n\t"
  "s_addc_u32 s29, s29, 0\n\t"
  "ds_read_b32 v72, v6 offset:6144\n\t"
  "ds_read_b32 v73, v6 offset:25856\n\t"
  "ds_read_b32 v74, v7 offset:6144\n\t"
  "ds_read_b32 v75, v7 offset:25856\n\t"
  "s_waitcnt lgkmcnt(12)\n\t"
  "v_lshlrev_b32 v10, 16, v182\n\t"
  "v_and_b32 v11, 0xffff0000, v182\n\t"
  "v_lshlrev_b32 v12, 16, v183\n\t"
  "v_and_b32 v13, 0xffff0000, v183\n\t"
  "v_add_f32 v10, v10, v60\n\t"
  "v_add_f32 v11, v11, v61\n\t"
  "v_add_f32 v12, v12, v62\n\t"
  "v_add_f32 v13, v13, v63\n\t"
  "v_cvt_pk_bf16_f32 v182, v10, v11\n\t"
  "v_cvt_pk_bf16_f32 v183, v12, v13\n\t"
  "global_store_dwordx2 v5, v[182:183], s[28:29] offset:0 sc1\n\t"
  "s_add_u32 s28, s28, 294912\n\t"
  "s_addc_u32 s29, s29, 0\n\t"
  "ds_read_b32 v76, v6 offset:7168\n\t"
  "ds_read_b32 v77, v6 offset:26880\n\t"
  "ds_read_b32 v78, v7 offset:7168\n\t"
  "ds_read_b32 v79, v7 offset:26880\n\t"
  "s_waitcnt lgkmcnt(12)\n\t"
  "v_lshlrev_b32 v10, 16, v184\n\t"
  "v_and_b32 v11, 0xffff0000, v184\n\t"
  "v_lshlrev_b32 v12, 16, v185\n\t"
  "v_and_b32 v13, 0xffff0000, v185\n\t"
  "v_add_f32 v10, v10, v64\n\t"
  "v_add_f32 v11, v11, v65\n\t"
  "v_add_f32 v12, v12, v66\n\t"
  "v_add_f32 v13, v13, v67\n\t"
  "v_cvt_pk_bf16_f32 v184, v10, v11\n\t"
  "v_cvt_pk_bf16_f32 v185, v12, v13\n\t"
  "global_store_dwordx2 v5, v[184:185], s[28:29] offset:0 sc1\n\t"
  "s_add_u32 s28, s28, 294912\n\t"
  "s_addc_u32 s29, s29, 0\n\t"
  "s_waitcnt lgkmcnt(8)\n\t"
  "v_lshlrev_b32 v10, 16, v186\n\t"
  "v_and_b32 v11, 0xffff0000, v186\n\t"
  "v_lshlrev_b32 v12, 16, v187\n\t"
  "v_and_b32 v13, 0xffff0000, v187\n\t"
  "v_add_f32 v10, v10, v68\n\t"
  "v_add_f32 v11, v11, v69\n\t"
  "v_add_f32 v12, v12, v70\n\t"
  "v_add_f32 v13, v13, v71\n\t"
  "v_cvt_pk_bf16_f32 v186, v10, v11\n\t"
  "v_cvt_pk_bf16_f32 v187, v12, v13\n\t"
  "global_store_dwordx2 v5, v[186:187], s[28:29] offset:0 sc1\n\t"
  "s_add_u32 s28, s28, 294912\n\t"
  "s_addc_u32 s29, s29, 0\n\t"
  "s_waitcnt lgkmcnt(4)\n\t"
  "v_lshlrev_b32 v10, 16, v188\n\t"
  "v_and_b32 v11, 0xffff0000, v188\n\t"
  "v_lshlrev_b32 v12, 16, v189\n\t"
  "v_and_b32 v13, 0xffff0000, v189\n\t"
  "v_add_f32 v10, v10, v72\n\t"
  "v_add_f32 v11, v11, v73\n\t"
  "v_add_f32 v12, v12, v74\n\t"
  "v_add_f32 v13, v13, v75\n\t"
  "v_cvt_pk_bf16_f32 v188, v10, v11\n\t"
  "v_cvt_pk_bf16_f32 v189, v12, v13\n\t"
  "global_store_dwordx2 v5, v[188:189], s[28:29] offset:0 sc1\n\t"
  "s_add_u32 s28, s28, 294912\n\t"
  "s_addc_u32 s29, s29, 0\n\t"
  "s_waitcnt lgkmcnt(0)\n\t"
  "v_lshlrev_b32 v10, 16, v190\n\t"
  "v_and_b32 v11, 0xffff0000, v190\n\t"
  "v_lshlrev_b32 v12, 16, v191\n\t"
  "v_and_b32 v13, 0xffff0000, v191\n\t"
  "v_add_f32 v10, v10, v76\n\t"
  "v_add_f32 v11, v11, v77\n\t"
  "v_add_f32 v12, v12, v78\n\t"
  "v_add_f32 v13, v13, v79\n\t"
  "v_cvt_pk_bf16_f32 v190, v10, v11\n\t"
  "v_cvt_pk_bf16_f32 v191, v12, v13\n\t"
  "global_store_dwordx2 v5, v[190:191], s[28:29] offset:0 sc1\n\t"
    :: "s"(ws), "s"(b), "s"(w), "s"(out) : "v0", "v1", "v2", "v3", "v4", "v5", "v6", "v7", "v8", "v9", "v10", "v11", "v12", "v13", "v14", "v15", "v16", "v17", "v18", "v19", "v20", "v21", "v22", "v23", "v24", "v25", "v26", "v27", "v28", "v29", "v30", "v31", "v32", "v33", "v34", "v35", "v36", "v37", "v38", "v39", "v40", "v41", "v42", "v43", "v44", "v45", "v46", "v47", "v48", "v49", "v50", "v51", "v52", "v53", "v54", "v55", "v56", "v57", "v58", "v59", "v60", "v61", "v62", "v63", "v64", "v65", "v66", "v67", "v68", "v69", "v70", "v71", "v72", "v73", "v74", "v75", "v76", "v77", "v78", "v79", "v80", "v81", "v82", "v83", "v84", "v85", "v86", "v87", "v88", "v89", "v90", "v91", "v92", "v93", "v94", "v95", "v96", "v97", "v98", "v99", "v100", "v101", "v102", "v103", "v104", "v105", "v106", "v107", "v108", "v109", "v110", "v111", "v112", "v113", "v114", "v115", "v116", "v117", "v118", "v119", "v120", "v121", "v122", "v123", "v124", "v125", "v126", "v127", "v128", "v129", "v130", "v131", "v132", "v133", "v134", "v135", "v136", "v137", "v138", "v139", "v140", "v141", "v142", "v143", "v144", "v145", "v146", "v147", "v148", "v149", "v150", "v151", "v152", "v153", "v154", "v155", "v156", "v157", "v158", "v159", "v160", "v161", "v162", "v163", "v164", "v165", "v166", "v167", "v168", "v169", "v170", "v171", "v172", "v173", "v174", "v175", "v176", "v177", "v178", "v179", "v180", "v181", "v182", "v183", "v184", "v185", "v186", "v187", "v188", "v189", "v190", "v191", "v192", "v193", "v194", "v195", "v196", "v197", "v198", "v199", "s16", "s17", "s18", "s19", "s20", "s21", "s22", "s23", "s24", "s25", "s26", "s27", "s28", "s29", "s30", "s31", "s34", "s35", "s36", "s37", "s38", "s39", "s40", "s41", "s42", "s43", "s44", "s45", "s46", "s47", "s48", "s49", "s50", "s51", "s52", "s53", "s54", "s55", "s56", "s57", "s58", "s59", "s60", "s61", "s62", "s63", "vcc", "scc", "memory");
  } else if (b < 216) { role_w2(b - 144, wd, ws, smem); asm volatile("CODE_END2:"); }
  else { const int v = b - 216; role_w1x4(2 * (v < 32 ? 9 * (v >> 2) + ((v & 2) ? 8 : 1) : 9 * ((v - 32) >> 1) + 6) + (v & 1), wg, wu, ws, smem); asm volatile("CODE_END3:"); }
  asm volatile(
  "s_branch FFN0_%=\n\t"
  ".p2align 12\n\t"
  "FFN0_%=:\n\t"
  "s_mov_b64 s[8:9], %0\n\t"
  "s_mov_b32 s10, %1\n\t"
  "s_mov_b32 s11, %2\n\t"
  "s_mov_b64 s[90:91], %3\n\t"
  "v_mbcnt_lo_u32_b32 v227, -1, 0\n\t"
  "v_mbcnt_hi_u32_b32 v227, -1, v227\n\t"
  "s_and_b32 s14, s10, 1\n\t"
  "s_lshl_b32 s14, s14, 2\n\t"
  "s_bfe_u32 s15, s10, 0x20003\n\t"
  "s_add_u32 s16, s14, s15\n\t"
  "s_bfe_u32 s19, s10, 0x20001\n\t"
  "s_lshl_b32 s14, s19, 3\n\t"
  "s_lshr_b32 s15, s10, 5\n\t"
  "s_add_u32 s17, s14, s15\n\t"
  "s_and_b32 s18, s17, 1\n\t"
  "v_lshlrev_b32 v216, 4, v227\n\t"
  "s_lshl_b32 s14, s16, 4\n\t"
  "s_lshl_b32 s15, s11, 2\n\t"
  "s_add_u32 s98, s14, s15\n\t"
  "s_mul_i32 s14, s98, 18432\n\t"
  "s_add_u32 s14, s14, 2363392\n\t"
  "s_add_u32 s22, s8, s14\n\t"
  "s_addc_u32 s23, s9, 0\n\t"
  "s_add_u32 s24, s22, 18432\n\t"
  "s_addc_u32 s25, s23, 0\n\t"
  "s_add_u32 s26, s24, 18432\n\t"
  "s_addc_u32 s27, s25, 0\n\t"
  "s_add_u32 s28, s26, 18432\n\t"
  "s_addc_u32 s29, s27, 0\n\t"
  "s_mul_i32 s14, s17, 9\n\t"
  "s_add_u32 s14, s14, s18\n\t"
  "s_mul_i32 s14, s14, 36864\n\t"
  "s_add_u32 s14, s14, 10027008\n\t"
  "s_add_u32 s30, s8, s14\n\t"
  "s_addc_u32 s31, s9, 0\n\t"
  "s_mul_i32 s46, s11, 3072\n\t"
  "s_mul_i32 s14, s11, 3\n\t"
  "s_add_u32 s14, s14, 0\n\t"
  "s_lshr_b32 s14, s14, 2\n\t"
  "s_lshl_b32 s14, s14, 15\n\t"
  "s_add_u32 s14, s14, s46\n\t"
  "v_add_u32 v224, s14, v216\n\t"
  "s_mul_i32 s14, s11, 3\n\t"
  "s_add_u32 s14, s14, 1\n\t"
  "s_lshr_b32 s14, s14, 2\n\t"
  "s_lshl_b32 s14, s14, 15\n\t"
  "s_add_u32 s14, s14, s46\n\t"
  "v_add_u32 v225, s14, v216\n\t"
  "s_mul_i32 s14, s11, 3\n\t"
  "s_add_u32 s14, s14, 2\n\t"
  "s_lshr_b32 s14, s14, 2\n\t"
  "s_lshl_b32 s14, s14, 15\n\t"
  "s_add_u32 s14, s14, s46\n\t"
  "v_add_u32 v226, s14, v216\n\t"
  "s_lshl_b32 s14, s16, 8\n\t"
  "s_lshl_b32 s15, s17, 3\n\t"
  "s_add_u32 s47, s14, s15\n\t"
  "s_lshr_b32 s14, s47, 4\n\t"
  "s_mul_i32 s14, s14, 18432\n\t"
  "s_and_b32 s15, s47, 15\n\t"
  "s_lshl_b32 s15, s15, 4\n\t"
  "s_add_u32 s14, s14, s15\n\t"
  "s_mul_i32 s15, s11, 4608\n\t"
  "s_add_u32 s14, s14, s15\n\t"
  "s_add_u32 s14, s14, 2361344\n\t"
  "s_add_u32 s92, s8, s14\n\t"
  "s_addc_u32 s93, s9, 0\n\t"
  "v_and_b32 v109, 7, v227\n\t"
  "v_lshrrev_b32 v108, 3, v227\n\t"
  "v_lshlrev_b32 v108, 8, v108\n\t"
  "v_lshl_add_u32 v108, v109, 4, v108\n\t"
  "v_lshlrev_b32 v110, 3, v109\n\t"
  "s_lshl_b32 s14, s11, 6\n\t"
  "v_add_u32 v111, s14, v110\n\t"
  "v_add_lshl_u32 v109, v109, s47, 2\n\t"
  "s_add_u32 s14, s11, 4\n\t"
  "s_lshl_b32 s14, s14, 8\n\t"
  "s_add_u32 s14, s14, s10\n\t"
  "s_lshl_b32 s14, s14, 7\n\t"
  "s_lshl_b32 s15, s10, 7\n\t"
  "s_cmp_eq_u32 s11, 0\n\t"
  "s_cselect_b32 s96, s15, s14\n\t"
  "s_cselect_b32 s16, 32768, 0\n\t"
  "s_cselect_b64 vcc, -1, 0\n\t"
  "s_add_u32 s14, s14, 65536\n\t"
  "v_mov_b32 v112, s14\n\t"
  "v_cndmask_b32 v109, v112, v109, vcc\n\t"
  "v_mov_b32 v104, 0\n\t"
  "v_mov_b32 v105, 0\n\t"
  "v_mov_b32 v106, 0\n\t"
  "v_mov_b32 v107, 0\n\t"
  "s_add_u32 s58, s8, 44892160\n\t"
  "s_addc_u32 s59, s9, 0\n\t"
  "s_lshl_b32 s14, s10, 7\n\t"
  "v_mov_b32 v236, s96\n\t"
  "v_mov_b32 v244, 1\n\t"
  "v_add_u32 v219, 65536, v216\n\t"
  "v_add_u32 v220, 131072, v216\n\t"
  "s_add_u32 s42, s8, 44826624\n\t"
  "s_addc_u32 s43, s9, 0\n\t"
  "s_mul_i32 s14, s11, 4608\n\t"
  "v_min_u32 v118, 35, v227\n\t"
  "v_lshl_add_u32 v118, v118, 7, s14\n\t"
  "s_mul_i32 s14, s11, 3584\n\t"
  "s_add_u32 s14, s14, 18432\n\t"
  "v_min_u32 v241, 27, v227\n\t"
  "v_lshl_add_u32 v241, v241, 7, s14\n\t"
  "s_waitcnt vmcnt(0) lgkmcnt(0)\n\t"
  "s_barrier\n\t"
  "global_store_dword v236, v244, s[58:59] offset:0 sc1\n\t"
  "s_add_u32 m0, s46, 0\n\t"
  "s_add_u32 s44, s30, 24576\n\t"
  "s_addc_u32 s45, s31, 0\n\t"
  "global_load_lds_dwordx4 v224, s[44:45] offset:0 sc1\n\t"
  "global_load_lds_dwordx4 v225, s[44:45] offset:1024 sc1\n\t"
  "global_load_lds_dwordx4 v226, s[44:45] offset:2048 sc1\n\t"
  "s_add_u32 m0, s46, 12288\n\t"
  "s_add_u32 s44, s30, 28672\n\t"
  "s_addc_u32 s45, s31, 0\n\t"
  "global_load_lds_dwordx4 v224, s[44:45] offset:0 sc1\n\t"
  "global_load_lds_dwordx4 v225, s[44:45] offset:1024 sc1\n\t"
  "global_load_lds_dwordx4 v226, s[44:45] offset:2048 sc1\n\t"
  "s_add_u32 m0, s46, 24576\n\t"
  "s_add_u32 s44, s30, 32768\n\t"
  "s_addc_u32 s45, s31, 0\n\t"
  "global_load_lds_dwordx4 v224, s[44:45] offset:0 sc1\n\t"
  "global_load_lds_dwordx4 v225, s[44:45] offset:1024 sc1\n\t"
  "global_load_lds_dwordx4 v226, s[44:45] offset:2048 sc1\n\t"
  "s_mov_b32 s97, 0\n\t"
  "s_mov_b64 exec, 0\n\t"
  "s_cmp_eq_u32 s11, 0\n\t"
  "s_cbranch_scc1 PG0_%=\n\t"
  "s_cmp_eq_u32 s11, 1\n\t"
  "s_cbranch_scc1 PG1_%=\n\t"
  "s_cmp_eq_u32 s11, 2\n\t"
  "s_cbranch_scc1 PG2_%=\n\t"
  "s_branch PG3_%=\n\t"
  "PGBACK_%=:\n\t"
  "s_mov_b64 exec, -1\n\t"
  "s_cmp_lt_u32 s10, 144\n\t"
  "s_cbranch_scc1 GSET_%=\n\t"
  "s_bfe_u32 s14, s10, 0x20001\n\t"
  "s_lshl_b32 s14, s14, 3\n\t"
  "s_lshr_b32 s15, s10, 5\n\t"
  "s_add_u32 s14, s14, s15\n\t"
  "s_and_b32 s15, s14, 1\n\t"
  "s_mul_i32 s14, s14, 9\n\t"
  "s_add_u32 s14, s14, s15\n\t"
  "s_mul_i32 s14, s14, 36864\n\t"
  "s_add_u32 s14, s14, 10027008\n\t"
  "s_add_u32 s56, s8, s14\n\t"
  "s_addc_u32 s57, s9, 0\n\t"
  "s_mul_i32 s15, s11, 3072\n\t"
  "s_mul_i32 s14, s11, 3\n\t"
  "s_add_u32 s14, s14, 0\n\t"
  "s_lshr_b32 s14, s14, 2\n\t"
  "s_lshl_b32 s14, s14, 15\n\t"
  "s_add_u32 s14, s14, s15\n\t"
  "v_add_u32 v128, s14, v216\n\t"
  "s_mul_i32 s14, s11, 3\n\t"
  "s_add_u32 s14, s14, 1\n\t"
  "s_lshr_b32 s14, s14, 2\n\t"
  "s_lshl_b32 s14, s14, 15\n\t"
  "s_add_u32 s14, s14, s15\n\t"
  "v_add_u32 v129, s14, v216\n\t"
  "s_mul_i32 s14, s11, 3\n\t"
  "s_add_u32 s14, s14, 2\n\t"
  "s_lshr_b32 s14, s14, 2\n\t"
  "s_lshl_b32 s14, s14, 15\n\t"
  "s_add_u32 s14, s14, s15\n\t"
  "v_add_u32 v130, s14, v216\n\t"
  "s_add_u32 m0, s15, 126976\n\t"
  "s_add_u32 s44, s56, 0\n\t"
  "s_addc_u32 s45, s57, 0\n\t"
  "global_load_lds_dwordx4 v128, s[44:45] offset:0\n\t"
  "global_load_lds_dwordx4 v129, s[44:45] offset:1024\n\t"
  "global_load_lds_dwordx4 v130, s[44:45] offset:2048\n\t"
  "s_add_u32 m0, s15, 139264\n\t"
  "s_add_u32 s44, s56, 4096\n\t"
  "s_addc_u32 s45, s57, 0\n\t"
  "global_load_lds_dwordx4 v128, s[44:45] offset:0\n\t"
  "global_load_lds_dwordx4 v129, s[44:45] offset:1024\n\t"
  "global_load_lds_dwordx4 v130, s[44:45] offset:2048\n\t"
  "s_add_u32 m0, s15, 151552\n\t"
  "s_add_u32 s44, s56, 8192\n\t"
  "s_addc_u32 s45, s57, 0\n\t"
  "global_load_lds_dwordx4 v128, s[44:45] offset:0\n\t"
  "global_load_lds_dwordx4 v129, s[44:45] offset:1024\n\t"
  "global_load_lds_dwordx4 v130, s[44:45] offset:2048\n\t"
  "s_add_u32 m0, s15, 90112\n\t"
  "s_add_u32 s44, s56, 12288\n\t"
  "s_addc_u32 s45, s57, 0\n\t"
  "global_load_lds_dwordx4 v128, s[44:45] offset:0\n\t"
  "global_load_lds_dwordx4 v129, s[44:45] offset:1024\n\t"
  "global_load_lds_dwordx4 v130, s[44:45] offset:2048\n\t"
  "s_add_u32 m0, s15, 102400\n\t"
  "s_add_u32 s44, s56, 16384\n\t"
  "s_addc_u32 s45, s57, 0\n\t"
  "global_load_lds_dwordx4 v128, s[44:45] offset:0\n\t"
  "global_load_lds_dwordx4 v129, s[44:45] offset:1024\n\t"
  "global_load_lds_dwordx4 v130, s[44:45] offset:2048\n\t"
  "s_add_u32 m0, s15, 114688\n\t"
  "s_add_u32 s44, s56, 20480\n\t"
  "s_addc_u32 s45, s57, 0\n\t"
  "global_load_lds_dwordx4 v128, s[44:45] offset:0\n\t"
  "global_load_lds_dwordx4 v129, s[44:45] offset:1024\n\t"
  "global_load_lds_dwordx4 v130, s[44:45] offset:2048\n\t"
  "GSET_%=:\n\t"
  "v_add_u32 v217, 8192, v216\n\t"
  "v_add_u32 v218, 16384, v216\n\t"
  "v_add_u32 v221, s46, v216\n\t"
  "s_mul_i32 s14, s18, 331776\n\t"
  "s_sub_u32 s94, s30, s14\n\t"
  "s_subb_u32 s95, s31, 0\n\t"
  "s_mul_i32 s14, s98, 147456\n\t"
  "s_mul_i32 s15, s17, 9\n\t"
  "s_add_u32 s15, s15, s18\n\t"
  "s_lshl_b32 s15, s15, 9\n\t"
  "s_add_u32 s14, s14, s15\n\t"
  "s_add_u32 s14, s14, 25953280\n\t"
  "s_add_u32 s34, s8, s14\n\t"
  "s_addc_u32 s35, s9, 0\n\t"
  "s_add_u32 s36, s34, 147456\n\t"
  "s_addc_u32 s37, s35, 0\n\t"
  "s_add_u32 s38, s36, 147456\n\t"
  "s_addc_u32 s39, s37, 0\n\t"
  "s_add_u32 s40, s38, 147456\n\t"
  "s_addc_u32 s41, s39, 0\n\t"
  "s_lshl_b32 s14, s98, 6\n\t"
  "v_and_b32 v253, 15, v227\n\t"
  "v_lshl_add_u32 v223, v253, 2, s14\n\t"
  "s_mul_i32 s14, s18, 4608\n\t"
  "s_sub_u32 s14, 5120, s14\n\t"
  "v_lshrrev_b32 v252, 5, v227\n\t"
  "v_lshl_add_u32 v222, v252, 8, s14\n\t"
  "v_lshl_add_u32 v222, v253, 4, v222\n\t"
  "v_bfe_u32 v252, v227, 4, 1\n\t"
  "v_lshl_add_u32 v222, v252, 3, v222\n\t"
  "v_add_u32 v228, 24576, v216\n\t"
  "v_add_u32 v229, 32768, v216\n\t"
  "s_and_b32 s14, s10, 1\n\t"
  "s_lshl_b32 s14, s14, 3\n\t"
  "s_bfe_u32 s15, s10, 0x30003\n\t"
  "s_add_u32 s20, s14, s15\n\t"
  "s_lshr_b32 s21, s10, 6\n\t"
  "s_lshl_b32 s14, s20, 3\n\t"
  "s_lshl_b32 s15, s11, 1\n\t"
  "s_add_u32 s98, s14, s15\n\t"
  "s_mul_i32 s14, s98, 147456\n\t"
  "s_mul_i32 s15, s19, 36864\n\t"
  "s_add_u32 s14, s14, s15\n\t"
  "s_add_u32 s14, s14, 25956352\n\t"
  "s_add_u32 s50, s8, s14\n\t"
  "s_addc_u32 s51, s9, 0\n\t"
  "s_add_u32 s52, s50, 147456\n\t"
  "s_addc_u32 s53, s51, 0\n\t"
  "s_lshl_b32 s14, s21, 2\n\t"
  "s_add_u32 s14, s14, s19\n\t"
  "s_mul_i32 s14, s14, 331776\n\t"
  "s_add_u32 s14, s14, 20643840\n\t"
  "s_add_u32 s54, s8, s14\n\t"
  "s_addc_u32 s55, s9, 0\n\t"
  "s_mul_i32 s48, s11, 7168\n\t"
  "s_mul_i32 s49, s11, 6144\n\t"
  "v_add_u32 v230, s48, v216\n\t"
  "v_add_u32 v232, s49, v216\n\t"
  "v_add_u32 v231, 4096, v230\n\t"
  "v_add_u32 v233, 4096, v232\n\t"
  "s_lshl_b32 s14, s20, 2\n\t"
  "s_add_u32 s14, s14, s21\n\t"
  "s_mul_i32 s14, s14, 196608\n\t"
  "s_mul_i32 s15, s11, 3072\n\t"
  "s_add_u32 s14, s14, s15\n\t"
  "s_add_u32 s14, s14, 45219840\n\t"
  "s_add_u32 s56, s8, s14\n\t"
  "s_addc_u32 s57, s9, 0\n\t"
  "s_add_u32 s58, s8, 44892160\n\t"
  "s_addc_u32 s59, s9, 0\n\t"
  "s_add_u32 s14, s19, 1\n\t"
  "s_and_b32 s14, s14, 3\n\t"
  "s_mul_i32 s15, s14, 49152\n\t"
  "v_mov_b32 v252, s19\n\t"
  "v_mul_u32_u24 v245, 12288, v252\n\t"
  "v_add3_u32 v245, v245, s15, v216\n\t"
  "s_mul_i32 s14, s14, 12288\n\t"
  "v_mul_u32_u24 v249, 49152, v252\n\t"
  "v_add3_u32 v249, v249, s14, v216\n\t"
  "s_add_u32 s14, s19, 2\n\t"
  "s_and_b32 s14, s14, 3\n\t"
  "s_mul_i32 s15, s14, 49152\n\t"
  "v_mov_b32 v252, s19\n\t"
  "v_mul_u32_u24 v246, 12288, v252\n\t"
  "v_add3_u32 v246, v246, s15, v216\n\t"
  "s_mul_i32 s14, s14, 12288\n\t"
  "v_mul_u32_u24 v250, 49152, v252\n\t"
  "v_add3_u32 v250, v250, s14, v216\n\t"
  "s_add_u32 s14, s19, 3\n\t"
  "s_and_b32 s14, s14, 3\n\t"
  "s_mul_i32 s15, s14, 49152\n\t"
  "v_mov_b32 v252, s19\n\t"
  "v_mul_u32_u24 v247, 12288, v252\n\t"
  "v_add3_u32 v247, v247, s15, v216\n\t"
  "s_mul_i32 s14, s14, 12288\n\t"
  "v_mul_u32_u24 v251, 49152, v252\n\t"
  "v_add3_u32 v251, v251, s14, v216\n\t"
  "s_mul_i32 s14, s11, 49152\n\t"
  "s_mul_i32 s15, s19, 12288\n\t"
  "s_add_u32 s14, s14, s15\n\t"
  "s_add_u32 s14, s14, 2048\n\t"
  "v_add_u32 v248, s14, v216\n\t"
  "s_cmp_eq_u32 s11, s19\n\t"
  "s_cselect_b32 s99, -1, 0\n\t"
  "s_bfe_u32 s14, s20, 0x20001\n\t"
  "s_lshl_b32 s14, s14, 3\n\t"
  "s_and_b32 s15, s10, 7\n\t"
  "s_add_u32 s14, s14, s15\n\t"
  "s_lshl_b32 s14, s14, 7\n\t"
  "v_and_b32 v252, 7, v227\n\t"
  "v_lshl_add_u32 v235, v252, 12, s14\n\t"
  "s_andn2_b32 s14, s10, 6\n\t"
  "s_lshl_b32 s14, s14, 7\n\t"
  "v_and_b32 v252, 3, v227\n\t"
  "v_lshl_add_u32 v234, v252, 8, s14\n\t"
  "v_cmp_gt_u32 vcc, 32, v227\n\t"
  "s_nop 1\n\t"
  "v_cndmask_b32 v234, v234, v235, vcc\n\t"
  "s_mul_i32 s14, s98, 72\n\t"
  "s_mul_i32 s15, s21, 18\n\t"
  "s_add_u32 s14, s14, s15\n\t"
  "s_lshl_b32 s14, s14, 8\n\t"
  "s_add_u32 s14, s14, 2359296\n\t"
  "s_add_u32 s60, s8, s14\n\t"
  "s_addc_u32 s61, s9, 0\n\t"
  "s_lshl_b32 s14, s19, 10\n\t"
  "v_add_u32 v237, s14, v216\n\t"
  "v_lshrrev_b32 v252, 4, v227\n\t"
  "v_lshrrev_b32 v238, 5, v227\n\t"
  "v_lshlrev_b32 v238, 8, v238\n\t"
  "v_lshl_add_u32 v238, v253, 4, v238\n\t"
  "v_and_b32 v239, 1, v252\n\t"
  "v_lshl_add_u32 v238, v239, 3, v238\n\t"
  "v_add_u32 v238, 4096, v238\n\t"
  "s_mul_i32 s14, s98, 18432\n\t"
  "s_mul_i32 s15, s21, 288\n\t"
  "s_add_u32 s14, s14, s15\n\t"
  "s_add_u32 s62, s90, s14\n\t"
  "s_addc_u32 s63, s91, 0\n\t"
  "s_lshl_b32 s14, s19, 6\n\t"
  "v_mul_u32_u24 v240, 1152, v253\n\t"
  "v_lshl_add_u32 v239, v252, 4, v240\n\t"
  "v_add_u32 v239, s14, v239\n\t"
  "v_lshl_add_u32 v240, v252, 3, v240\n\t"
  "s_lshl_b32 s14, s98, 6\n\t"
  "v_lshl_add_u32 v242, v253, 2, s14\n\t"
  "v_mov_b32 v252, -1\n\t"
  "v_mov_b32 v253, -1\n\t"
  "v_mov_b32 v254, -1\n\t"
  "v_mov_b32 v255, -1\n\t"
  "s_cmp_lt_u32 s10, 144\n\t"
  "s_cbranch_scc1 GCONV_%=\n\t"
  "GWAIT_%=:\n\t"
  "s_memrealtime s[44:45]\n\t"
  "s_waitcnt lgkmcnt(0)\n\t"
  "s_sub_u32 s44, s44, %4\n\t"
  "s_cmp_gt_u32 s44, 580\n\t"
  "s_cbranch_scc1 GLOOP_%=\n\t"
  "s_sleep 2\n\t"
  "s_branch GWAIT_%=\n\t"
  "GCONV_%=:\n\t"
  "s_sleep 4\n\t"
  "GLOOP_%=:\n\t"
  "global_load_dword v243, v118, s[58:59] sc1\n\t"
  "s_waitcnt vmcnt(0)\n\t"
  "v_cmp_eq_u32 vcc, 0, v243\n\t"
  "s_nop 4\n\t"
  "s_cbranch_vccz GGO_%=\n\t"
  "s_add_u32 s97, s97, 1\n\t"
  "s_cmp_gt_u32 s97, 0x20000\n\t"
  "s_cbranch_scc1 GGO_%=\n\t"
  "s_sleep 2\n\t"
  "s_branch GLOOP_%=\n\t"
  "GGO_%=:\n\t"
  "s_barrier\n\t"
  "global_load_dwordx4 a[0:3], v216, s[22:23] offset:-4096 sc1\n\t"
  "global_load_dwordx4 a[16:19], v216, s[22:23] offset:-3072 sc1\n\t"
  "global_load_dwordx4 a[4:7], v216, s[24:25] offset:-4096 sc1\n\t"
  "global_load_dwordx4 a[20:23], v216, s[24:25] offset:-3072 sc1\n\t"
  "global_load_dwordx4 a[8:11], v216, s[26:27] offset:-4096 sc1\n\t"
  "global_load_dwordx4 a[24:27], v216, s[26:27] offset:-3072 sc1\n\t"
  "global_load_dwordx4 a[12:15], v216, s[28:29] offset:-4096 sc1\n\t"
  "global_load_dwordx4 a[28:31], v216, s[28:29] offset:-3072 sc1\n\t"
  "global_load_dwordx4 a[32:35], v216, s[22:23] offset:-2048 sc1\n\t"
  "global_load_dwordx4 a[48:51], v216, s[22:23] offset:-1024 sc1\n\t"
  "global_load_dwordx4 a[36:39], v216, s[24:25] offset:-2048 sc1\n\t"
  "global_load_dwordx4 a[52:55], v216, s[24:25] offset:-1024 sc1\n\t"
  "global_load_dwordx4 a[40:43], v216, s[26:27] offset:-2048 sc1\n\t"
  "global_load_dwordx4 a[56:59], v216, s[26:27] offset:-1024 sc1\n\t"
  "global_load_dwordx4 a[44:47], v216, s[28:29] offset:-2048 sc1\n\t"
  "global_load_dwordx4 a[60:63], v216, s[28:29] offset:-1024 sc1\n\t"
  "ds_read_b128 v[160:163], v219 offset:61440\n\t"
  "ds_read_b128 v[164:167], v219 offset:63488\n\t"
  "ds_read_b128 v[168:171], v219 offset:62464\n\t"
  "ds_read_b128 v[172:175], v219 offset:64512\n\t"
  "s_waitcnt vmcnt(14) lgkmcnt(2)\n\t"
  "v_mfma_f32_16x16x32_bf16 v[128:131], v[160:163], a[0:3], 0\n\t"
  "ds_read_b128 v[176:179], v220 offset:0\n\t"
  "v_mfma_f32_16x16x32_bf16 v[128:131], v[164:167], a[16:19], v[128:131]\n\t"
  "ds_read_b128 v[180:183], v220 offset:2048\n\t"
  "s_waitcnt vmcnt(12)\n\t"
  "v_mfma_f32_16x16x32_bf16 v[132:135], v[160:163], a[4:7], 0\n\t"
  "global_load_dwordx4 v[96:99], v108, s[92:93] offset:-2048 sc1\n\t"
  "v_mfma_f32_16x16x32_bf16 v[132:135], v[164:167], a[20:23], v[132:135]\n\t"
  "s_waitcnt vmcnt(11)\n\t"
  "v_mfma_f32_16x16x32_bf16 v[136:139], v[160:163], a[8:11], 0\n\t"
  "global_load_dwordx4 v[100:103], v108, s[92:93] offset:0 sc1\n\t"
  "v_mfma_f32_16x16x32_bf16 v[136:139], v[164:167], a[24:27], v[136:139]\n\t"
  "s_waitcnt vmcnt(10)\n\t"
  "v_mfma_f32_16x16x32_bf16 v[140:143], v[160:163], a[12:15], 0\n\t"
  "s_mov_b64 exec, 0xffff\n\t"
  "global_load_dwordx4 v[104:107], v108, s[92:93] offset:2048 sc1\n\t"
  "s_mov_b64 exec, -1\n\t"
  "v_mfma_f32_16x16x32_bf16 v[140:143], v[164:167], a[28:31], v[140:143]\n\t"
  "s_waitcnt lgkmcnt(2)\n\t"
  "v_mfma_f32_16x16x32_bf16 v[144:147], v[168:171], a[0:3], 0\n\t"
  "ds_read_b128 v[160:163], v220 offset:1024\n\t"
  "v_mfma_f32_16x16x32_bf16 v[144:147], v[172:175], a[16:19], v[144:147]\n\t"
  "ds_read_b128 v[164:167], v220 offset:3072\n\t"
  "v_mfma_f32_16x16x32_bf16 v[148:151], v[168:171], a[4:7], 0\n\t"
  "global_load_dwordx4 a[64:67], v216, s[22:23] offset:0 sc1\n\t"
  "v_mfma_f32_16x16x32_bf16 v[148:151], v[172:175], a[20:23], v[148:151]\n\t"
  "v_mfma_f32_16x16x32_bf16 v[152:155], v[168:171], a[8:11], 0\n\t"
  "global_load_dwordx4 a[80:83], v216, s[22:23] offset:1024 sc1\n\t"
  "v_mfma_f32_16x16x32_bf16 v[152:155], v[172:175], a[24:27], v[152:155]\n\t"
  "v_mfma_f32_16x16x32_bf16 v[156:159], v[168:171], a[12:15], 0\n\t"
  "global_load_dwordx4 a[68:71], v216, s[24:25] offset:0 sc1\n\t"
  "v_mfma_f32_16x16x32_bf16 v[156:159], v[172:175], a[28:31], v[156:159]\n\t"
  "s_waitcnt lgkmcnt(2)\n\t"
  "v_mfma_f32_16x16x32_bf16 v[32:35], v[176:179], a[0:3], 0\n\t"
  "ds_read_b128 v[168:171], v220 offset:4096\n\t"
  "v_mfma_f32_16x16x32_bf16 v[32:35], v[180:183], a[16:19], v[32:35]\n\t"
  "ds_read_b128 v[172:175], v220 offset:6144\n\t"
  "v_mfma_f32_16x16x32_bf16 v[36:39], v[176:179], a[4:7], 0\n\t"
  "global_load_dwordx4 a[84:87], v216, s[24:25] offset:1024 sc1\n\t"
  "v_mfma_f32_16x16x32_bf16 v[36:39], v[180:183], a[20:23], v[36:39]\n\t"
  "v_mfma_f32_16x16x32_bf16 v[40:43], v[176:179], a[8:11], 0\n\t"
  "global_load_dwordx4 a[72:75], v216, s[26:27] offset:0 sc1\n\t"
  "v_mfma_f32_16x16x32_bf16 v[40:43], v[180:183], a[24:27], v[40:43]\n\t"
  "v_mfma_f32_16x16x32_bf16 v[44:47], v[176:179], a[12:15], 0\n\t"
  "global_load_dwordx4 a[88:91], v216, s[26:27] offset:1024 sc1\n\t"
  "v_mfma_f32_16x16x32_bf16 v[44:47], v[180:183], a[28:31], v[44:47]\n\t"
  "s_waitcnt lgkmcnt(2)\n\t"
  "v_mfma_f32_16x16x32_bf16 v[48:51], v[160:163], a[0:3], 0\n\t"
  "ds_read_b128 v[176:179], v220 offset:5120\n\t"
  "v_mfma_f32_16x16x32_bf16 v[48:51], v[164:167], a[16:19], v[48:51]\n\t"
  "ds_read_b128 v[180:183], v220 offset:7168\n\t"
  "v_mfma_f32_16x16x32_bf16 v[52:55], v[160:163], a[4:7], 0\n\t"
  "global_load_dwordx4 a[76:79], v216, s[28:29] offset:0 sc1\n\t"
  "v_mfma_f32_16x16x32_bf16 v[52:55], v[164:167], a[20:23], v[52:55]\n\t"
  "v_mfma_f32_16x16x32_bf16 v[56:59], v[160:163], a[8:11], 0\n\t"
  "global_load_dwordx4 a[92:95], v216, s[28:29] offset:1024 sc1\n\t"
  "v_mfma_f32_16x16x32_bf16 v[56:59], v[164:167], a[24:27], v[56:59]\n\t"
  "v_mfma_f32_16x16x32_bf16 v[60:63], v[160:163], a[12:15], 0\n\t"
  "v_mfma_f32_16x16x32_bf16 v[60:63], v[164:167], a[28:31], v[60:63]\n\t"
  "s_waitcnt lgkmcnt(2)\n\t"
  "v_mfma_f32_16x16x32_bf16 v[64:67], v[168:171], a[0:3], 0\n\t"
  "ds_read_b128 v[160:163], v220 offset:8192\n\t"
  "v_mfma_f32_16x16x32_bf16 v[64:67], v[172:175], a[16:19], v[64:67]\n\t"
  "ds_read_b128 v[164:167], v220 offset:10240\n\t"
  "v_mfma_f32_16x16x32_bf16 v[68:71], v[168:171], a[4:7], 0\n\t"
  "global_load_dwordx4 a[96:99], v216, s[22:23] offset:2048 sc1\n\t"
  "v_mfma_f32_16x16x32_bf16 v[68:71], v[172:175], a[20:23], v[68:71]\n\t"
  "v_mfma_f32_16x16x32_bf16 v[72:75], v[168:171], a[8:11], 0\n\t"
  "v_mfma_f32_16x16x32_bf16 v[72:75], v[172:175], a[24:27], v[72:75]\n\t"
  "global_load_dwordx4 a[112:115], v216, s[22:23] offset:3072 sc1\n\t"
  "v_mfma_f32_16x16x32_bf16 v[76:79], v[168:171], a[12:15], 0\n\t"
  "v_mfma_f32_16x16x32_bf16 v[76:79], v[172:175], a[28:31], v[76:79]\n\t"
  "s_waitcnt lgkmcnt(2)\n\t"
  "v_mfma_f32_16x16x32_bf16 v[80:83], v[176:179], a[0:3], 0\n\t"
  "ds_read_b128 v[168:171], v220 offset:9216\n\t"
  "v_mfma_f32_16x16x32_bf16 v[80:83], v[180:183], a[16:19], v[80:83]\n\t"
  "ds_read_b128 v[172:175], v220 offset:11264\n\t"
  "v_mfma_f32_16x16x32_bf16 v[84:87], v[176:179], a[4:7], 0\n\t"
  "global_load_dwordx4 a[100:103], v216, s[24:25] offset:2048 sc1\n\t"
  "v_mfma_f32_16x16x32_bf16 v[84:87], v[180:183], a[20:23], v[84:87]\n\t"
  "v_mfma_f32_16x16x32_bf16 v[88:91], v[176:179], a[8:11], 0\n\t"
  "v_mfma_f32_16x16x32_bf16 v[88:91], v[180:183], a[24:27], v[88:91]\n\t"
  "global_load_dwordx4 a[116:119], v216, s[24:25] offset:3072 sc1\n\t"
  "v_mfma_f32_16x16x32_bf16 v[92:95], v[176:179], a[12:15], 0\n\t"
  "v_mfma_f32_16x16x32_bf16 v[92:95], v[180:183], a[28:31], v[92:95]\n\t"
  "s_waitcnt vmcnt(21) lgkmcnt(2)\n\t"
  "v_mfma_f32_16x16x32_bf16 v[128:131], v[160:163], a[32:35], v[128:131]\n\t"
  "ds_read_b128 v[176:179], v220 offset:12288\n\t"
  "v_mfma_f32_16x16x32_bf16 v[128:131], v[164:167], a[48:51], v[128:131]\n\t"
  "ds_read_b128 v[180:183], v220 offset:14336\n\t"
  "s_waitcnt vmcnt(19)\n\t"
  "v_mfma_f32_16x16x32_bf16 v[132:135], v[160:163], a[36:39], v[132:135]\n\t"
  "global_load_dwordx4 a[104:107], v216, s[26:27] offset:2048 sc1\n\t"
  "v_mfma_f32_16x16x32_bf16 v[132:135], v[164:167], a[52:55], v[132:135]\n\t"
  "s_waitcnt vmcnt(18)\n\t"
  "v_mfma_f32_16x16x32_bf16 v[136:139], v[160:163], a[40:43], v[136:139]\n\t"
  "v_mfma_f32_16x16x32_bf16 v[136:139], v[164:167], a[56:59], v[136:139]\n\t"
  "global_load_dwordx4 a[120:123], v216, s[26:27] offset:3072 sc1\n\t"
  "s_waitcnt vmcnt(17)\n\t"
  "v_mfma_f32_16x16x32_bf16 v[140:143], v[160:163], a[44:47], v[140:143]\n\t"
  "v_mfma_f32_16x16x32_bf16 v[140:143], v[164:167], a[60:63], v[140:143]\n\t"
  "s_waitcnt lgkmcnt(2)\n\t"
  "v_mfma_f32_16x16x32_bf16 v[144:147], v[168:171], a[32:35], v[144:147]\n\t"
  "ds_read_b128 v[160:163], v220 offset:13312\n\t"
  "v_mfma_f32_16x16x32_bf16 v[144:147], v[172:175], a[48:51], v[144:147]\n\t"
  "ds_read_b128 v[164:167], v220 offset:15360\n\t"
  "v_mfma_f32_16x16x32_bf16 v[148:151], v[168:171], a[36:39], v[148:151]\n\t"
  "global_load_dwordx4 a[108:111], v216, s[28:29] offset:2048 sc1\n\t"
  "v_mfma_f32_16x16x32_bf16 v[148:151], v[172:175], a[52:55], v[148:151]\n\t"
  "v_mfma_f32_16x16x32_bf16 v[152:155], v[168:171], a[40:43], v[152:155]\n\t"
  "v_mfma_f32_16x16x32_bf16 v[152:155], v[172:175], a[56:59], v[152:155]\n\t"
  "global_load_dwordx4 a[124:127], v216, s[28:29] offset:3072 sc1\n\t"
  "v_mfma_f32_16x16x32_bf16 v[156:159], v[168:171], a[44:47], v[156:159]\n\t"
  "v_mfma_f32_16x16x32_bf16 v[156:159], v[172:175], a[60:63], v[156:159]\n\t"
  "s_waitcnt lgkmcnt(2)\n\t"
  "v_mfma_f32_16x16x32_bf16 v[32:35], v[176:179], a[32:35], v[32:35]\n\t"
  "ds_read_b128 v[168:171], v220 offset:16384\n\t"
  "v_mfma_f32_16x16x32_bf16 v[32:35], v[180:183], a[48:51], v[32:35]\n\t"
  "ds_read_b128 v[172:175], v220 offset:18432\n\t"
  "v_mfma_f32_16x16x32_bf16 v[36:39], v[176:179], a[36:39], v[36:39]\n\t"
  "v_mfma_f32_16x16x32_bf16 v[36:39], v[180:183], a[52:55], v[36:39]\n\t"
  "v_mfma_f32_16x16x32_bf16 v[40:43], v[176:179], a[40:43], v[40:43]\n\t"
  "v_mfma_f32_16x16x32_bf16 v[40:43], v[180:183], a[56:59], v[40:43]\n\t"
  "v_mfma_f32_16x16x32_bf16 v[44:47], v[176:179], a[44:47], v[44:47]\n\t"
  "v_mfma_f32_16x16x32_bf16 v[44:47], v[180:183], a[60:63], v[44:47]\n\t"
  "s_waitcnt lgkmcnt(2)\n\t"
  "v_mfma_f32_16x16x32_bf16 v[48:51], v[160:163], a[32:35], v[48:51]\n\t"
  "ds_read_b128 v[176:179], v220 offset:17408\n\t"
  "v_mfma_f32_16x16x32_bf16 v[48:51], v[164:167], a[48:51], v[48:51]\n\t"
  "ds_read_b128 v[180:183], v220 offset:19456\n\t"
  "v_mfma_f32_16x16x32_bf16 v[52:55], v[160:163], a[36:39], v[52:55]\n\t"
  "v_mfma_f32_16x16x32_bf16 v[52:55], v[164:167], a[52:55], v[52:55]\n\t"
  "v_mfma_f32_16x16x32_bf16 v[56:59], v[160:163], a[40:43], v[56:59]\n\t"
  "v_mfma_f32_16x16x32_bf16 v[56:59], v[164:167], a[56:59], v[56:59]\n\t"
  "v_mfma_f32_16x16x32_bf16 v[60:63], v[160:163], a[44:47], v[60:63]\n\t"
  "v_mfma_f32_16x16x32_bf16 v[60:63], v[164:167], a[60:63], v[60:63]\n\t"
  "s_waitcnt lgkmcnt(2)\n\t"
  "v_mfma_f32_16x16x32_bf16 v[64:67], v[168:171], a[32:35], v[64:67]\n\t"
  "ds_read_b128 v[160:163], v220 offset:20480\n\t"
  "s_waitcnt vmcnt(16)\n\t"
  "v_mfma_f32_16x16x32_bf16 v[64:67], v[172:175], a[48:51], v[64:67]\n\t"
  "ds_read_b128 v[164:167], v220 offset:22528\n\t"
  "v_lshlrev_b32 v116, 16, v96\n\t"
  "v_mfma_f32_16x16x32_bf16 v[68:71], v[168:171], a[36:39], v[68:71]\n\t"
  "global_load_dwordx4 a[128:131], v217, s[22:23] offset:-4096 sc1\n\t"
  "v_and_b32 v117, 0xffff0000, v96\n\t"
  "v_mfma_f32_16x16x32_bf16 v[68:71], v[172:175], a[52:55], v[68:71]\n\t"
  "v_mov_b32 v112, v116\n\t"
  "v_mov_b32 v114, v117\n\t"
  "v_mfma_f32_16x16x32_bf16 v[72:75], v[168:171], a[40:43], v[72:75]\n\t"
  "v_mul_f32 v113, v116, v116\n\t"
  "v_mul_f32 v115, v117, v117\n\t"
  "v_mfma_f32_16x16x32_bf16 v[72:75], v[172:175], a[56:59], v[72:75]\n\t"
  "global_load_dwordx4 a[144:147], v217, s[22:23] offset:-3072 sc1\n\t"
  "v_lshlrev_b32 v116, 16, v97\n\t"
  "v_mfma_f32_16x16x32_bf16 v[76:79], v[168:171], a[44:47], v[76:79]\n\t"
  "v_and_b32 v117, 0xffff0000, v97\n\t"
  "v_add_f32 v112, v112, v116\n\t"
  "v_mfma_f32_16x16x32_bf16 v[76:79], v[172:175], a[60:63], v[76:79]\n\t"
  "v_add_f32 v114, v114, v117\n\t"
  "s_waitcnt lgkmcnt(2)\n\t"
  "v_mfma_f32_16x16x32_bf16 v[80:83], v[176:179], a[32:35], v[80:83]\n\t"
  "ds_read_b128 v[168:171], v220 offset:21504\n\t"
  "v_fmac_f32 v113, v116, v116\n\t"
  "v_mfma_f32_16x16x32_bf16 v[80:83], v[180:183], a[48:51], v[80:83]\n\t"
  "ds_read_b128 v[172:175], v220 offset:23552\n\t"
  "v_fmac_f32 v115, v117, v117\n\t"
  "v_mfma_f32_16x16x32_bf16 v[84:87], v[176:179], a[36:39], v[84:87]\n\t"
  "global_load_dwordx4 a[132:135], v217, s[24:25] offset:-4096 sc1\n\t"
  "v_lshlrev_b32 v116, 16, v98\n\t"
  "v_mfma_f32_16x16x32_bf16 v[84:87], v[180:183], a[52:55], v[84:87]\n\t"
  "v_and_b32 v117, 0xffff0000, v98\n\t"
  "v_add_f32 v112, v112, v116\n\t"
  "v_mfma_f32_16x16x32_bf16 v[88:91], v[176:179], a[40:43], v[88:91]\n\t"
  "v_add_f32 v114, v114, v117\n\t"
  "v_fmac_f32 v113, v116, v116\n\t"
  "v_mfma_f32_16x16x32_bf16 v[88:91], v[180:183], a[56:59], v[88:91]\n\t"
  "global_load_dwordx4 a[148:151], v217, s[24:25] offset:-3072 sc1\n\t"
  "v_fmac_f32 v115, v117, v117\n\t"
  "v_mfma_f32_16x16x32_bf16 v[92:95], v[176:179], a[44:47], v[92:95]\n\t"
  "v_lshlrev_b32 v116, 16, v99\n\t"
  "v_and_b32 v117, 0xffff0000, v99\n\t"
  "v_mfma_f32_16x16x32_bf16 v[92:95], v[180:183], a[60:63], v[92:95]\n\t"
  "v_add_f32 v112, v112, v116\n\t"
  "s_waitcnt vmcnt(18) lgkmcnt(2)\n\t"
  "v_mfma_f32_16x16x32_bf16 v[128:131], v[160:163], a[64:67], v[128:131]\n\t"
  "ds_read_b128 v[176:179], v220 offset:24576\n\t"
  "v_add_f32 v114, v114, v117\n\t"
  "v_mfma_f32_16x16x32_bf16 v[128:131], v[164:167], a[80:83], v[128:131]\n\t"
  "ds_read_b128 v[180:183], v220 offset:26624\n\t"
  "s_waitcnt vmcnt(16)\n\t"
  "v_mfma_f32_16x16x32_bf16 v[132:135], v[160:163], a[68:71], v[132:135]\n\t"
  "global_load_dwordx4 a[136:139], v217, s[26:27] offset:-4096 sc1\n\t"
  "v_fmac_f32 v113, v116, v116\n\t"
  "v_mfma_f32_16x16x32_bf16 v[132:135], v[164:167], a[84:87], v[132:135]\n\t"
  "v_fmac_f32 v115, v117, v117\n\t"
  "s_waitcnt vmcnt(15)\n\t"
  "v_mfma_f32_16x16x32_bf16 v[136:139], v[160:163], a[72:75], v[136:139]\n\t"
  "v_lshlrev_b32 v116, 16, v100\n\t"
  "v_and_b32 v117, 0xffff0000, v100\n\t"
  "v_mfma_f32_16x16x32_bf16 v[136:139], v[164:167], a[88:91], v[136:139]\n\t"
  "global_load_dwordx4 a[152:155], v217, s[26:27] offset:-3072 sc1\n\t"
  "s_waitcnt vmcnt(14)\n\t"
  "v_mfma_f32_16x16x32_bf16 v[140:143], v[160:163], a[76:79], v[140:143]\n\t"
  "v_add_f32 v112, v112, v116\n\t"
  "v_add_f32 v114, v114, v117\n\t"
  "v_mfma_f32_16x16x32_bf16 v[140:143], v[164:167], a[92:95], v[140:143]\n\t"
  "v_fmac_f32 v113, v116, v116\n\t"
  "s_waitcnt lgkmcnt(2)\n\t"
  "v_mfma_f32_16x16x32_bf16 v[144:147], v[168:171], a[64:67], v[144:147]\n\t"
  "ds_read_b128 v[160:163], v220 offset:25600\n\t"
  "v_fmac_f32 v115, v117, v117\n\t"
  "v_mfma_f32_16x16x32_bf16 v[144:147], v[172:175], a[80:83], v[144:147]\n\t"
  "ds_read_b128 v[164:167], v220 offset:27648\n\t"
  "v_lshlrev_b32 v116, 16, v101\n\t"
  "v_mfma_f32_16x16x32_bf16 v[148:151], v[168:171], a[68:71], v[148:151]\n\t"
  "global_load_dwordx4 a[140:143], v217, s[28:29] offset:-4096 sc1\n\t"
  "v_and_b32 v117, 0xffff0000, v101\n\t"
  "v_mfma_f32_16x16x32_bf16 v[148:151], v[172:175], a[84:87], v[148:151]\n\t"
  "v_add_f32 v112, v112, v116\n\t"
  "v_add_f32 v114, v114, v117\n\t"
  "v_mfma_f32_16x16x32_bf16 v[152:155], v[168:171], a[72:75], v[152:155]\n\t"
  "v_fmac_f32 v113, v116, v116\n\t"
  "v_fmac_f32 v115, v117, v117\n\t"
  "v_mfma_f32_16x16x32_bf16 v[152:155], v[172:175], a[88:91], v[152:155]\n\t"
  "global_load_dwordx4 a[156:159], v217, s[28:29] offset:-3072 sc1\n\t"
  "v_lshlrev_b32 v116, 16, v102\n\t"
  "v_mfma_f32_16x16x32_bf16 v[156:159], v[168:171], a[76:79], v[156:159]\n\t"
  "v_and_b32 v117, 0xffff0000, v102\n\t"
  "v_add_f32 v112, v112, v116\n\t"
  "v_mfma_f32_16x16x32_bf16 v[156:159], v[172:175], a[92:95], v[156:159]\n\t"
  "v_add_f32 v114, v114, v117\n\t"
  "s_waitcnt lgkmcnt(2)\n\t"
  "v_mfma_f32_16x16x32_bf16 v[32:35], v[176:179], a[64:67], v[32:35]\n\t"
  "ds_read_b128 v[168:171], v220 offset:28672\n\t"
  "v_fmac_f32 v113, v116, v116\n\t"
  "v_mfma_f32_16x16x32_bf16 v[32:35], v[180:183], a[80:83], v[32:35]\n\t"
  "ds_read_b128 v[172:175], v220 offset:30720\n\t"
  "v_fmac_f32 v115, v117, v117\n\t"
  "v_mfma_f32_16x16x32_bf16 v[36:39], v[176:179], a[68:71], v[36:39]\n\t"
  "v_lshlrev_b32 v116, 16, v103\n\t"
  "v_and_b32 v117, 0xffff0000, v103\n\t"
  "v_mfma_f32_16x16x32_bf16 v[36:39], v[180:183], a[84:87], v[36:39]\n\t"
  "v_add_f32 v112, v112, v116\n\t"
  "v_add_f32 v114, v114, v117\n\t"
  "v_mfma_f32_16x16x32_bf16 v[40:43], v[176:179], a[72:75], v[40:43]\n\t"
  "v_fmac_f32 v113, v116, v116\n\t"
  "v_fmac_f32 v115, v117, v117\n\t"
  "v_mfma_f32_16x16x32_bf16 v[40:43], v[180:183], a[88:91], v[40:43]\n\t"
  "v_lshlrev_b32 v116, 16, v104\n\t"
  "v_and_b32 v117, 0xffff0000, v104\n\t"
  "v_mfma_f32_16x16x32_bf16 v[44:47], v[176:179], a[76:79], v[44:47]\n\t"
  "v_add_f32 v112, v112, v116\n\t"
  "v_add_f32 v114, v114, v117\n\t"
  "v_mfma_f32_16x16x32_bf16 v[44:47], v[180:183], a[92:95], v[44:47]\n\t"
  "v_fmac_f32 v113, v116, v116\n\t"
  "s_waitcnt lgkmcnt(2)\n\t"
  "v_mfma_f32_16x16x32_bf16 v[48:51], v[160:163], a[64:67], v[48:51]\n\t"
  "ds_read_b128 v[176:179], v220 offset:29696\n\t"
  "v_fmac_f32 v115, v117, v117\n\t"
  "v_mfma_f32_16x16x32_bf16 v[48:51], v[164:167], a[80:83], v[48:51]\n\t"
  "ds_read_b128 v[180:183], v220 offset:31744\n\t"
  "v_lshlrev_b32 v116, 16, v105\n\t"
  "v_mfma_f32_16x16x32_bf16 v[52:55], v[160:163], a[68:71], v[52:55]\n\t"
  "v_and_b32 v117, 0xffff0000, v105\n\t"
  "PG0_%=:\n\t"
  "s_cbranch_execz PG4_%=\n\t"
  "v_add_f32 v112, v112, v116\n\t"
  "v_mfma_f32_16x16x32_bf16 v[52:55], v[164:167], a[84:87], v[52:55]\n\t"
  "v_add_f32 v114, v114, v117\n\t"
  "v_fmac_f32 v113, v116, v116\n\t"
  "v_mfma_f32_16x16x32_bf16 v[56:59], v[160:163], a[72:75], v[56:59]\n\t"
  "v_fmac_f32 v115, v117, v117\n\t"
  "v_lshlrev_b32 v116, 16, v106\n\t"
  "v_mfma_f32_16x16x32_bf16 v[56:59], v[164:167], a[88:91], v[56:59]\n\t"
  "v_and_b32 v117, 0xffff0000, v106\n\t"
  "v_add_f32 v112, v112, v116\n\t"
  "v_mfma_f32_16x16x32_bf16 v[60:63], v[160:163], a[76:79], v[60:63]\n\t"
  "v_add_f32 v114, v114, v117\n\t"
  "v_fmac_f32 v113, v116, v116\n\t"
  "v_mfma_f32_16x16x32_bf16 v[60:63], v[164:167], a[92:95], v[60:63]\n\t"
  "v_fmac_f32 v115, v117, v117\n\t"
  "s_waitcnt lgkmcnt(2)\n\t"
  "v_mfma_f32_16x16x32_bf16 v[64:67], v[168:171], a[64:67], v[64:67]\n\t"
  "ds_read_b128 v[160:163], v219 offset:24576\n\t"
  "v_lshlrev_b32 v116, 16, v107\n\t"
  "v_mfma_f32_16x16x32_bf16 v[64:67], v[172:175], a[80:83], v[64:67]\n\t"
  "ds_read_b128 v[164:167], v219 offset:26624\n\t"
  "v_and_b32 v117, 0xffff0000, v107\n\t"
  "v_mfma_f32_16x16x32_bf16 v[68:71], v[168:171], a[68:71], v[68:71]\n\t"
  "global_load_dwordx4 a[160:163], v217, s[22:23] offset:-2048 sc1\n\t"
  "v_add_f32 v112, v112, v116\n\t"
  "v_mfma_f32_16x16x32_bf16 v[68:71], v[172:175], a[84:87], v[68:71]\n\t"
  "v_add_f32 v114, v114, v117\n\t"
  "v_fmac_f32 v113, v116, v116\n\t"
  "v_mfma_f32_16x16x32_bf16 v[72:75], v[168:171], a[72:75], v[72:75]\n\t"
  "v_fmac_f32 v115, v117, v117\n\t"
  "v_add_f32 v112, v112, v114\n\t"
  "v_mfma_f32_16x16x32_bf16 v[72:75], v[172:175], a[88:91], v[72:75]\n\t"
  "global_load_dwordx4 a[176:179], v217, s[22:23] offset:-1024 sc1\n\t"
  "v_add_f32 v113, v113, v115\n\t"
  "v_mfma_f32_16x16x32_bf16 v[76:79], v[168:171], a[76:79], v[76:79]\n\t"
  "v_add_f32_dpp v112, v112, v112 row_ror:8 row_mask:0xf bank_mask:0xf\n\t"
  "v_add_f32_dpp v113, v113, v113 row_ror:8 row_mask:0xf bank_mask:0xf\n\t"
  "v_mfma_f32_16x16x32_bf16 v[76:79], v[172:175], a[92:95], v[76:79]\n\t"
  "v_mov_b32 v114, v112\n\t"
  "s_waitcnt lgkmcnt(2)\n\t"
  "v_mfma_f32_16x16x32_bf16 v[80:83], v[176:179], a[64:67], v[80:83]\n\t"
  "ds_read_b128 v[168:171], v219 offset:25600\n\t"
  "v_mov_b32 v115, v113\n\t"
  "v_mfma_f32_16x16x32_bf16 v[80:83], v[180:183], a[80:83], v[80:83]\n\t"
  "ds_read_b128 v[172:175], v219 offset:27648\n\t"
  "v_permlane16_swap_b32 v112, v114\n\t"
  "v_mfma_f32_16x16x32_bf16 v[84:87], v[176:179], a[68:71], v[84:87]\n\t"
  "global_load_dwordx4 a[164:167], v217, s[24:25] offset:-2048 sc1\n\t"
  "v_permlane16_swap_b32 v113, v115\n\t"
  "v_mfma_f32_16x16x32_bf16 v[84:87], v[180:183], a[84:87], v[84:87]\n\t"
  "v_add_f32 v112, v112, v114\n\t"
  "v_add_f32 v113, v113, v115\n\t"
  "v_mfma_f32_16x16x32_bf16 v[88:91], v[176:179], a[72:75], v[88:91]\n\t"
  "v_mov_b32 v114, v112\n\t"
  "v_mov_b32 v115, v113\n\t"
  "v_mfma_f32_16x16x32_bf16 v[88:91], v[180:183], a[88:91], v[88:91]\n\t"
  "global_load_dwordx4 a[180:183], v217, s[24:25] offset:-1024 sc1\n\t"
  "v_permlane32_swap_b32 v112, v114\n\t"
  "v_mfma_f32_16x16x32_bf16 v[92:95], v[176:179], a[76:79], v[92:95]\n\t"
  "v_permlane32_swap_b32 v113, v115\n\t"
  "v_add_f32 v112, v112, v114\n\t"
  "v_mfma_f32_16x16x32_bf16 v[92:95], v[180:183], a[92:95], v[92:95]\n\t"
  "v_add_f32 v113, v113, v115\n\t"
  "s_waitcnt vmcnt(18) lgkmcnt(2)\n\t"
  "v_mfma_f32_16x16x32_bf16 v[128:131], v[160:163], a[96:99], v[128:131]\n\t"
  "ds_read_b128 v[176:179], v219 offset:28672\n\t"
  "ds_write_b64 v111, v[112:113] offset:40960\n\t"
  "v_mfma_f32_16x16x32_bf16 v[128:131], v[164:167], a[112:115], v[128:131]\n\t"
  "ds_read_b128 v[180:183], v219 offset:30720\n\t"
  "s_waitcnt vmcnt(16)\n\t"
  "v_mfma_f32_16x16x32_bf16 v[132:135], v[160:163], a[100:103], v[132:135]\n\t"
  "global_load_dwordx4 a[168:171], v217, s[26:27] offset:-2048 sc1\n\t"
  "s_waitcnt lgkmcnt(1)\n\t"
  "v_mfma_f32_16x16x32_bf16 v[132:135], v[164:167], a[116:119], v[132:135]\n\t"
  "s_waitcnt vmcnt(15)\n\t"
  "v_mfma_f32_16x16x32_bf16 v[136:139], v[160:163], a[104:107], v[136:139]\n\t"
  "v_mfma_f32_16x16x32_bf16 v[136:139], v[164:167], a[120:123], v[136:139]\n\t"
  "global_load_dwordx4 a[184:187], v217, s[26:27] offset:-1024 sc1\n\t"
  "s_waitcnt vmcnt(14)\n\t"
  "v_mfma_f32_16x16x32_bf16 v[140:143], v[160:163], a[108:111], v[140:143]\n\t"
  "v_mfma_f32_16x16x32_bf16 v[140:143], v[164:167], a[124:127], v[140:143]\n\t"
  "v_mfma_f32_16x16x32_bf16 v[144:147], v[168:171], a[96:99], v[144:147]\n\t"
  "ds_read_b128 v[160:163], v219 offset:29696\n\t"
  "v_mfma_f32_16x16x32_bf16 v[144:147], v[172:175], a[112:115], v[144:147]\n\t"
  "ds_read_b128 v[164:167], v219 offset:31744\n\t"
  "v_mfma_f32_16x16x32_bf16 v[148:151], v[168:171], a[100:103], v[148:151]\n\t"
  "global_load_dwordx4 a[172:175], v217, s[28:29] offset:-2048 sc1\n\t"
  "v_mfma_f32_16x16x32_bf16 v[148:151], v[172:175], a[116:119], v[148:151]\n\t"
  "v_mfma_f32_16x16x32_bf16 v[152:155], v[168:171], a[104:107], v[152:155]\n\t"
  "v_mfma_f32_16x16x32_bf16 v[152:155], v[172:175], a[120:123], v[152:155]\n\t"
  "global_load_dwordx4 a[188:191], v217, s[28:29] offset:-1024 sc1\n\t"
  "v_mfma_f32_16x16x32_bf16 v[156:159], v[168:171], a[108:111], v[156:159]\n\t"
  "v_mfma_f32_16x16x32_bf16 v[156:159], v[172:175], a[124:127], v[156:159]\n\t"
  "s_waitcnt lgkmcnt(2)\n\t"
  "v_mfma_f32_16x16x32_bf16 v[32:35], v[176:179], a[96:99], v[32:35]\n\t"
  "ds_read_b128 v[168:171], v219 offset:32768\n\t"
  "v_mfma_f32_16x16x32_bf16 v[32:35], v[180:183], a[112:115], v[32:35]\n\t"
  "ds_read_b128 v[172:175], v219 offset:34816\n\t"
  "v_mfma_f32_16x16x32_bf16 v[36:39], v[176:179], a[100:103], v[36:39]\n\t"
  "v_mfma_f32_16x16x32_bf16 v[36:39], v[180:183], a[116:119], v[36:39]\n\t"
  "v_mfma_f32_16x16x32_bf16 v[40:43], v[176:179], a[104:107], v[40:43]\n\t"
  "v_mfma_f32_16x16x32_bf16 v[40:43], v[180:183], a[120:123], v[40:43]\n\t"
  "v_mfma_f32_16x16x32_bf16 v[44:47], v[176:179], a[108:111], v[44:47]\n\t"
  "v_mfma_f32_16x16x32_bf16 v[44:47], v[180:183], a[124:127], v[44:47]\n\t"
  "s_waitcnt lgkmcnt(2)\n\t"
  "v_mfma_f32_16x16x32_bf16 v[48:51], v[160:163], a[96:99], v[48:51]\n\t"
  "ds_read_b128 v[176:179], v219 offset:33792\n\t"
  "v_mfma_f32_16x16x32_bf16 v[48:51], v[164:167], a[112:115], v[48:51]\n\t"
  "ds_read_b128 v[180:183], v219 offset:35840\n\t"
  "v_mfma_f32_16x16x32_bf16 v[52:55], v[160:163], a[100:103], v[52:55]\n\t"
  "v_mfma_f32_16x16x32_bf16 v[52:55], v[164:167], a[116:119], v[52:55]\n\t"
  "v_mfma_f32_16x16x32_bf16 v[56:59], v[160:163], a[104:107], v[56:59]\n\t"
  "v_mfma_f32_16x16x32_bf16 v[56:59], v[164:167], a[120:123], v[56:59]\n\t"
  "v_mfma_f32_16x16x32_bf16 v[60:63], v[160:163], a[108:111], v[60:63]\n\t"
  "v_mfma_f32_16x16x32_bf16 v[60:63], v[164:167], a[124:127], v[60:63]\n\t"
  "s_waitcnt lgkmcnt(2)\n\t"
  "s_barrier\n\t"
  "v_mfma_f32_16x16x32_bf16 v[64:67], v[168:171], a[96:99], v[64:67]\n\t"
  "ds_read_b128 v[160:163], v219 offset:36864\n\t"
  "ds_read_b64 v[96:97], v110 offset:40960\n\t"
  "v_mfma_f32_16x16x32_bf16 v[64:67], v[172:175], a[112:115], v[64:67]\n\t"
  "ds_read_b128 v[164:167], v219 offset:38912\n\t"
  "ds_read_b64 v[98:99], v110 offset:41024\n\t"
  "v_mfma_f32_16x16x32_bf16 v[68:71], v[168:171], a[100:103], v[68:71]\n\t"
  "ds_read_b64 v[100:101], v110 offset:41088\n\t"
  "v_mfma_f32_16x16x32_bf16 v[68:71], v[172:175], a[116:119], v[68:71]\n\t"
  "ds_read_b64 v[102:103], v110 offset:41152\n\t"
  "s_waitcnt lgkmcnt(0)\n\t"
  "v_mfma_f32_16x16x32_bf16 v[72:75], v[168:171], a[104:107], v[72:75]\n\t"
  "global_load_dword v243, v241, s[58:59] offset:0 sc1\n\t"
  "v_add_f32 v112, v96, v98\n\t"
  "v_mfma_f32_16x16x32_bf16 v[72:75], v[172:175], a[120:123], v[72:75]\n\t"
  "global_load_dwordx4 a[192:195], v217, s[22:23] offset:0 sc1\n\t"
  "v_add_f32 v114, v100, v102\n\t"
  "v_mfma_f32_16x16x32_bf16 v[76:79], v[168:171], a[108:111], v[76:79]\n\t"
  "v_add_f32 v113, v97, v99\n\t"
  "v_add_f32 v115, v101, v103\n\t"
  "v_mfma_f32_16x16x32_bf16 v[76:79], v[172:175], a[124:127], v[76:79]\n\t"
  "v_add_f32 v112, v112, v114\n\t"
  "v_mfma_f32_16x16x32_bf16 v[80:83], v[176:179], a[96:99], v[80:83]\n\t"
  "ds_read_b128 v[168:171], v219 offset:37888\n\t"
  "v_add_f32 v113, v113, v115\n\t"
  "v_mfma_f32_16x16x32_bf16 v[80:83], v[180:183], a[112:115], v[80:83]\n\t"
  "ds_read_b128 v[172:175], v219 offset:39936\n\t"
  "v_mul_f32 v112, 0x3ae38e39, v112\n\t"
  "v_mfma_f32_16x16x32_bf16 v[84:87], v[176:179], a[100:103], v[84:87]\n\t"
  "global_load_dwordx4 a[208:211], v217, s[22:23] offset:1024 sc1\n\t"
  "v_mul_f32 v113, 0x3ae38e39, v113\n\t"
  "v_mfma_f32_16x16x32_bf16 v[84:87], v[180:183], a[116:119], v[84:87]\n\t"
  "v_fma_f32 v113, -v112, v112, v113\n\t"
  "v_max_f32 v113, 0, v113\n\t"
  "v_mfma_f32_16x16x32_bf16 v[88:91], v[176:179], a[104:107], v[88:91]\n\t"
  "v_add_f32 v113, 0x3727c5ac, v113\n\t"
  "v_mfma_f32_16x16x32_bf16 v[88:91], v[180:183], a[120:123], v[88:91]\n\t"
  "global_load_dwordx4 a[196:199], v217, s[24:25] offset:0 sc1\n\t"
  "v_mfma_f32_16x16x32_bf16 v[92:95], v[176:179], a[108:111], v[92:95]\n\t"
  "v_rsq_f32 v113, v113\n\t"
  "v_mfma_f32_16x16x32_bf16 v[92:95], v[180:183], a[124:127], v[92:95]\n\t"
  "v_or_b32 v113, 0x80000000, v113\n\t"
  "s_waitcnt vmcnt(18)\n\t"
  "v_mfma_f32_16x16x32_bf16 v[128:131], v[160:163], a[128:131], v[128:131]\n\t"
  "ds_read_b128 v[176:179], v219 offset:40960\n\t"
  "s_mov_b64 exec, 0xff\n\t"
  "global_store_dword v109, v113, s[42:43] offset:0 sc1\n\t"
  "s_mov_b64 exec, -1\n\t"
  "v_mfma_f32_16x16x32_bf16 v[128:131], v[164:167], a[144:147], v[128:131]\n\t"
  "ds_read_b128 v[180:183], v219 offset:43008\n\t"
  "s_waitcnt vmcnt(17)\n\t"
  "v_mfma_f32_16x16x32_bf16 v[132:135], v[160:163], a[132:135], v[132:135]\n\t"
  "global_load_dwordx4 a[212:215], v217, s[24:25] offset:1024 sc1\n\t"
  "v_mfma_f32_16x16x32_bf16 v[132:135], v[164:167], a[148:151], v[132:135]\n\t"
  "s_waitcnt vmcnt(16)\n\t"
  "v_mfma_f32_16x16x32_bf16 v[136:139], v[160:163], a[136:139], v[136:139]\n\t"
  "v_mfma_f32_16x16x32_bf16 v[136:139], v[164:167], a[152:155], v[136:139]\n\t"
  "global_load_dwordx4 a[200:203], v217, s[26:27] offset:0 sc1\n\t"
  "s_waitcnt vmcnt(15)\n\t"
  "v_mfma_f32_16x16x32_bf16 v[140:143], v[160:163], a[140:143], v[140:143]\n\t"
  "v_mfma_f32_16x16x32_bf16 v[140:143], v[164:167], a[156:159], v[140:143]\n\t"
  "s_waitcnt lgkmcnt(2)\n\t"
  "v_mfma_f32_16x16x32_bf16 v[144:147], v[168:171], a[128:131], v[144:147]\n\t"
  "ds_read_b128 v[160:163], v219 offset:41984\n\t"
  "v_mfma_f32_16x16x32_bf16 v[144:147], v[172:175], a[144:147], v[144:147]\n\t"
  "ds_read_b128 v[164:167], v219 offset:44032\n\t"
  "v_mfma_f32_16x16x32_bf16 v[148:151], v[168:171], a[132:135], v[148:151]\n\t"
  "global_load_dwordx4 a[216:219], v217, s[26:27] offset:1024 sc1\n\t"
  "v_mfma_f32_16x16x32_bf16 v[148:151], v[172:175], a[148:151], v[148:151]\n\t"
  "v_mfma_f32_16x16x32_bf16 v[152:155], v[168:171], a[136:139], v[152:155]\n\t"
  "v_mfma_f32_16x16x32_bf16 v[152:155], v[172:175], a[152:155], v[152:155]\n\t"
  "global_load_dwordx4 a[204:207], v217, s[28:29] offset:0 sc1\n\t"
  "v_mfma_f32_16x16x32_bf16 v[156:159], v[168:171], a[140:143], v[156:159]\n\t"
  "v_mfma_f32_16x16x32_bf16 v[156:159], v[172:175], a[156:159], v[156:159]\n\t"
  "s_waitcnt lgkmcnt(2)\n\t"
  "v_mfma_f32_16x16x32_bf16 v[32:35], v[176:179], a[128:131], v[32:35]\n\t"
  "ds_read_b128 v[168:171], v219 offset:45056\n\t"
  "v_mfma_f32_16x16x32_bf16 v[32:35], v[180:183], a[144:147], v[32:35]\n\t"
  "ds_read_b128 v[172:175], v219 offset:47104\n\t"
  "v_mfma_f32_16x16x32_bf16 v[36:39], v[176:179], a[132:135], v[36:39]\n\t"
  "global_load_dwordx4 a[220:223], v217, s[28:29] offset:1024 sc1\n\t"
  "v_mfma_f32_16x16x32_bf16 v[36:39], v[180:183], a[148:151], v[36:39]\n\t"
  "v_mfma_f32_16x16x32_bf16 v[40:43], v[176:179], a[136:139], v[40:43]\n\t"
  "v_mfma_f32_16x16x32_bf16 v[40:43], v[180:183], a[152:155], v[40:43]\n\t"
  "v_mfma_f32_16x16x32_bf16 v[44:47], v[176:179], a[140:143], v[44:47]\n\t"
  "v_mfma_f32_16x16x32_bf16 v[44:47], v[180:183], a[156:159], v[44:47]\n\t"
  "s_waitcnt lgkmcnt(2)\n\t"
  "v_mfma_f32_16x16x32_bf16 v[48:51], v[160:163], a[128:131], v[48:51]\n\t"
  "ds_read_b128 v[176:179], v219 offset:46080\n\t"
  "v_mfma_f32_16x16x32_bf16 v[48:51], v[164:167], a[144:147], v[48:51]\n\t"
  "ds_read_b128 v[180:183], v219 offset:48128\n\t"
  "v_mfma_f32_16x16x32_bf16 v[52:55], v[160:163], a[132:135], v[52:55]\n\t"
  "v_mfma_f32_16x16x32_bf16 v[52:55], v[164:167], a[148:151], v[52:55]\n\t"
  "v_mfma_f32_16x16x32_bf16 v[56:59], v[160:163], a[136:139], v[56:59]\n\t"
  "v_mfma_f32_16x16x32_bf16 v[56:59], v[164:167], a[152:155], v[56:59]\n\t"
  "v_mfma_f32_16x16x32_bf16 v[60:63], v[160:163], a[140:143], v[60:63]\n\t"
  "v_mfma_f32_16x16x32_bf16 v[60:63], v[164:167], a[156:159], v[60:63]\n\t"
  "s_waitcnt lgkmcnt(2)\n\t"
  "s_barrier\n\t"
  "v_mfma_f32_16x16x32_bf16 v[64:67], v[168:171], a[128:131], v[64:67]\n\t"
  "ds_read_b128 v[160:163], v219 offset:49152\n\t"
  "v_mfma_f32_16x16x32_bf16 v[64:67], v[172:175], a[144:147], v[64:67]\n\t"
  "ds_read_b128 v[164:167], v219 offset:51200\n\t"
  "v_mfma_f32_16x16x32_bf16 v[68:71], v[168:171], a[132:135], v[68:71]\n\t"
  "global_load_dwordx4 a[224:227], v217, s[22:23] offset:2048 sc1\n\t"
  "v_mfma_f32_16x16x32_bf16 v[68:71], v[172:175], a[148:151], v[68:71]\n\t"
  "v_mfma_f32_16x16x32_bf16 v[72:75], v[168:171], a[136:139], v[72:75]\n\t"
  "v_mfma_f32_16x16x32_bf16 v[72:75], v[172:175], a[152:155], v[72:75]\n\t"
  "global_load_dwordx4 a[240:243], v217, s[22:23] offset:3072 sc1\n\t"
  "v_mfma_f32_16x16x32_bf16 v[76:79], v[168:171], a[140:143], v[76:79]\n\t"
  "v_mfma_f32_16x16x32_bf16 v[76:79], v[172:175], a[156:159], v[76:79]\n\t"
  "s_waitcnt lgkmcnt(2)\n\t"
  "v_mfma_f32_16x16x32_bf16 v[80:83], v[176:179], a[128:131], v[80:83]\n\t"
  "ds_read_b128 v[168:171], v219 offset:50176\n\t"
  "v_mfma_f32_16x16x32_bf16 v[80:83], v[180:183], a[144:147], v[80:83]\n\t"
  "ds_read_b128 v[172:175], v219 offset:52224\n\t"
  "v_mfma_f32_16x16x32_bf16 v[84:87], v[176:179], a[132:135], v[84:87]\n\t"
  "global_load_dwordx4 a[228:231], v217, s[24:25] offset:2048 sc1\n\t"
  "v_mfma_f32_16x16x32_bf16 v[84:87], v[180:183], a[148:151], v[84:87]\n\t"
  "v_mfma_f32_16x16x32_bf16 v[88:91], v[176:179], a[136:139], v[88:91]\n\t"
  "v_mfma_f32_16x16x32_bf16 v[88:91], v[180:183], a[152:155], v[88:91]\n\t"
  "global_load_dwordx4 a[244:247], v217, s[24:25] offset:3072 sc1\n\t"
  "v_mfma_f32_16x16x32_bf16 v[92:95], v[176:179], a[140:143], v[92:95]\n\t"
  "v_mfma_f32_16x16x32_bf16 v[92:95], v[180:183], a[156:159], v[92:95]\n\t"
  "s_waitcnt vmcnt(20) lgkmcnt(2)\n\t"
  "v_mfma_f32_16x16x32_bf16 v[128:131], v[160:163], a[160:163], v[128:131]\n\t"
  "ds_read_b128 v[176:179], v219 offset:53248\n\t"
  "v_mfma_f32_16x16x32_bf16 v[128:131], v[164:167], a[176:179], v[128:131]\n\t"
  "ds_read_b128 v[180:183], v219 offset:55296\n\t"
  "s_waitcnt vmcnt(18)\n\t"
  "v_mfma_f32_16x16x32_bf16 v[132:135], v[160:163], a[164:167], v[132:135]\n\t"
  "global_load_dwordx4 a[232:235], v217, s[26:27] offset:2048 sc1\n\t"
  "v_mfma_f32_16x16x32_bf16 v[132:135], v[164:167], a[180:183], v[132:135]\n\t"
  "s_waitcnt vmcnt(17)\n\t"
  "v_mfma_f32_16x16x32_bf16 v[136:139], v[160:163], a[168:171], v[136:139]\n\t"
  "v_mfma_f32_16x16x32_bf16 v[136:139], v[164:167], a[184:187], v[136:139]\n\t"
  "global_load_dwordx4 a[248:251], v217, s[26:27] offset:3072 sc1\n\t"
  "s_waitcnt vmcnt(16)\n\t"
  "v_mfma_f32_16x16x32_bf16 v[140:143], v[160:163], a[172:175], v[140:143]\n\t"
  "v_mfma_f32_16x16x32_bf16 v[140:143], v[164:167], a[188:191], v[140:143]\n\t"
  "s_waitcnt lgkmcnt(2)\n\t"
  "v_mfma_f32_16x16x32_bf16 v[144:147], v[168:171], a[160:163], v[144:147]\n\t"
  "ds_read_b128 v[160:163], v219 offset:54272\n\t"
  "v_mfma_f32_16x16x32_bf16 v[144:147], v[172:175], a[176:179], v[144:147]\n\t"
  "ds_read_b128 v[164:167], v219 offset:56320\n\t"
  "v_mfma_f32_16x16x32_bf16 v[148:151], v[168:171], a[164:167], v[148:151]\n\t"
  "global_load_dwordx4 a[236:239], v217, s[28:29] offset:2048 sc1\n\t"
  "v_mfma_f32_16x16x32_bf16 v[148:151], v[172:175], a[180:183], v[148:151]\n\t"
  "v_mfma_f32_16x16x32_bf16 v[152:155], v[168:171], a[168:171], v[152:155]\n\t"
  "v_mfma_f32_16x16x32_bf16 v[152:155], v[172:175], a[184:187], v[152:155]\n\t"
  "global_load_dwordx4 a[252:255], v217, s[28:29] offset:3072 sc1\n\t"
  "v_mfma_f32_16x16x32_bf16 v[156:159], v[168:171], a[172:175], v[156:159]\n\t"
  "v_mfma_f32_16x16x32_bf16 v[156:159], v[172:175], a[188:191], v[156:159]\n\t"
  "s_waitcnt lgkmcnt(2)\n\t"
  "v_mfma_f32_16x16x32_bf16 v[32:35], v[176:179], a[160:163], v[32:35]\n\t"
  "ds_read_b128 v[168:171], v219 offset:57344\n\t"
  "v_mfma_f32_16x16x32_bf16 v[32:35], v[180:183], a[176:179], v[32:35]\n\t"
  "ds_read_b128 v[172:175], v219 offset:59392\n\t"
  "v_mfma_f32_16x16x32_bf16 v[36:39], v[176:179], a[164:167], v[36:39]\n\t"
  "v_mfma_f32_16x16x32_bf16 v[36:39], v[180:183], a[180:183], v[36:39]\n\t"
  "v_mfma_f32_16x16x32_bf16 v[40:43], v[176:179], a[168:171], v[40:43]\n\t"
  "v_mfma_f32_16x16x32_bf16 v[40:43], v[180:183], a[184:187], v[40:43]\n\t"
  "v_mfma_f32_16x16x32_bf16 v[44:47], v[176:179], a[172:175], v[44:47]\n\t"
  "v_mfma_f32_16x16x32_bf16 v[44:47], v[180:183], a[188:191], v[44:47]\n\t"
  "s_waitcnt lgkmcnt(2)\n\t"
  "v_mfma_f32_16x16x32_bf16 v[48:51], v[160:163], a[160:163], v[48:51]\n\t"
  "ds_read_b128 v[176:179], v219 offset:58368\n\t"
  "v_mfma_f32_16x16x32_bf16 v[48:51], v[164:167], a[176:179], v[48:51]\n\t"
  "ds_read_b128 v[180:183], v219 offset:60416\n\t"
  "v_mfma_f32_16x16x32_bf16 v[52:55], v[160:163], a[164:167], v[52:55]\n\t"
  "v_mfma_f32_16x16x32_bf16 v[52:55], v[164:167], a[180:183], v[52:55]\n\t"
  "v_mfma_f32_16x16x32_bf16 v[56:59], v[160:163], a[168:171], v[56:59]\n\t"
  "v_mfma_f32_16x16x32_bf16 v[56:59], v[164:167], a[184:187], v[56:59]\n\t"
  "v_mfma_f32_16x16x32_bf16 v[60:63], v[160:163], a[172:175], v[60:63]\n\t"
  "v_mfma_f32_16x16x32_bf16 v[60:63], v[164:167], a[188:191], v[60:63]\n\t"
  "s_waitcnt vmcnt(17) lgkmcnt(2)\n\t"
  "s_barrier\n\t"
  "v_mfma_f32_16x16x32_bf16 v[64:67], v[168:171], a[160:163], v[64:67]\n\t"
  "ds_read_b128 v[160:163], v216 offset:0\n\t"
  "v_mfma_f32_16x16x32_bf16 v[64:67], v[172:175], a[176:179], v[64:67]\n\t"
  "ds_read_b128 v[164:167], v216 offset:2048\n\t"
  "v_mfma_f32_16x16x32_bf16 v[68:71], v[168:171], a[164:167], v[68:71]\n\t"
  "v_cmp_gt_u32 vcc, 1, v243\n\t"
  "v_mfma_f32_16x16x32_bf16 v[68:71], v[172:175], a[180:183], v[68:71]\n\t"
  "v_mfma_f32_16x16x32_bf16 v[72:75], v[168:171], a[168:171], v[72:75]\n\t"
  "v_mfma_f32_16x16x32_bf16 v[72:75], v[172:175], a[184:187], v[72:75]\n\t"
  "s_cbranch_vccnz RETRYW_%=\n\t"
  "BACKW_%=:\n\t"
  "v_mfma_f32_16x16x32_bf16 v[76:79], v[168:171], a[172:175], v[76:79]\n\t"
  "global_load_dwordx4 v[0:3], v218, s[22:23] offset:-4096 sc1\n\t"
  "v_mfma_f32_16x16x32_bf16 v[76:79], v[172:175], a[188:191], v[76:79]\n\t"
  "s_waitcnt lgkmcnt(2)\n\t"
  "v_mfma_f32_16x16x32_bf16 v[80:83], v[176:179], a[160:163], v[80:83]\n\t"
  "ds_read_b128 v[168:171], v216 offset:1024\n\t"
  "v_mfma_f32_16x16x32_bf16 v[80:83], v[180:183], a[176:179], v[80:83]\n\t"
  "ds_read_b128 v[172:175], v216 offset:3072\n\t"
  "v_mfma_f32_16x16x32_bf16 v[84:87], v[176:179], a[164:167], v[84:87]\n\t"
  "global_load_dwordx4 v[16:19], v218, s[22:23] offset:-3072 sc1\n\t"
  "v_mfma_f32_16x16x32_bf16 v[84:87], v[180:183], a[180:183], v[84:87]\n\t"
  "v_mfma_f32_16x16x32_bf16 v[88:91], v[176:179], a[168:171], v[88:91]\n\t"
  "global_load_dwordx4 v[4:7], v218, s[24:25] offset:-4096 sc1\n\t"
  "v_mfma_f32_16x16x32_bf16 v[88:91], v[180:183], a[184:187], v[88:91]\n\t"
  "v_mfma_f32_16x16x32_bf16 v[92:95], v[176:179], a[172:175], v[92:95]\n\t"
  "v_mfma_f32_16x16x32_bf16 v[92:95], v[180:183], a[188:191], v[92:95]\n\t"
  "global_load_dwordx4 v[20:23], v218, s[24:25] offset:-3072 sc1\n\t"
  "s_waitcnt vmcnt(19) lgkmcnt(2)\n\t"
  "v_mfma_f32_16x16x32_bf16 v[128:131], v[160:163], a[192:195], v[128:131]\n\t"
  "ds_read_b128 v[176:179], v216 offset:4096\n\t"
  "v_mfma_f32_16x16x32_bf16 v[128:131], v[164:167], a[208:211], v[128:131]\n\t"
  "ds_read_b128 v[180:183], v216 offset:6144\n\t"
  "s_waitcnt vmcnt(16)\n\t"
  "v_mfma_f32_16x16x32_bf16 v[132:135], v[160:163], a[196:199], v[132:135]\n\t"
  "v_mfma_f32_16x16x32_bf16 v[132:135], v[164:167], a[212:215], v[132:135]\n\t"
  "global_load_dwordx4 v[8:11], v218, s[26:27] offset:-4096 sc1\n\t"
  "s_waitcnt vmcnt(15)\n\t"
  "v_mfma_f32_16x16x32_bf16 v[136:139], v[160:163], a[200:203], v[136:139]\n\t"
  "v_mfma_f32_16x16x32_bf16 v[136:139], v[164:167], a[216:219], v[136:139]\n\t"
  "global_load_dwordx4 v[24:27], v218, s[26:27] offset:-3072 sc1\n\t"
  "s_waitcnt vmcnt(14)\n\t"
  "v_mfma_f32_16x16x32_bf16 v[140:143], v[160:163], a[204:207], v[140:143]\n\t"
  "v_mfma_f32_16x16x32_bf16 v[140:143], v[164:167], a[220:223], v[140:143]\n\t"
  "s_waitcnt lgkmcnt(2)\n\t"
  "v_mfma_f32_16x16x32_bf16 v[144:147], v[168:171], a[192:195], v[144:147]\n\t"
  "ds_read_b128 v[160:163], v216 offset:5120\n\t"
  "v_mfma_f32_16x16x32_bf16 v[144:147], v[172:175], a[208:211], v[144:147]\n\t"
  "ds_read_b128 v[164:167], v216 offset:7168\n\t"
  "v_mfma_f32_16x16x32_bf16 v[148:151], v[168:171], a[196:199], v[148:151]\n\t"
  "global_load_dwordx4 v[12:15], v218, s[28:29] offset:-4096 sc1\n\t"
  "v_mfma_f32_16x16x32_bf16 v[148:151], v[172:175], a[212:215], v[148:151]\n\t"
  "v_mfma_f32_16x16x32_bf16 v[152:155], v[168:171], a[200:203], v[152:155]\n\t"
  "global_load_dwordx4 v[28:31], v218, s[28:29] offset:-3072 sc1\n\t"
  "v_mfma_f32_16x16x32_bf16 v[152:155], v[172:175], a[216:219], v[152:155]\n\t"
  "v_mfma_f32_16x16x32_bf16 v[156:159], v[168:171], a[204:207], v[156:159]\n\t"
  "v_mfma_f32_16x16x32_bf16 v[156:159], v[172:175], a[220:223], v[156:159]\n\t"
  "s_add_u32 m0, s46, 90112\n\t"
  "s_waitcnt lgkmcnt(2)\n\t"
  "v_mfma_f32_16x16x32_bf16 v[32:35], v[176:179], a[192:195], v[32:35]\n\t"
  "ds_read_b128 v[168:171], v216 offset:8192\n\t"
  "v_mfma_f32_16x16x32_bf16 v[32:35], v[180:183], a[208:211], v[32:35]\n\t"
  "ds_read_b128 v[172:175], v216 offset:10240\n\t"
  "v_mfma_f32_16x16x32_bf16 v[36:39], v[176:179], a[196:199], v[36:39]\n\t"
  "v_mfma_f32_16x16x32_bf16 v[36:39], v[180:183], a[212:215], v[36:39]\n\t"
  "s_add_u32 s44, s30, 110592\n\t"
  "s_addc_u32 s45, s31, 0\n\t"
  "v_mfma_f32_16x16x32_bf16 v[40:43], v[176:179], a[200:203], v[40:43]\n\t"
  "v_mfma_f32_16x16x32_bf16 v[40:43], v[180:183], a[216:219], v[40:43]\n\t"
  "global_load_lds_dwordx4 v221, s[44:45] offset:0 sc1\n\t"
  "v_mfma_f32_16x16x32_bf16 v[44:47], v[176:179], a[204:207], v[44:47]\n\t"
  "v_mfma_f32_16x16x32_bf16 v[44:47], v[180:183], a[220:223], v[44:47]\n\t"
  "s_waitcnt lgkmcnt(2)\n\t"
  "v_mfma_f32_16x16x32_bf16 v[48:51], v[160:163], a[192:195], v[48:51]\n\t"
  "ds_read_b128 v[176:179], v216 offset:9216\n\t"
  "v_mfma_f32_16x16x32_bf16 v[48:51], v[164:167], a[208:211], v[48:51]\n\t"
  "ds_read_b128 v[180:183], v216 offset:11264\n\t"
  "v_mfma_f32_16x16x32_bf16 v[52:55], v[160:163], a[196:199], v[52:55]\n\t"
  "global_load_lds_dwordx4 v221, s[44:45] offset:1024 sc1\n\t"
  "v_mfma_f32_16x16x32_bf16 v[52:55], v[164:167], a[212:215], v[52:55]\n\t"
  "v_mfma_f32_16x16x32_bf16 v[56:59], v[160:163], a[200:203], v[56:59]\n\t"
  "global_load_lds_dwordx4 v221, s[44:45] offset:2048 sc1\n\t"
  "v_mfma_f32_16x16x32_bf16 v[56:59], v[164:167], a[216:219], v[56:59]\n\t"
  "v_mfma_f32_16x16x32_bf16 v[60:63], v[160:163], a[204:207], v[60:63]\n\t"
  "v_mfma_f32_16x16x32_bf16 v[60:63], v[164:167], a[220:223], v[60:63]\n\t"
  "s_waitcnt lgkmcnt(2)\n\t"
  "s_barrier\n\t"
  "v_mfma_f32_16x16x32_bf16 v[64:67], v[168:171], a[192:195], v[64:67]\n\t"
  "ds_read_b128 v[160:163], v216 offset:12288\n\t"
  "v_mov_b32 v241, v242\n\t"
  "v_mfma_f32_16x16x32_bf16 v[64:67], v[172:175], a[208:211], v[64:67]\n\t"
  "ds_read_b128 v[164:167], v216 offset:14336\n\t"
  "v_mfma_f32_16x16x32_bf16 v[68:71], v[168:171], a[196:199], v[68:71]\n\t"
  "s_add_u32 m0, s46, 102400\n\t"
  "v_mfma_f32_16x16x32_bf16 v[68:71], v[172:175], a[212:215], v[68:71]\n\t"
  "v_mfma_f32_16x16x32_bf16 v[72:75], v[168:171], a[200:203], v[72:75]\n\t"
  "s_add_u32 s44, s30, 122880\n\t"
  "s_addc_u32 s45, s31, 0\n\t"
  "v_mfma_f32_16x16x32_bf16 v[72:75], v[172:175], a[216:219], v[72:75]\n\t"
  "v_mfma_f32_16x16x32_bf16 v[76:79], v[168:171], a[204:207], v[76:79]\n\t"
  "v_mfma_f32_16x16x32_bf16 v[76:79], v[172:175], a[220:223], v[76:79]\n\t"
  "global_load_lds_dwordx4 v221, s[44:45] offset:0 sc1\n\t"
  "s_waitcnt lgkmcnt(2)\n\t"
  "v_mfma_f32_16x16x32_bf16 v[80:83], v[176:179], a[192:195], v[80:83]\n\t"
  "ds_read_b128 v[168:171], v216 offset:13312\n\t"
  "v_mfma_f32_16x16x32_bf16 v[80:83], v[180:183], a[208:211], v[80:83]\n\t"
  "ds_read_b128 v[172:175], v216 offset:15360\n\t"
  "v_mfma_f32_16x16x32_bf16 v[84:87], v[176:179], a[196:199], v[84:87]\n\t"
  "v_mfma_f32_16x16x32_bf16 v[84:87], v[180:183], a[212:215], v[84:87]\n\t"
  "global_load_lds_dwordx4 v221, s[44:45] offset:1024 sc1\n\t"
  "v_mfma_f32_16x16x32_bf16 v[88:91], v[176:179], a[200:203], v[88:91]\n\t"
  "v_mfma_f32_16x16x32_bf16 v[88:91], v[180:183], a[216:219], v[88:91]\n\t"
  "v_mfma_f32_16x16x32_bf16 v[92:95], v[176:179], a[204:207], v[92:95]\n\t"
  "global_load_lds_dwordx4 v221, s[44:45] offset:2048 sc1\n\t"
  "v_mfma_f32_16x16x32_bf16 v[92:95], v[180:183], a[220:223], v[92:95]\n\t"
  "s_waitcnt vmcnt(20) lgkmcnt(2)\n\t"
  "v_mfma_f32_16x16x32_bf16 v[128:131], v[160:163], a[224:227], v[128:131]\n\t"
  "ds_read_b128 v[176:179], v216 offset:16384\n\t"
  "v_mfma_f32_16x16x32_bf16 v[128:131], v[164:167], a[240:243], v[128:131]\n\t"
  "ds_read_b128 v[180:183], v216 offset:18432\n\t"
  "s_waitcnt vmcnt(18)\n\t"
  "v_mfma_f32_16x16x32_bf16 v[132:135], v[160:163], a[228:231], v[132:135]\n\t"
  "s_add_u32 m0, s46, 114688\n\t"
  "v_mfma_f32_16x16x32_bf16 v[132:135], v[164:167], a[244:247], v[132:135]\n\t"
  "s_waitcnt vmcnt(16)\n\t"
  "v_mfma_f32_16x16x32_bf16 v[136:139], v[160:163], a[232:235], v[136:139]\n\t"
  "v_mfma_f32_16x16x32_bf16 v[136:139], v[164:167], a[248:251], v[136:139]\n\t"
  "s_add_u32 s44, s30, 135168\n\t"
  "s_addc_u32 s45, s31, 0\n\t"
  "s_waitcnt vmcnt(14)\n\t"
  "v_mfma_f32_16x16x32_bf16 v[140:143], v[160:163], a[236:239], v[140:143]\n\t"
  "v_mfma_f32_16x16x32_bf16 v[140:143], v[164:167], a[252:255], v[140:143]\n\t"
  "global_load_lds_dwordx4 v221, s[44:45] offset:0 sc1\n\t"
  "s_waitcnt lgkmcnt(2)\n\t"
  "v_mfma_f32_16x16x32_bf16 v[144:147], v[168:171], a[224:227], v[144:147]\n\t"
  "ds_read_b128 v[160:163], v216 offset:17408\n\t"
  "v_mfma_f32_16x16x32_bf16 v[144:147], v[172:175], a[240:243], v[144:147]\n\t"
  "ds_read_b128 v[164:167], v216 offset:19456\n\t"
  "v_mfma_f32_16x16x32_bf16 v[148:151], v[168:171], a[228:231], v[148:151]\n\t"
  "v_mfma_f32_16x16x32_bf16 v[148:151], v[172:175], a[244:247], v[148:151]\n\t"
  "v_mfma_f32_16x16x32_bf16 v[152:155], v[168:171], a[232:235], v[152:155]\n\t"
  "global_load_lds_dwordx4 v221, s[44:45] offset:1024 sc1\n\t"
  "v_mfma_f32_16x16x32_bf16 v[152:155], v[172:175], a[248:251], v[152:155]\n\t"
  "v_mfma_f32_16x16x32_bf16 v[156:159], v[168:171], a[236:239], v[156:159]\n\t"
  "global_load_lds_dwordx4 v221, s[44:45] offset:2048 sc1\n\t"
  "v_mfma_f32_16x16x32_bf16 v[156:159], v[172:175], a[252:255], v[156:159]\n\t"
  "s_waitcnt lgkmcnt(2)\n\t"
  "v_mfma_f32_16x16x32_bf16 v[32:35], v[176:179], a[224:227], v[32:35]\n\t"
  "ds_read_b128 v[168:171], v216 offset:20480\n\t"
  "v_mfma_f32_16x16x32_bf16 v[32:35], v[180:183], a[240:243], v[32:35]\n\t"
  "ds_read_b128 v[172:175], v216 offset:22528\n\t"
  "v_mfma_f32_16x16x32_bf16 v[36:39], v[176:179], a[228:231], v[36:39]\n\t"
  "v_mfma_f32_16x16x32_bf16 v[36:39], v[180:183], a[244:247], v[36:39]\n\t"
  "global_load_dword v200, v223, s[42:43] offset:0 sc1\n\t"
  "v_mfma_f32_16x16x32_bf16 v[40:43], v[176:179], a[232:235], v[40:43]\n\t"
  "v_mfma_f32_16x16x32_bf16 v[40:43], v[180:183], a[248:251], v[40:43]\n\t"
  "global_load_dword v201, v223, s[42:43] offset:64 sc1\n\t"
  "v_mfma_f32_16x16x32_bf16 v[44:47], v[176:179], a[236:239], v[44:47]\n\t"
  "v_mfma_f32_16x16x32_bf16 v[44:47], v[180:183], a[252:255], v[44:47]\n\t"
  "s_waitcnt lgkmcnt(2)\n\t"
  "v_mfma_f32_16x16x32_bf16 v[48:51], v[160:163], a[224:227], v[48:51]\n\t"
  "ds_read_b128 v[176:179], v216 offset:21504\n\t"
  "v_mfma_f32_16x16x32_bf16 v[48:51], v[164:167], a[240:243], v[48:51]\n\t"
  "ds_read_b128 v[180:183], v216 offset:23552\n\t"
  "v_mfma_f32_16x16x32_bf16 v[52:55], v[160:163], a[228:231], v[52:55]\n\t"
  "global_load_dword v202, v223, s[42:43] offset:128 sc1\n\t"
  "v_mfma_f32_16x16x32_bf16 v[52:55], v[164:167], a[244:247], v[52:55]\n\t"
  "v_mfma_f32_16x16x32_bf16 v[56:59], v[160:163], a[232:235], v[56:59]\n\t"
  "global_load_dword v203, v223, s[42:43] offset:192 sc1\n\t"
  "v_mfma_f32_16x16x32_bf16 v[56:59], v[164:167], a[248:251], v[56:59]\n\t"
  "v_mfma_f32_16x16x32_bf16 v[60:63], v[160:163], a[236:239], v[60:63]\n\t"
  "v_mfma_f32_16x16x32_bf16 v[60:63], v[164:167], a[252:255], v[60:63]\n\t"
  "s_waitcnt lgkmcnt(2)\n\t"
  "s_barrier\n\t"
  "v_mfma_f32_16x16x32_bf16 v[64:67], v[168:171], a[224:227], v[64:67]\n\t"
  "ds_read_b128 v[160:163], v216 offset:24576\n\t"
  "v_mfma_f32_16x16x32_bf16 v[64:67], v[172:175], a[240:243], v[64:67]\n\t"
  "ds_read_b128 v[164:167], v216 offset:26624\n\t"
  "v_mfma_f32_16x16x32_bf16 v[68:71], v[168:171], a[228:231], v[68:71]\n\t"
  "s_add_u32 m0, s46, 126976\n\t"
  "v_mfma_f32_16x16x32_bf16 v[68:71], v[172:175], a[244:247], v[68:71]\n\t"
  "v_mfma_f32_16x16x32_bf16 v[72:75], v[168:171], a[232:235], v[72:75]\n\t"
  "v_mfma_f32_16x16x32_bf16 v[72:75], v[172:175], a[248:251], v[72:75]\n\t"
  "s_add_u32 s44, s30, 147456\n\t"
  "s_addc_u32 s45, s31, 0\n\t"
  "v_mfma_f32_16x16x32_bf16 v[76:79], v[168:171], a[236:239], v[76:79]\n\t"
  "v_mfma_f32_16x16x32_bf16 v[76:79], v[172:175], a[252:255], v[76:79]\n\t"
  "s_waitcnt lgkmcnt(2)\n\t"
  "v_mfma_f32_16x16x32_bf16 v[80:83], v[176:179], a[224:227], v[80:83]\n\t"
  "ds_read_b128 v[168:171], v216 offset:25600\n\t"
  "v_mfma_f32_16x16x32_bf16 v[80:83], v[180:183], a[240:243], v[80:83]\n\t"
  "ds_read_b128 v[172:175], v216 offset:27648\n\t"
  "v_mfma_f32_16x16x32_bf16 v[84:87], v[176:179], a[228:231], v[84:87]\n\t"
  "global_load_lds_dwordx4 v221, s[44:45] offset:0 sc1\n\t"
  "v_mfma_f32_16x16x32_bf16 v[84:87], v[180:183], a[244:247], v[84:87]\n\t"
  "v_mfma_f32_16x16x32_bf16 v[88:91], v[176:179], a[232:235], v[88:91]\n\t"
  "v_mfma_f32_16x16x32_bf16 v[88:91], v[180:183], a[248:251], v[88:91]\n\t"
  "global_load_lds_dwordx4 v221, s[44:45] offset:1024 sc1\n\t"
  "v_mfma_f32_16x16x32_bf16 v[92:95], v[176:179], a[236:239], v[92:95]\n\t"
  "v_mfma_f32_16x16x32_bf16 v[92:95], v[180:183], a[252:255], v[92:95]\n\t"
  "s_waitcnt vmcnt(21) lgkmcnt(2)\n\t"
  "v_mfma_f32_16x16x32_bf16 v[128:131], v[160:163], v[0:3], v[128:131]\n\t"
  "ds_read_b128 v[176:179], v216 offset:28672\n\t"
  "v_mfma_f32_16x16x32_bf16 v[128:131], v[164:167], v[16:19], v[128:131]\n\t"
  "ds_read_b128 v[180:183], v216 offset:30720\n\t"
  "s_waitcnt vmcnt(19)\n\t"
  "v_mfma_f32_16x16x32_bf16 v[132:135], v[160:163], v[4:7], v[132:135]\n\t"
  "global_load_lds_dwordx4 v221, s[44:45] offset:2048 sc1\n\t"
  "v_mfma_f32_16x16x32_bf16 v[132:135], v[164:167], v[20:23], v[132:135]\n\t"
  "s_waitcnt vmcnt(18)\n\t"
  "v_mfma_f32_16x16x32_bf16 v[136:139], v[160:163], v[8:11], v[136:139]\n\t"
  "v_mfma_f32_16x16x32_bf16 v[136:139], v[164:167], v[24:27], v[136:139]\n\t"
  "s_waitcnt vmcnt(16)\n\t"
  "v_mfma_f32_16x16x32_bf16 v[140:143], v[160:163], v[12:15], v[140:143]\n\t"
  "v_mfma_f32_16x16x32_bf16 v[140:143], v[164:167], v[28:31], v[140:143]\n\t"
  "s_waitcnt lgkmcnt(2)\n\t"
  "v_mfma_f32_16x16x32_bf16 v[144:147], v[168:171], v[0:3], v[144:147]\n\t"
  "ds_read_b128 v[160:163], v216 offset:29696\n\t"
  "v_mfma_f32_16x16x32_bf16 v[144:147], v[172:175], v[16:19], v[144:147]\n\t"
  "ds_read_b128 v[164:167], v216 offset:31744\n\t"
  "v_mfma_f32_16x16x32_bf16 v[148:151], v[168:171], v[4:7], v[148:151]\n\t"
  "v_mfma_f32_16x16x32_bf16 v[148:151], v[172:175], v[20:23], v[148:151]\n\t"
  "v_mfma_f32_16x16x32_bf16 v[152:155], v[168:171], v[8:11], v[152:155]\n\t"
  "v_mfma_f32_16x16x32_bf16 v[152:155], v[172:175], v[24:27], v[152:155]\n\t"
  "v_mfma_f32_16x16x32_bf16 v[156:159], v[168:171], v[12:15], v[156:159]\n\t"
  "v_mfma_f32_16x16x32_bf16 v[156:159], v[172:175], v[28:31], v[156:159]\n\t"
  "s_waitcnt lgkmcnt(2)\n\t"
  "v_mfma_f32_16x16x32_bf16 v[32:35], v[176:179], v[0:3], v[32:35]\n\t"
  "ds_read_b128 v[168:171], v216 offset:32768\n\t"
  "v_mfma_f32_16x16x32_bf16 v[32:35], v[180:183], v[16:19], v[32:35]\n\t"
  "ds_read_b128 v[172:175], v216 offset:34816\n\t"
  "v_mfma_f32_16x16x32_bf16 v[36:39], v[176:179], v[4:7], v[36:39]\n\t"
  "v_mfma_f32_16x16x32_bf16 v[36:39], v[180:183], v[20:23], v[36:39]\n\t"
  "v_mfma_f32_16x16x32_bf16 v[40:43], v[176:179], v[8:11], v[40:43]\n\t"
  "v_mfma_f32_16x16x32_bf16 v[40:43], v[180:183], v[24:27], v[40:43]\n\t"
  "v_mfma_f32_16x16x32_bf16 v[44:47], v[176:179], v[12:15], v[44:47]\n\t"
  "v_mfma_f32_16x16x32_bf16 v[44:47], v[180:183], v[28:31], v[44:47]\n\t"
  "s_waitcnt lgkmcnt(2)\n\t"
  "v_mfma_f32_16x16x32_bf16 v[48:51], v[160:163], v[0:3], v[48:51]\n\t"
  "ds_read_b128 v[176:179], v216 offset:33792\n\t"
  "v_mfma_f32_16x16x32_bf16 v[48:51], v[164:167], v[16:19], v[48:51]\n\t"
  "ds_read_b128 v[180:183], v216 offset:35840\n\t"
  "v_mfma_f32_16x16x32_bf16 v[52:55], v[160:163], v[4:7], v[52:55]\n\t"
  "v_mfma_f32_16x16x32_bf16 v[52:55], v[164:167], v[20:23], v[52:55]\n\t"
  "v_mfma_f32_16x16x32_bf16 v[56:59], v[160:163], v[8:11], v[56:59]\n\t"
  "v_mfma_f32_16x16x32_bf16 v[56:59], v[164:167], v[24:27], v[56:59]\n\t"
  "v_mfma_f32_16x16x32_bf16 v[60:63], v[160:163], v[12:15], v[60:63]\n\t"
  "v_mfma_f32_16x16x32_bf16 v[60:63], v[164:167], v[28:31], v[60:63]\n\t"
  "s_waitcnt vmcnt(3) lgkmcnt(2)\n\t"
  "s_barrier\n\t"
  "v_mfma_f32_16x16x32_bf16 v[64:67], v[168:171], v[0:3], v[64:67]\n\t"
  "ds_read_b128 v[160:163], v219 offset:24576\n\t"
  "v_mfma_f32_16x16x32_bf16 v[64:67], v[172:175], v[16:19], v[64:67]\n\t"
  "ds_read_b128 v[164:167], v219 offset:26624\n\t"
  "v_mfma_f32_16x16x32_bf16 v[68:71], v[168:171], v[4:7], v[68:71]\n\t"
  "s_add_u32 m0, s46, 139264\n\t"
  "v_min_u32 v208, v200, v201\n\t"
  "v_mfma_f32_16x16x32_bf16 v[68:71], v[172:175], v[20:23], v[68:71]\n\t"
  "v_min3_u32 v208, v208, v202, v203\n\t"
  "v_mfma_f32_16x16x32_bf16 v[72:75], v[168:171], v[8:11], v[72:75]\n\t"
  "v_cmp_eq_u32 vcc, 0, v208\n\t"
  "v_mfma_f32_16x16x32_bf16 v[72:75], v[172:175], v[24:27], v[72:75]\n\t"
  "s_add_u32 s44, s30, 159744\n\t"
  "s_addc_u32 s45, s31, 0\n\t"
  "v_mfma_f32_16x16x32_bf16 v[76:79], v[168:171], v[12:15], v[76:79]\n\t"
  "v_mfma_f32_16x16x32_bf16 v[76:79], v[172:175], v[28:31], v[76:79]\n\t"
  "s_cbranch_vccnz RETRYG_%=\n\t"
  "BACKG_%=:\n\t"
  "s_waitcnt lgkmcnt(2)\n\t"
  "v_mfma_f32_16x16x32_bf16 v[80:83], v[176:179], v[0:3], v[80:83]\n\t"
  "ds_read_b128 v[168:171], v219 offset:25600\n\t"
  "v_mul_f32 v204, v200, v200\n\t"
  "v_mul_f32 v212, 0x3dd2d3e8, v200\n\t"
  "v_mfma_f32_16x16x32_bf16 v[80:83], v[180:183], v[16:19], v[80:83]\n\t"
  "ds_read_b128 v[172:175], v219 offset:27648\n\t"
  "PG1_%=:\n\t"
  "s_cbranch_execz PG5_%=\n\t"
  "v_mul_f32 v205, v201, v201\n\t"
  "v_mul_f32 v213, 0x3dd2d3e8, v201\n\t"
  "v_mfma_f32_16x16x32_bf16 v[84:87], v[176:179], v[4:7], v[84:87]\n\t"
  "global_load_lds_dwordx4 v221, s[44:45] offset:0 sc1\n\t"
  "v_mul_f32 v206, v202, v202\n\t"
  "v_mfma_f32_16x16x32_bf16 v[84:87], v[180:183], v[20:23], v[84:87]\n\t"
  "v_mul_f32 v214, 0x3dd2d3e8, v202\n\t"
  "v_mul_f32 v207, v203, v203\n\t"
  "v_mfma_f32_16x16x32_bf16 v[88:91], v[176:179], v[8:11], v[88:91]\n\t"
  "v_mul_f32 v215, 0x3dd2d3e8, v203\n\t"
  "v_mul_f32 v204, v204, v212\n\t"
  "v_mul_f32 v200, 0x40135761, v200\n\t"
  "v_mfma_f32_16x16x32_bf16 v[88:91], v[180:183], v[24:27], v[88:91]\n\t"
  "global_load_lds_dwordx4 v221, s[44:45] offset:1024 sc1\n\t"
  "v_mul_f32 v205, v205, v213\n\t"
  "v_mfma_f32_16x16x32_bf16 v[92:95], v[176:179], v[12:15], v[92:95]\n\t"
  "v_mul_f32 v201, 0x40135761, v201\n\t"
  "v_mul_f32 v206, v206, v214\n\t"
  "v_mfma_f32_16x16x32_bf16 v[92:95], v[180:183], v[28:31], v[92:95]\n\t"
  "v_mul_f32 v202, 0x40135761, v202\n\t"
  "s_waitcnt lgkmcnt(2)\n\t"
  "v_mfma_f32_16x16x32_bf16 v[96:99], v[160:163], a[0:3], 0\n\t"
  "ds_read_b128 v[176:179], v219 offset:28672\n\t"
  "v_mul_f32 v207, v207, v215\n\t"
  "v_mul_f32 v203, 0x40135761, v203\n\t"
  "v_mfma_f32_16x16x32_bf16 v[96:99], v[164:167], a[16:19], v[96:99]\n\t"
  "ds_read_b128 v[180:183], v219 offset:30720\n\t"
  "v_mfma_f32_16x16x32_bf16 v[100:103], v[160:163], a[4:7], 0\n\t"
  "global_load_lds_dwordx4 v221, s[44:45] offset:2048 sc1\n\t"
  "v_mfma_f32_16x16x32_bf16 v[100:103], v[164:167], a[20:23], v[100:103]\n\t"
  "v_mul_f32 v208, v128, v128\n\t"
  "v_mul_f32 v209, v129, v129\n\t"
  "v_mul_f32 v210, v130, v130\n\t"
  "v_mfma_f32_16x16x32_bf16 v[104:107], v[160:163], a[8:11], 0\n\t"
  "v_mul_f32 v211, v131, v131\n\t"
  "v_fma_f32 v208, v208, v204, v200\n\t"
  "v_mfma_f32_16x16x32_bf16 v[104:107], v[164:167], a[24:27], v[104:107]\n\t"
  "v_fma_f32 v209, v209, v204, v200\n\t"
  "v_fma_f32 v210, v210, v204, v200\n\t"
  "v_mfma_f32_16x16x32_bf16 v[108:111], v[160:163], a[12:15], 0\n\t"
  "v_fma_f32 v211, v211, v204, v200\n\t"
  "v_mul_f32 v208, v128, v208\n\t"
  "v_mfma_f32_16x16x32_bf16 v[108:111], v[164:167], a[28:31], v[108:111]\n\t"
  "v_mul_f32 v209, v129, v209\n\t"
  "v_mul_f32 v210, v130, v210\n\t"
  "s_waitcnt lgkmcnt(2)\n\t"
  "v_mfma_f32_16x16x32_bf16 v[112:115], v[168:171], a[0:3], 0\n\t"
  "ds_read_b128 v[160:163], v219 offset:29696\n\t"
  "v_mul_f32 v211, v131, v211\n\t"
  "v_mfma_f32_16x16x32_bf16 v[112:115], v[172:175], a[16:19], v[112:115]\n\t"
  "ds_read_b128 v[164:167], v219 offset:31744\n\t"
  "v_mul_f32 v144, v128, v144\n\t"
  "v_mfma_f32_16x16x32_bf16 v[116:119], v[168:171], a[4:7], 0\n\t"
  "v_exp_f32 v208, v208\n\t"
  "v_mul_f32 v145, v129, v145\n\t"
  "v_mfma_f32_16x16x32_bf16 v[116:119], v[172:175], a[20:23], v[116:119]\n\t"
  "v_exp_f32 v209, v209\n\t"
  "v_mfma_f32_16x16x32_bf16 v[120:123], v[168:171], a[8:11], 0\n\t"
  "v_exp_f32 v210, v210\n\t"
  "v_mfma_f32_16x16x32_bf16 v[120:123], v[172:175], a[24:27], v[120:123]\n\t"
  "v_exp_f32 v211, v211\n\t"
  "v_mfma_f32_16x16x32_bf16 v[124:127], v[168:171], a[12:15], 0\n\t"
  "v_mul_f32 v146, v130, v146\n\t"
  "v_mul_f32 v147, v131, v147\n\t"
  "v_add_f32 v208, 1.0, v208\n\t"
  "v_mfma_f32_16x16x32_bf16 v[124:127], v[172:175], a[28:31], v[124:127]\n\t"
  "v_add_f32 v209, 1.0, v209\n\t"
  "s_waitcnt lgkmcnt(2)\n\t"
  "v_mfma_f32_16x16x32_bf16 v[96:99], v[176:179], a[32:35], v[96:99]\n\t"
  "ds_read_b128 v[168:171], v219 offset:32768\n\t"
  "v_add_f32 v210, 1.0, v210\n\t"
  "v_mfma_f32_16x16x32_bf16 v[96:99], v[180:183], a[48:51], v[96:99]\n\t"
  "ds_read_b128 v[172:175], v219 offset:34816\n\t"
  "v_add_f32 v211, 1.0, v211\n\t"
  "v_mul_f32 v212, v132, v132\n\t"
  "v_mfma_f32_16x16x32_bf16 v[100:103], v[176:179], a[36:39], v[100:103]\n\t"
  "v_rcp_f32 v208, v208\n\t"
  "v_mfma_f32_16x16x32_bf16 v[100:103], v[180:183], a[52:55], v[100:103]\n\t"
  "v_rcp_f32 v209, v209\n\t"
  "v_mfma_f32_16x16x32_bf16 v[104:107], v[176:179], a[40:43], v[104:107]\n\t"
  "v_rcp_f32 v210, v210\n\t"
  "v_mfma_f32_16x16x32_bf16 v[104:107], v[180:183], a[56:59], v[104:107]\n\t"
  "v_rcp_f32 v211, v211\n\t"
  "v_mul_f32 v144, v144, v208\n\t"
  "v_mfma_f32_16x16x32_bf16 v[108:111], v[176:179], a[44:47], v[108:111]\n\t"
  "v_mul_f32 v145, v145, v209\n\t"
  "v_mul_f32 v146, v146, v210\n\t"
  "v_mfma_f32_16x16x32_bf16 v[108:111], v[180:183], a[60:63], v[108:111]\n\t"
  "v_mul_f32 v147, v147, v211\n\t"
  "s_waitcnt lgkmcnt(2)\n\t"
  "v_mfma_f32_16x16x32_bf16 v[112:115], v[160:163], a[32:35], v[112:115]\n\t"
  "ds_read_b128 v[176:179], v219 offset:33792\n\t"
  "v_cvt_pk_bf16_f32 v184, v144, v145\n\t"
  "v_cvt_pk_bf16_f32 v185, v146, v147\n\t"
  "v_mfma_f32_16x16x32_bf16 v[112:115], v[164:167], a[48:51], v[112:115]\n\t"
  "ds_read_b128 v[180:183], v219 offset:35840\n\t"
  "v_mul_f32 v213, v133, v133\n\t"
  "v_mfma_f32_16x16x32_bf16 v[116:119], v[160:163], a[36:39], v[116:119]\n\t"
  "v_mul_f32 v214, v134, v134\n\t"
  "v_mul_f32 v215, v135, v135\n\t"
  "v_mfma_f32_16x16x32_bf16 v[116:119], v[164:167], a[52:55], v[116:119]\n\t"
  "v_fma_f32 v212, v212, v205, v201\n\t"
  "v_fma_f32 v213, v213, v205, v201\n\t"
  "v_mfma_f32_16x16x32_bf16 v[120:123], v[160:163], a[40:43], v[120:123]\n\t"
  "v_fma_f32 v214, v214, v205, v201\n\t"
  "v_fma_f32 v215, v215, v205, v201\n\t"
  "v_mul_f32 v212, v132, v212\n\t"
  "v_mfma_f32_16x16x32_bf16 v[120:123], v[164:167], a[56:59], v[120:123]\n\t"
  "v_exp_f32 v212, v212\n\t"
  "v_mfma_f32_16x16x32_bf16 v[124:127], v[160:163], a[44:47], v[124:127]\n\t"
  "v_mul_f32 v213, v133, v213\n\t"
  "v_mul_f32 v214, v134, v214\n\t"
  "v_mfma_f32_16x16x32_bf16 v[124:127], v[164:167], a[60:63], v[124:127]\n\t"
  "v_mul_f32 v215, v135, v215\n\t"
  "s_waitcnt lgkmcnt(2)\n\t"
  "s_barrier\n\t"
  "v_mfma_f32_16x16x32_bf16 v[96:99], v[168:171], a[64:67], v[96:99]\n\t"
  "ds_read_b128 v[160:163], v219 offset:36864\n\t"
  "v_mul_f32 v148, v132, v148\n\t"
  "v_mfma_f32_16x16x32_bf16 v[96:99], v[172:175], a[80:83], v[96:99]\n\t"
  "ds_read_b128 v[164:167], v219 offset:38912\n\t"
  "v_mul_f32 v149, v133, v149\n\t"
  "v_mfma_f32_16x16x32_bf16 v[100:103], v[168:171], a[68:71], v[100:103]\n\t"
  "s_add_u32 m0, s46, 151552\n\t"
  "v_mul_f32 v150, v134, v150\n\t"
  "v_mfma_f32_16x16x32_bf16 v[100:103], v[172:175], a[84:87], v[100:103]\n\t"
  "v_exp_f32 v213, v213\n\t"
  "v_mul_f32 v151, v135, v151\n\t"
  "v_mfma_f32_16x16x32_bf16 v[104:107], v[168:171], a[72:75], v[104:107]\n\t"
  "v_exp_f32 v214, v214\n\t"
  "v_mfma_f32_16x16x32_bf16 v[104:107], v[172:175], a[88:91], v[104:107]\n\t"
  "s_add_u32 s44, s30, 172032\n\t"
  "s_addc_u32 s45, s31, 0\n\t"
  "v_mfma_f32_16x16x32_bf16 v[108:111], v[168:171], a[76:79], v[108:111]\n\t"
  "v_exp_f32 v215, v215\n\t"
  "v_mfma_f32_16x16x32_bf16 v[108:111], v[172:175], a[92:95], v[108:111]\n\t"
  "v_add_f32 v212, 1.0, v212\n\t"
  "v_add_f32 v213, 1.0, v213\n\t"
  "s_waitcnt lgkmcnt(2)\n\t"
  "v_mfma_f32_16x16x32_bf16 v[112:115], v[176:179], a[64:67], v[112:115]\n\t"
  "ds_read_b128 v[168:171], v219 offset:37888\n\t"
  "v_add_f32 v214, 1.0, v214\n\t"
  "v_mfma_f32_16x16x32_bf16 v[112:115], v[180:183], a[80:83], v[112:115]\n\t"
  "ds_read_b128 v[172:175], v219 offset:39936\n\t"
  "v_add_f32 v215, 1.0, v215\n\t"
  "v_mfma_f32_16x16x32_bf16 v[116:119], v[176:179], a[68:71], v[116:119]\n\t"
  "global_load_lds_dwordx4 v221, s[44:45] offset:0 sc1\n\t"
  "v_mul_f32 v208, v136, v136\n\t"
  "v_mul_f32 v209, v137, v137\n\t"
  "v_mfma_f32_16x16x32_bf16 v[116:119], v[180:183], a[84:87], v[116:119]\n\t"
  "v_rcp_f32 v212, v212\n\t"
  "v_mfma_f32_16x16x32_bf16 v[120:123], v[176:179], a[72:75], v[120:123]\n\t"
  "v_rcp_f32 v213, v213\n\t"
  "v_mfma_f32_16x16x32_bf16 v[120:123], v[180:183], a[88:91], v[120:123]\n\t"
  "global_load_lds_dwordx4 v221, s[44:45] offset:1024 sc1\n\t"
  "v_mul_f32 v148, v148, v212\n\t"
  "v_mfma_f32_16x16x32_bf16 v[124:127], v[176:179], a[76:79], v[124:127]\n\t"
  "v_rcp_f32 v214, v214\n\t"
  "v_mul_f32 v149, v149, v213\n\t"
  "v_mfma_f32_16x16x32_bf16 v[124:127], v[180:183], a[92:95], v[124:127]\n\t"
  "v_mul_f32 v150, v150, v214\n\t"
  "s_waitcnt lgkmcnt(2)\n\t"
  "v_mfma_f32_16x16x32_bf16 v[96:99], v[160:163], a[96:99], v[96:99]\n\t"
  "ds_read_b128 v[176:179], v219 offset:40960\n\t"
  "v_cvt_pk_bf16_f32 v188, v148, v149\n\t"
  "v_mfma_f32_16x16x32_bf16 v[96:99], v[164:167], a[112:115], v[96:99]\n\t"
  "ds_read_b128 v[180:183], v219 offset:43008\n\t"
  "v_mul_f32 v210, v138, v138\n\t"
  "v_mul_f32 v211, v139, v139\n\t"
  "v_mfma_f32_16x16x32_bf16 v[100:103], v[160:163], a[100:103], v[100:103]\n\t"
  "global_load_lds_dwordx4 v221, s[44:45] offset:2048 sc1\n\t"
  "v_fma_f32 v208, v208, v206, v202\n\t"
  "v_mfma_f32_16x16x32_bf16 v[100:103], v[164:167], a[116:119], v[100:103]\n\t"
  "v_rcp_f32 v215, v215\n\t"
  "v_mfma_f32_16x16x32_bf16 v[104:107], v[160:163], a[104:107], v[104:107]\n\t"
  "v_mul_f32 v151, v151, v215\n\t"
  "v_cvt_pk_bf16_f32 v189, v150, v151\n\t"
  "v_mfma_f32_16x16x32_bf16 v[104:107], v[164:167], a[120:123], v[104:107]\n\t"
  "global_store_dwordx4 v245, v[252:255], s[56:57] offset:0 sc1\n\t"
  "v_fma_f32 v209, v209, v206, v202\n\t"
  "v_fma_f32 v210, v210, v206, v202\n\t"
  "v_mfma_f32_16x16x32_bf16 v[108:111], v[160:163], a[108:111], v[108:111]\n\t"
  "v_fma_f32 v211, v211, v206, v202\n\t"
  "v_mul_f32 v208, v136, v208\n\t"
  "v_mfma_f32_16x16x32_bf16 v[108:111], v[164:167], a[124:127], v[108:111]\n\t"
  "v_mul_f32 v209, v137, v209\n\t"
  "s_waitcnt lgkmcnt(2)\n\t"
  "v_mfma_f32_16x16x32_bf16 v[112:115], v[168:171], a[96:99], v[112:115]\n\t"
  "ds_read_b128 v[160:163], v219 offset:41984\n\t"
  "v_mul_f32 v210, v138, v210\n\t"
  "v_mul_f32 v211, v139, v211\n\t"
  "v_mfma_f32_16x16x32_bf16 v[112:115], v[172:175], a[112:115], v[112:115]\n\t"
  "ds_read_b128 v[164:167], v219 offset:44032\n\t"
  "v_mul_f32 v152, v136, v152\n\t"
  "v_mfma_f32_16x16x32_bf16 v[116:119], v[168:171], a[100:103], v[116:119]\n\t"
  "global_store_dwordx4 v245, v[252:255], s[56:57] offset:1024 sc1\n\t"
  "v_mul_f32 v153, v137, v153\n\t"
  "v_mfma_f32_16x16x32_bf16 v[116:119], v[172:175], a[116:119], v[116:119]\n\t"
  "v_exp_f32 v208, v208\n\t"
  "v_mfma_f32_16x16x32_bf16 v[120:123], v[168:171], a[104:107], v[120:123]\n\t"
  "v_exp_f32 v209, v209\n\t"
  "v_mul_f32 v154, v138, v154\n\t"
  "v_mfma_f32_16x16x32_bf16 v[120:123], v[172:175], a[120:123], v[120:123]\n\t"
  "v_exp_f32 v210, v210\n\t"
  "v_mfma_f32_16x16x32_bf16 v[124:127], v[168:171], a[108:111], v[124:127]\n\t"
  "v_exp_f32 v211, v211\n\t"
  "v_mfma_f32_16x16x32_bf16 v[124:127], v[172:175], a[124:127], v[124:127]\n\t"
  "v_mul_f32 v155, v139, v155\n\t"
  "v_add_f32 v208, 1.0, v208\n\t"
  "s_waitcnt lgkmcnt(2)\n\t"
  "v_mfma_f32_16x16x32_bf16 v[96:99], v[176:179], a[128:131], v[96:99]\n\t"
  "ds_read_b128 v[168:171], v219 offset:45056\n\t"
  "v_add_f32 v209, 1.0, v209\n\t"
  "v_mfma_f32_16x16x32_bf16 v[96:99], v[180:183], a[144:147], v[96:99]\n\t"
  "ds_read_b128 v[172:175], v219 offset:47104\n\t"
  "v_add_f32 v210, 1.0, v210\n\t"
  "v_mfma_f32_16x16x32_bf16 v[100:103], v[176:179], a[132:135], v[100:103]\n\t"
  "v_rcp_f32 v208, v208\n\t"
  "v_mfma_f32_16x16x32_bf16 v[100:103], v[180:183], a[148:151], v[100:103]\n\t"
  "v_rcp_f32 v209, v209\n\t"
  "v_add_f32 v211, 1.0, v211\n\t"
  "v_mfma_f32_16x16x32_bf16 v[104:107], v[176:179], a[136:139], v[104:107]\n\t"
  "v_rcp_f32 v210, v210\n\t"
  "v_mfma_f32_16x16x32_bf16 v[104:107], v[180:183], a[152:155], v[104:107]\n\t"
  "v_rcp_f32 v211, v211\n\t"
  "v_mfma_f32_16x16x32_bf16 v[108:111], v[176:179], a[140:143], v[108:111]\n\t"
  "v_mul_f32 v152, v152, v208\n\t"
  "v_mul_f32 v153, v153, v209\n\t"
  "v_mfma_f32_16x16x32_bf16 v[108:111], v[180:183], a[156:159], v[108:111]\n\t"
  "v_mul_f32 v154, v154, v210\n\t"
  "v_mul_f32 v155, v155, v211\n\t"
  "s_waitcnt lgkmcnt(2)\n\t"
  "v_mfma_f32_16x16x32_bf16 v[112:115], v[160:163], a[128:131], v[112:115]\n\t"
  "ds_read_b128 v[176:179], v219 offset:46080\n\t"
  "v_cvt_pk_bf16_f32 v192, v152, v153\n\t"
  "v_mfma_f32_16x16x32_bf16 v[112:115], v[164:167], a[144:147], v[112:115]\n\t"
  "ds_read_b128 v[180:183], v219 offset:48128\n\t"
  "v_cvt_pk_bf16_f32 v193, v154, v155\n\t"
  "v_mfma_f32_16x16x32_bf16 v[116:119], v[160:163], a[132:135], v[116:119]\n\t"
  "v_mul_f32 v212, v140, v140\n\t"
  "v_mul_f32 v213, v141, v141\n\t"
  "v_mul_f32 v214, v142, v142\n\t"
  "v_mfma_f32_16x16x32_bf16 v[116:119], v[164:167], a[148:151], v[116:119]\n\t"
  "v_mul_f32 v215, v143, v143\n\t"
  "v_fma_f32 v212, v212, v207, v203\n\t"
  "v_mfma_f32_16x16x32_bf16 v[120:123], v[160:163], a[136:139], v[120:123]\n\t"
  "v_fma_f32 v213, v213, v207, v203\n\t"
  "v_fma_f32 v214, v214, v207, v203\n\t"
  "v_mfma_f32_16x16x32_bf16 v[120:123], v[164:167], a[152:155], v[120:123]\n\t"
  "v_fma_f32 v215, v215, v207, v203\n\t"
  "v_mul_f32 v212, v140, v212\n\t"
  "v_mfma_f32_16x16x32_bf16 v[124:127], v[160:163], a[140:143], v[124:127]\n\t"
  "v_exp_f32 v212, v212\n\t"
  "v_mul_f32 v213, v141, v213\n\t"
  "v_mfma_f32_16x16x32_bf16 v[124:127], v[164:167], a[156:159], v[124:127]\n\t"
  "s_waitcnt lgkmcnt(2)\n\t"
  "s_barrier\n\t"
  "v_mfma_f32_16x16x32_bf16 v[96:99], v[168:171], a[160:163], v[96:99]\n\t"
  "ds_read_b128 v[160:163], v219 offset:49152\n\t"
  "v_mul_f32 v214, v142, v214\n\t"
  "v_mfma_f32_16x16x32_bf16 v[96:99], v[172:175], a[176:179], v[96:99]\n\t"
  "ds_read_b128 v[164:167], v219 offset:51200\n\t"
  "v_mul_f32 v215, v143, v215\n\t"
  "v_mul_f32 v156, v140, v156\n\t"
  "v_mfma_f32_16x16x32_bf16 v[100:103], v[168:171], a[164:167], v[100:103]\n\t"
  "s_add_u32 m0, s46, 90112\n\t"
  "v_mul_f32 v157, v141, v157\n\t"
  "v_mfma_f32_16x16x32_bf16 v[100:103], v[172:175], a[180:183], v[100:103]\n\t"
  "v_exp_f32 v213, v213\n\t"
  "v_mfma_f32_16x16x32_bf16 v[104:107], v[168:171], a[168:171], v[104:107]\n\t"
  "v_exp_f32 v214, v214\n\t"
  "v_mfma_f32_16x16x32_bf16 v[104:107], v[172:175], a[184:187], v[104:107]\n\t"
  "s_add_u32 s44, s30, 184320\n\t"
  "s_addc_u32 s45, s31, 0\n\t"
  "v_mul_f32 v158, v142, v158\n\t"
  "v_mfma_f32_16x16x32_bf16 v[108:111], v[168:171], a[172:175], v[108:111]\n\t"
  "v_exp_f32 v215, v215\n\t"
  "v_mfma_f32_16x16x32_bf16 v[108:111], v[172:175], a[188:191], v[108:111]\n\t"
  "v_mul_f32 v159, v143, v159\n\t"
  "s_waitcnt lgkmcnt(2)\n\t"
  "v_mfma_f32_16x16x32_bf16 v[112:115], v[176:179], a[160:163], v[112:115]\n\t"
  "ds_read_b128 v[168:171], v219 offset:50176\n\t"
  "v_add_f32 v212, 1.0, v212\n\t"
  "v_mfma_f32_16x16x32_bf16 v[112:115], v[180:183], a[176:179], v[112:115]\n\t"
  "ds_read_b128 v[172:175], v219 offset:52224\n\t"
  "v_add_f32 v213, 1.0, v213\n\t"
  "v_add_f32 v214, 1.0, v214\n\t"
  "v_mfma_f32_16x16x32_bf16 v[116:119], v[176:179], a[164:167], v[116:119]\n\t"
  "global_load_lds_dwordx4 v221, s[44:45] offset:0 sc1\n\t"
  "v_add_f32 v215, 1.0, v215\n\t"
  "v_mfma_f32_16x16x32_bf16 v[116:119], v[180:183], a[180:183], v[116:119]\n\t"
  "v_rcp_f32 v212, v212\n\t"
  "v_mfma_f32_16x16x32_bf16 v[120:123], v[176:179], a[168:171], v[120:123]\n\t"
  "v_rcp_f32 v213, v213\n\t"
  "v_mul_f32 v156, v156, v212\n\t"
  "v_mfma_f32_16x16x32_bf16 v[120:123], v[180:183], a[184:187], v[120:123]\n\t"
  "global_load_lds_dwordx4 v221, s[44:45] offset:1024 sc1\n\t"
  "v_mul_f32 v157, v157, v213\n\t"
  "v_mfma_f32_16x16x32_bf16 v[124:127], v[176:179], a[172:175], v[124:127]\n\t"
  "v_rcp_f32 v214, v214\n\t"
  "v_mfma_f32_16x16x32_bf16 v[124:127], v[180:183], a[188:191], v[124:127]\n\t"
  "v_mul_f32 v158, v158, v214\n\t"
  "s_waitcnt lgkmcnt(2)\n\t"
  "v_mfma_f32_16x16x32_bf16 v[96:99], v[160:163], a[192:195], v[96:99]\n\t"
  "ds_read_b128 v[176:179], v219 offset:53248\n\t"
  "v_cvt_pk_bf16_f32 v196, v156, v157\n\t"
  "v_mul_f32 v208, v32, v32\n\t"
  "v_mfma_f32_16x16x32_bf16 v[96:99], v[164:167], a[208:211], v[96:99]\n\t"
  "ds_read_b128 v[180:183], v219 offset:55296\n\t"
  "v_mul_f32 v209, v33, v33\n\t"
  "v_mfma_f32_16x16x32_bf16 v[100:103], v[160:163], a[196:199], v[100:103]\n\t"
  "global_load_lds_dwordx4 v221, s[44:45] offset:2048 sc1\n\t"
  "v_mul_f32 v210, v34, v34\n\t"
  "v_mfma_f32_16x16x32_bf16 v[100:103], v[164:167], a[212:215], v[100:103]\n\t"
  "v_rcp_f32 v215, v215\n\t"
  "v_mul_f32 v211, v35, v35\n\t"
  "v_mfma_f32_16x16x32_bf16 v[104:107], v[160:163], a[200:203], v[104:107]\n\t"
  "v_mul_f32 v159, v159, v215\n\t"
  "v_cvt_pk_bf16_f32 v197, v158, v159\n\t"
  "v_mfma_f32_16x16x32_bf16 v[104:107], v[164:167], a[216:219], v[104:107]\n\t"
  "global_store_dwordx4 v246, v[252:255], s[56:57] offset:0 sc1\n\t"
  "v_fma_f32 v208, v208, v204, v200\n\t"
  "v_mfma_f32_16x16x32_bf16 v[108:111], v[160:163], a[204:207], v[108:111]\n\t"
  "v_fma_f32 v209, v209, v204, v200\n\t"
  "v_fma_f32 v210, v210, v204, v200\n\t"
  "v_mfma_f32_16x16x32_bf16 v[108:111], v[164:167], a[220:223], v[108:111]\n\t"
  "v_fma_f32 v211, v211, v204, v200\n\t"
  "v_mul_f32 v208, v32, v208\n\t"
  "s_waitcnt lgkmcnt(2)\n\t"
  "v_mfma_f32_16x16x32_bf16 v[112:115], v[168:171], a[192:195], v[112:115]\n\t"
  "ds_read_b128 v[160:163], v219 offset:54272\n\t"
  "v_mul_f32 v209, v33, v209\n\t"
  "v_mfma_f32_16x16x32_bf16 v[112:115], v[172:175], a[208:211], v[112:115]\n\t"
  "ds_read_b128 v[164:167], v219 offset:56320\n\t"
  "v_mul_f32 v210, v34, v210\n\t"
  "v_mfma_f32_16x16x32_bf16 v[116:119], v[168:171], a[196:199], v[116:119]\n\t"
  "global_store_dwordx4 v246, v[252:255], s[56:57] offset:1024 sc1\n\t"
  "v_mul_f32 v211, v35, v211\n\t"
  "v_mfma_f32_16x16x32_bf16 v[116:119], v[172:175], a[212:215], v[116:119]\n\t"
  "v_exp_f32 v208, v208\n\t"
  "v_mul_f32 v48, v32, v48\n\t"
  "v_mfma_f32_16x16x32_bf16 v[120:123], v[168:171], a[200:203], v[120:123]\n\t"
  "v_exp_f32 v209, v209\n\t"
  "v_mfma_f32_16x16x32_bf16 v[120:123], v[172:175], a[216:219], v[120:123]\n\t"
  "v_exp_f32 v210, v210\n\t"
  "v_mfma_f32_16x16x32_bf16 v[124:127], v[168:171], a[204:207], v[124:127]\n\t"
  "v_exp_f32 v211, v211\n\t"
  "v_mul_f32 v49, v33, v49\n\t"
  "v_mfma_f32_16x16x32_bf16 v[124:127], v[172:175], a[220:223], v[124:127]\n\t"
  "v_mul_f32 v50, v34, v50\n\t"
  "s_waitcnt lgkmcnt(2)\n\t"
  "v_mfma_f32_16x16x32_bf16 v[96:99], v[176:179], a[224:227], v[96:99]\n\t"
  "ds_read_b128 v[168:171], v219 offset:57344\n\t"
  "v_mul_f32 v51, v35, v51\n\t"
  "v_mfma_f32_16x16x32_bf16 v[96:99], v[180:183], a[240:243], v[96:99]\n\t"
  "ds_read_b128 v[172:175], v219 offset:59392\n\t"
  "v_add_f32 v208, 1.0, v208\n\t"
  "v_mfma_f32_16x16x32_bf16 v[100:103], v[176:179], a[228:231], v[100:103]\n\t"
  "v_rcp_f32 v208, v208\n\t"
  "v_add_f32 v209, 1.0, v209\n\t"
  "v_mfma_f32_16x16x32_bf16 v[100:103], v[180:183], a[244:247], v[100:103]\n\t"
  "v_rcp_f32 v209, v209\n\t"
  "v_mfma_f32_16x16x32_bf16 v[104:107], v[176:179], a[232:235], v[104:107]\n\t"
  "v_add_f32 v210, 1.0, v210\n\t"
  "v_add_f32 v211, 1.0, v211\n\t"
  "v_mfma_f32_16x16x32_bf16 v[104:107], v[180:183], a[248:251], v[104:107]\n\t"
  "v_rcp_f32 v210, v210\n\t"
  "v_mul_f32 v48, v48, v208\n\t"
  "v_mfma_f32_16x16x32_bf16 v[108:111], v[176:179], a[236:239], v[108:111]\n\t"
  "v_rcp_f32 v211, v211\n\t"
  "v_mfma_f32_16x16x32_bf16 v[108:111], v[180:183], a[252:255], v[108:111]\n\t"
  "v_mul_f32 v49, v49, v209\n\t"
  "s_waitcnt lgkmcnt(2)\n\t"
  "v_mfma_f32_16x16x32_bf16 v[112:115], v[160:163], a[224:227], v[112:115]\n\t"
  "ds_read_b128 v[176:179], v219 offset:58368\n\t"
  "v_mul_f32 v50, v50, v210\n\t"
  "v_mfma_f32_16x16x32_bf16 v[112:115], v[164:167], a[240:243], v[112:115]\n\t"
  "ds_read_b128 v[180:183], v219 offset:60416\n\t"
  "v_mul_f32 v51, v51, v211\n\t"
  "v_cvt_pk_bf16_f32 v186, v48, v49\n\t"
  "v_mfma_f32_16x16x32_bf16 v[116:119], v[160:163], a[228:231], v[116:119]\n\t"
  "v_cvt_pk_bf16_f32 v187, v50, v51\n\t"
  "v_mul_f32 v212, v36, v36\n\t"
  "v_mfma_f32_16x16x32_bf16 v[116:119], v[164:167], a[244:247], v[116:119]\n\t"
  "global_store_dwordx4 v216, v[184:187], s[34:35] offset:-1024 sc1\n\t"
  "v_mul_f32 v213, v37, v37\n\t"
  "v_mfma_f32_16x16x32_bf16 v[120:123], v[160:163], a[232:235], v[120:123]\n\t"
  "v_mul_f32 v214, v38, v38\n\t"
  "v_mul_f32 v215, v39, v39\n\t"
  "v_mfma_f32_16x16x32_bf16 v[120:123], v[164:167], a[248:251], v[120:123]\n\t"
  "v_fma_f32 v212, v212, v205, v201\n\t"
  "v_fma_f32 v213, v213, v205, v201\n\t"
  "v_fma_f32 v214, v214, v205, v201\n\t"
  "v_mfma_f32_16x16x32_bf16 v[124:127], v[160:163], a[236:239], v[124:127]\n\t"
  "v_fma_f32 v215, v215, v205, v201\n\t"
  "v_mul_f32 v212, v36, v212\n\t"
  "v_mfma_f32_16x16x32_bf16 v[124:127], v[164:167], a[252:255], v[124:127]\n\t"
  "s_waitcnt vmcnt(14) lgkmcnt(2)\n\t"
  "s_barrier\n\t"
  "v_mfma_f32_16x16x32_bf16 v[96:99], v[168:171], v[0:3], v[96:99]\n\t"
  "ds_read_b128 v[160:163], v219 offset:61440\n\t"
  "v_mul_f32 v213, v37, v213\n\t"
  "v_mul_f32 v214, v38, v214\n\t"
  "v_mfma_f32_16x16x32_bf16 v[96:99], v[172:175], v[16:19], v[96:99]\n\t"
  "ds_read_b128 v[164:167], v219 offset:63488\n\t"
  "v_mul_f32 v215, v39, v215\n\t"
  "v_mfma_f32_16x16x32_bf16 v[100:103], v[168:171], v[4:7], v[100:103]\n\t"
  "s_add_u32 m0, s46, 102400\n\t"
  "v_mul_f32 v52, v36, v52\n\t"
  "v_mfma_f32_16x16x32_bf16 v[100:103], v[172:175], v[20:23], v[100:103]\n\t"
  "v_exp_f32 v212, v212\n\t"
  "v_mfma_f32_16x16x32_bf16 v[104:107], v[168:171], v[8:11], v[104:107]\n\t"
  "v_exp_f32 v213, v213\n\t"
  "v_mul_f32 v53, v37, v53\n\t"
  "v_mfma_f32_16x16x32_bf16 v[104:107], v[172:175], v[24:27], v[104:107]\n\t"
  "s_add_u32 s44, s30, 196608\n\t"
  "s_addc_u32 s45, s31, 0\n\t"
  "v_mfma_f32_16x16x32_bf16 v[108:111], v[168:171], v[12:15], v[108:111]\n\t"
  "v_exp_f32 v214, v214\n\t"
  "v_mfma_f32_16x16x32_bf16 v[108:111], v[172:175], v[28:31], v[108:111]\n\t"
  "v_mul_f32 v54, v38, v54\n\t"
  "s_waitcnt lgkmcnt(2)\n\t"
  "v_mfma_f32_16x16x32_bf16 v[112:115], v[176:179], v[0:3], v[112:115]\n\t"
  "ds_read_b128 v[168:171], v219 offset:62464\n\t"
  "v_mul_f32 v55, v39, v55\n\t"
  "v_add_f32 v212, 1.0, v212\n\t"
  "v_mfma_f32_16x16x32_bf16 v[112:115], v[180:183], v[16:19], v[112:115]\n\t"
  "ds_read_b128 v[172:175], v219 offset:64512\n\t"
  "v_add_f32 v213, 1.0, v213\n\t"
  "v_mfma_f32_16x16x32_bf16 v[116:119], v[176:179], v[4:7], v[116:119]\n\t"
  "global_load_lds_dwordx4 v221, s[44:45] offset:0 sc1\n\t"
  "v_add_f32 v214, 1.0, v214\n\t"
  "v_mfma_f32_16x16x32_bf16 v[116:119], v[180:183], v[20:23], v[116:119]\n\t"
  "v_exp_f32 v215, v215\n\t"
  "v_mul_f32 v208, v40, v40\n\t"
  "v_mfma_f32_16x16x32_bf16 v[120:123], v[176:179], v[8:11], v[120:123]\n\t"
  "v_rcp_f32 v212, v212\n\t"
  "v_mfma_f32_16x16x32_bf16 v[120:123], v[180:183], v[24:27], v[120:123]\n\t"
  "global_load_lds_dwordx4 v221, s[44:45] offset:1024 sc1\n\t"
  "v_add_f32 v215, 1.0, v215\n\t"
  "v_mfma_f32_16x16x32_bf16 v[124:127], v[176:179], v[12:15], v[124:127]\n\t"
  "v_rcp_f32 v213, v213\n\t"
  "v_mfma_f32_16x16x32_bf16 v[124:127], v[180:183], v[28:31], v[124:127]\n\t"
  "v_mul_f32 v52, v52, v212\n\t"
  "v_mul_f32 v53, v53, v213\n\t"
  "s_waitcnt lgkmcnt(2)\n\t"
  "v_mfma_f32_16x16x32_bf16 v[128:131], v[160:163], a[0:3], 0\n\t"
  "ds_read_b128 v[176:179], v220 offset:0\n\t"
  "v_cvt_pk_bf16_f32 v190, v52, v53\n\t"
  "v_mfma_f32_16x16x32_bf16 v[128:131], v[164:167], a[16:19], v[128:131]\n\t"
  "ds_read_b128 v[180:183], v220 offset:2048\n\t"
  "v_mul_f32 v209, v41, v41\n\t"
  "v_mfma_f32_16x16x32_bf16 v[132:135], v[160:163], a[4:7], 0\n\t"
  "global_load_lds_dwordx4 v221, s[44:45] offset:2048 sc1\n\t"
  "v_mul_f32 v210, v42, v42\n\t"
  "v_mul_f32 v211, v43, v43\n\t"
  "v_mfma_f32_16x16x32_bf16 v[132:135], v[164:167], a[20:23], v[132:135]\n\t"
  "v_rcp_f32 v214, v214\n\t"
  "v_mfma_f32_16x16x32_bf16 v[136:139], v[160:163], a[8:11], 0\n\t"
  "v_rcp_f32 v215, v215\n\t"
  "v_mfma_f32_16x16x32_bf16 v[136:139], v[164:167], a[24:27], v[136:139]\n\t"
  "global_store_dwordx4 v247, v[252:255], s[56:57] offset:0 sc1\n\t"
  "v_mul_f32 v54, v54, v214\n\t"
  "v_mfma_f32_16x16x32_bf16 v[140:143], v[160:163], a[12:15], 0\n\t"
  "v_mul_f32 v55, v55, v215\n\t"
  "v_cvt_pk_bf16_f32 v191, v54, v55\n\t"
  "v_fma_f32 v208, v208, v206, v202\n\t"
  "v_mfma_f32_16x16x32_bf16 v[140:143], v[164:167], a[28:31], v[140:143]\n\t"
  "global_store_dwordx4 v216, v[188:191], s[36:37] offset:-1024 sc1\n\t"
  "s_waitcnt lgkmcnt(2)\n\t"
  "v_mfma_f32_16x16x32_bf16 v[144:147], v[168:171], a[0:3], 0\n\t"
  "ds_read_b128 v[160:163], v220 offset:1024\n\t"
  "v_fma_f32 v209, v209, v206, v202\n\t"
  "v_mfma_f32_16x16x32_bf16 v[144:147], v[172:175], a[16:19], v[144:147]\n\t"
  "ds_read_b128 v[164:167], v220 offset:3072\n\t"
  "v_fma_f32 v210, v210, v206, v202\n\t"
  "v_mfma_f32_16x16x32_bf16 v[148:151], v[168:171], a[4:7], 0\n\t"
  "global_store_dwordx4 v247, v[252:255], s[56:57] offset:1024 sc1\n\t"
  "v_fma_f32 v211, v211, v206, v202\n\t"
  "v_mul_f32 v208, v40, v208\n\t"
  "v_mfma_f32_16x16x32_bf16 v[148:151], v[172:175], a[20:23], v[148:151]\n\t"
  "v_exp_f32 v208, v208\n\t"
  "v_mfma_f32_16x16x32_bf16 v[152:155], v[168:171], a[8:11], 0\n\t"
  "v_mul_f32 v209, v41, v209\n\t"
  "v_mul_f32 v210, v42, v210\n\t"
  "v_mfma_f32_16x16x32_bf16 v[152:155], v[172:175], a[24:27], v[152:155]\n\t"
  "global_store_dwordx4 v248, v[252:255], s[56:57] offset:0 sc1\n\t"
  "v_mul_f32 v211, v43, v211\n\t"
  "v_mul_f32 v56, v40, v56\n\t"
  "v_mfma_f32_16x16x32_bf16 v[156:159], v[168:171], a[12:15], 0\n\t"
  "v_exp_f32 v209, v209\n\t"
  "v_mfma_f32_16x16x32_bf16 v[156:159], v[172:175], a[28:31], v[156:159]\n\t"
  "v_mul_f32 v57, v41, v57\n\t"
  "s_waitcnt lgkmcnt(2)\n\t"
  "v_mfma_f32_16x16x32_bf16 v[128:131], v[176:179], a[32:35], v[128:131]\n\t"
  "ds_read_b128 v[168:171], v220 offset:4096\n\t"
  "v_mul_f32 v58, v42, v58\n\t"
  "v_mfma_f32_16x16x32_bf16 v[128:131], v[180:183], a[48:51], v[128:131]\n\t"
  "ds_read_b128 v[172:175], v220 offset:6144\n\t"
  "v_mul_f32 v59, v43, v59\n\t"
  "v_add_f32 v208, 1.0, v208\n\t"
  "v_mfma_f32_16x16x32_bf16 v[132:135], v[176:179], a[36:39], v[132:135]\n\t"
  "v_exp_f32 v210, v210\n\t"
  "v_mfma_f32_16x16x32_bf16 v[132:135], v[180:183], a[52:55], v[132:135]\n\t"
  "v_exp_f32 v211, v211\n\t"
  "v_mfma_f32_16x16x32_bf16 v[136:139], v[176:179], a[40:43], v[136:139]\n\t"
  "v_rcp_f32 v208, v208\n\t"
  "v_mfma_f32_16x16x32_bf16 v[136:139], v[180:183], a[56:59], v[136:139]\n\t"
  "v_add_f32 v209, 1.0, v209\n\t"
  "v_add_f32 v210, 1.0, v210\n\t"
  "v_add_f32 v211, 1.0, v211\n\t"
  "v_mfma_f32_16x16x32_bf16 v[140:143], v[176:179], a[44:47], v[140:143]\n\t"
  "v_rcp_f32 v209, v209\n\t"
  "v_mfma_f32_16x16x32_bf16 v[140:143], v[180:183], a[60:63], v[140:143]\n\t"
  "v_mul_f32 v56, v56, v208\n\t"
  "s_waitcnt lgkmcnt(2)\n\t"
  "v_mfma_f32_16x16x32_bf16 v[144:147], v[160:163], a[32:35], v[144:147]\n\t"
  "ds_read_b128 v[176:179], v220 offset:5120\n\t"
  "v_mul_f32 v57, v57, v209\n\t"
  "v_cvt_pk_bf16_f32 v194, v56, v57\n\t"
  "v_mfma_f32_16x16x32_bf16 v[144:147], v[164:167], a[48:51], v[144:147]\n\t"
  "ds_read_b128 v[180:183], v220 offset:7168\n\t"
  "v_mul_f32 v212, v44, v44\n\t"
  "v_mfma_f32_16x16x32_bf16 v[148:151], v[160:163], a[36:39], v[148:151]\n\t"
  "v_rcp_f32 v210, v210\n\t"
  "v_mfma_f32_16x16x32_bf16 v[148:151], v[164:167], a[52:55], v[148:151]\n\t"
  "v_rcp_f32 v211, v211\n\t"
  "v_mfma_f32_16x16x32_bf16 v[152:155], v[160:163], a[40:43], v[152:155]\n\t"
  "v_mul_f32 v58, v58, v210\n\t"
  "v_mul_f32 v59, v59, v211\n\t"
  "v_cvt_pk_bf16_f32 v195, v58, v59\n\t"
  "v_mfma_f32_16x16x32_bf16 v[152:155], v[164:167], a[56:59], v[152:155]\n\t"
  "global_store_dwordx4 v216, v[192:195], s[38:39] offset:-1024 sc1\n\t"
  "v_mul_f32 v213, v45, v45\n\t"
  "v_mfma_f32_16x16x32_bf16 v[156:159], v[160:163], a[44:47], v[156:159]\n\t"
  "v_mul_f32 v214, v46, v46\n\t"
  "v_mul_f32 v215, v47, v47\n\t"
  "v_mfma_f32_16x16x32_bf16 v[156:159], v[164:167], a[60:63], v[156:159]\n\t"
  "s_waitcnt vmcnt(19) lgkmcnt(2)\n\t"
  "s_barrier\n\t"
  "v_mfma_f32_16x16x32_bf16 v[128:131], v[168:171], a[64:67], v[128:131]\n\t"
  "ds_read_b128 v[160:163], v220 offset:8192\n\t"
  "v_fma_f32 v212, v212, v207, v203\n\t"
  "v_fma_f32 v213, v213, v207, v203\n\t"
  "v_mfma_f32_16x16x32_bf16 v[128:131], v[172:175], a[80:83], v[128:131]\n\t"
  "ds_read_b128 v[164:167], v220 offset:10240\n\t"
  "v_fma_f32 v214, v214, v207, v203\n\t"
  "v_mfma_f32_16x16x32_bf16 v[132:135], v[168:171], a[68:71], v[132:135]\n\t"
  "s_add_u32 m0, s46, 114688\n\t"
  "v_fma_f32 v215, v215, v207, v203\n\t"
  "v_mfma_f32_16x16x32_bf16 v[132:135], v[172:175], a[84:87], v[132:135]\n\t"
  "v_mul_f32 v212, v44, v212\n\t"
  "v_mul_f32 v213, v45, v213\n\t"
  "v_mul_f32 v214, v46, v214\n\t"
  "v_mfma_f32_16x16x32_bf16 v[136:139], v[168:171], a[72:75], v[136:139]\n\t"
  "s_add_u32 s44, s30, 208896\n\t"
  "s_addc_u32 s45, s31, 0\n\t"
  "v_mfma_f32_16x16x32_bf16 v[136:139], v[172:175], a[88:91], v[136:139]\n\t"
  "v_exp_f32 v212, v212\n\t"
  "v_mfma_f32_16x16x32_bf16 v[140:143], v[168:171], a[76:79], v[140:143]\n\t"
  "global_load_lds_dwordx4 v221, s[44:45] offset:0 sc1\n\t"
  "v_mul_f32 v215, v47, v215\n\t"
  "v_mfma_f32_16x16x32_bf16 v[140:143], v[172:175], a[92:95], v[140:143]\n\t"
  "v_mul_f32 v60, v44, v60\n\t"
  "v_mul_f32 v61, v45, v61\n\t"
  "s_waitcnt lgkmcnt(2)\n\t"
  "v_mfma_f32_16x16x32_bf16 v[144:147], v[176:179], a[64:67], v[144:147]\n\t"
  "ds_read_b128 v[168:171], v220 offset:9216\n\t"
  "v_mul_f32 v62, v46, v62\n\t"
  "v_mfma_f32_16x16x32_bf16 v[144:147], v[180:183], a[80:83], v[144:147]\n\t"
  "ds_read_b128 v[172:175], v220 offset:11264\n\t"
  "v_mul_f32 v63, v47, v63\n\t"
  "v_mfma_f32_16x16x32_bf16 v[148:151], v[176:179], a[68:71], v[148:151]\n\t"
  "v_exp_f32 v213, v213\n\t"
  "v_mfma_f32_16x16x32_bf16 v[148:151], v[180:183], a[84:87], v[148:151]\n\t"
  "global_load_lds_dwordx4 v221, s[44:45] offset:1024 sc1\n\t"
  "v_add_f32 v212, 1.0, v212\n\t"
  "v_add_f32 v213, 1.0, v213\n\t"
  "v_mfma_f32_16x16x32_bf16 v[152:155], v[176:179], a[72:75], v[152:155]\n\t"
  "v_exp_f32 v214, v214\n\t"
  "v_mfma_f32_16x16x32_bf16 v[152:155], v[180:183], a[88:91], v[152:155]\n\t"
  "global_load_lds_dwordx4 v221, s[44:45] offset:2048 sc1\n\t"
  "v_add_f32 v214, 1.0, v214\n\t"
  "v_mfma_f32_16x16x32_bf16 v[156:159], v[176:179], a[76:79], v[156:159]\n\t"
  "v_exp_f32 v215, v215\n\t"
  "v_mfma_f32_16x16x32_bf16 v[156:159], v[180:183], a[92:95], v[156:159]\n\t"
  "s_add_u32 s44, s54, 0\n\t"
  "s_addc_u32 s45, s55, 0\n\t"
  "s_waitcnt lgkmcnt(2)\n\t"
  "v_mfma_f32_16x16x32_bf16 v[128:131], v[160:163], a[96:99], v[128:131]\n\t"
  "ds_read_b128 v[176:179], v220 offset:12288\n\t"
  "v_add_f32 v215, 1.0, v215\n\t"
  "v_mul_f32 v208, v64, v64\n\t"
  "v_mfma_f32_16x16x32_bf16 v[128:131], v[164:167], a[112:115], v[128:131]\n\t"
  "ds_read_b128 v[180:183], v220 offset:14336\n\t"
  "v_mul_f32 v209, v65, v65\n\t"
  "v_mfma_f32_16x16x32_bf16 v[132:135], v[160:163], a[100:103], v[132:135]\n\t"
  "v_rcp_f32 v212, v212\n\t"
  "v_mul_f32 v210, v66, v66\n\t"
  "v_mfma_f32_16x16x32_bf16 v[132:135], v[164:167], a[116:119], v[132:135]\n\t"
  "v_rcp_f32 v213, v213\n\t"
  "v_mfma_f32_16x16x32_bf16 v[136:139], v[160:163], a[104:107], v[136:139]\n\t"
  "s_add_u32 m0, s48, 0\n\t"
  "v_mul_f32 v60, v60, v212\n\t"
  "v_mfma_f32_16x16x32_bf16 v[136:139], v[164:167], a[120:123], v[136:139]\n\t"
  "v_rcp_f32 v214, v214\n\t"
  "v_mfma_f32_16x16x32_bf16 v[140:143], v[160:163], a[108:111], v[140:143]\n\t"
  "global_load_lds_dwordx4 v230, s[44:45] offset:0 sc1\n\t"
  "v_mul_f32 v61, v61, v213\n\t"
  "v_mul_f32 v62, v62, v214\n\t"
  "v_mfma_f32_16x16x32_bf16 v[140:143], v[164:167], a[124:127], v[140:143]\n\t"
  "v_cvt_pk_bf16_f32 v198, v60, v61\n\t"
  "s_waitcnt lgkmcnt(2)\n\t"
  "v_mfma_f32_16x16x32_bf16 v[144:147], v[168:171], a[96:99], v[144:147]\n\t"
  "ds_read_b128 v[160:163], v220 offset:13312\n\t"
  "v_mul_f32 v211, v67, v67\n\t"
  "v_mfma_f32_16x16x32_bf16 v[144:147], v[172:175], a[112:115], v[144:147]\n\t"
  "ds_read_b128 v[164:167], v220 offset:15360\n\t"
  "v_fma_f32 v208, v208, v204, v200\n\t"
  "v_mfma_f32_16x16x32_bf16 v[148:151], v[168:171], a[100:103], v[148:151]\n\t"
  "global_load_lds_dwordx4 v230, s[44:45] offset:1024 sc1\n\t"
  "v_fma_f32 v209, v209, v204, v200\n\t"
  "v_fma_f32 v210, v210, v204, v200\n\t"
  "v_mfma_f32_16x16x32_bf16 v[148:151], v[172:175], a[116:119], v[148:151]\n\t"
  "v_rcp_f32 v215, v215\n\t"
  "v_mfma_f32_16x16x32_bf16 v[152:155], v[168:171], a[104:107], v[152:155]\n\t"
  "v_mul_f32 v63, v63, v215\n\t"
  "v_cvt_pk_bf16_f32 v199, v62, v63\n\t"
  "v_mfma_f32_16x16x32_bf16 v[152:155], v[172:175], a[120:123], v[152:155]\n\t"
  "global_load_lds_dwordx4 v230, s[44:45] offset:2048 sc1\n\t"
  "global_store_dwordx4 v216, v[196:199], s[40:41] offset:-1024 sc1\n\t"
  "v_fma_f32 v211, v211, v204, v200\n\t"
  "v_mfma_f32_16x16x32_bf16 v[156:159], v[168:171], a[108:111], v[156:159]\n\t"
  "v_mul_f32 v208, v64, v208\n\t"
  "v_mul_f32 v209, v65, v209\n\t"
  "v_mfma_f32_16x16x32_bf16 v[156:159], v[172:175], a[124:127], v[156:159]\n\t"
  "global_load_lds_dwordx4 v230, s[44:45] offset:3072 sc1\n\t"
  "s_waitcnt lgkmcnt(2)\n\t"
  "v_mfma_f32_16x16x32_bf16 v[128:131], v[176:179], a[128:131], v[128:131]\n\t"
  "ds_read_b128 v[168:171], v220 offset:16384\n\t"
  "v_mul_f32 v210, v66, v210\n\t"
  "v_mfma_f32_16x16x32_bf16 v[128:131], v[180:183], a[144:147], v[128:131]\n\t"
  "ds_read_b128 v[172:175], v220 offset:18432\n\t"
  "v_mul_f32 v211, v67, v211\n\t"
  "v_mul_f32 v80, v64, v80\n\t"
  "v_mfma_f32_16x16x32_bf16 v[132:135], v[176:179], a[132:135], v[132:135]\n\t"
  "v_exp_f32 v208, v208\n\t"
  "v_mfma_f32_16x16x32_bf16 v[132:135], v[180:183], a[148:151], v[132:135]\n\t"
  "s_add_u32 m0, s48, 4096\n\t"
  "v_mul_f32 v81, v65, v81\n\t"
  "v_mfma_f32_16x16x32_bf16 v[136:139], v[176:179], a[136:139], v[136:139]\n\t"
  "PG2_%=:\n\t"
  "s_cbranch_execz PG6_%=\n\t"
  "v_exp_f32 v209, v209\n\t"
  "v_mfma_f32_16x16x32_bf16 v[136:139], v[180:183], a[152:155], v[136:139]\n\t"
  "v_exp_f32 v210, v210\n\t"
  "v_mul_f32 v82, v66, v82\n\t"
  "v_mfma_f32_16x16x32_bf16 v[140:143], v[176:179], a[140:143], v[140:143]\n\t"
  "global_load_lds_dwordx4 v231, s[44:45] offset:0 sc1\n\t"
  "v_mul_f32 v83, v67, v83\n\t"
  "v_mfma_f32_16x16x32_bf16 v[140:143], v[180:183], a[156:159], v[140:143]\n\t"
  "v_add_f32 v208, 1.0, v208\n\t"
  "s_waitcnt lgkmcnt(2)\n\t"
  "v_mfma_f32_16x16x32_bf16 v[144:147], v[160:163], a[128:131], v[144:147]\n\t"
  "ds_read_b128 v[176:179], v220 offset:17408\n\t"
  "v_add_f32 v209, 1.0, v209\n\t"
  "v_add_f32 v210, 1.0, v210\n\t"
  "v_mfma_f32_16x16x32_bf16 v[144:147], v[164:167], a[144:147], v[144:147]\n\t"
  "ds_read_b128 v[180:183], v220 offset:19456\n\t"
  "v_mul_f32 v212, v68, v68\n\t"
  "v_mfma_f32_16x16x32_bf16 v[148:151], v[160:163], a[132:135], v[148:151]\n\t"
  "global_load_lds_dwordx4 v231, s[44:45] offset:1024 sc1\n\t"
  "v_mul_f32 v213, v69, v69\n\t"
  "v_mfma_f32_16x16x32_bf16 v[148:151], v[164:167], a[148:151], v[148:151]\n\t"
  "v_exp_f32 v211, v211\n\t"
  "v_mfma_f32_16x16x32_bf16 v[152:155], v[160:163], a[136:139], v[152:155]\n\t"
  "global_load_lds_dwordx4 v231, s[44:45] offset:2048 sc1\n\t"
  "v_add_f32 v211, 1.0, v211\n\t"
  "v_mul_f32 v214, v70, v70\n\t"
  "v_mfma_f32_16x16x32_bf16 v[152:155], v[164:167], a[152:155], v[152:155]\n\t"
  "v_rcp_f32 v208, v208\n\t"
  "v_mfma_f32_16x16x32_bf16 v[156:159], v[160:163], a[140:143], v[156:159]\n\t"
  "v_rcp_f32 v209, v209\n\t"
  "v_mfma_f32_16x16x32_bf16 v[156:159], v[164:167], a[156:159], v[156:159]\n\t"
  "s_waitcnt vmcnt(27) lgkmcnt(2)\n\t"
  "s_barrier\n\t"
  "v_mfma_f32_16x16x32_bf16 v[128:131], v[168:171], a[160:163], v[128:131]\n\t"
  "ds_read_b128 v[160:163], v220 offset:20480\n\t"
  "v_mul_f32 v80, v80, v208\n\t"
  "v_mul_f32 v81, v81, v209\n\t"
  "v_mfma_f32_16x16x32_bf16 v[128:131], v[172:175], a[176:179], v[128:131]\n\t"
  "ds_read_b128 v[164:167], v220 offset:22528\n\t"
  "v_cvt_pk_bf16_f32 v184, v80, v81\n\t"
  "v_mfma_f32_16x16x32_bf16 v[132:135], v[168:171], a[164:167], v[132:135]\n\t"
  "s_add_u32 m0, s46, 126976\n\t"
  "v_mul_f32 v215, v71, v71\n\t"
  "v_mfma_f32_16x16x32_bf16 v[132:135], v[172:175], a[180:183], v[132:135]\n\t"
  "v_rcp_f32 v210, v210\n\t"
  "v_mfma_f32_16x16x32_bf16 v[136:139], v[168:171], a[168:171], v[136:139]\n\t"
  "v_rcp_f32 v211, v211\n\t"
  "v_mul_f32 v82, v82, v210\n\t"
  "v_mfma_f32_16x16x32_bf16 v[136:139], v[172:175], a[184:187], v[136:139]\n\t"
  "s_add_u32 s44, s30, 221184\n\t"
  "s_addc_u32 s45, s31, 0\n\t"
  "v_mfma_f32_16x16x32_bf16 v[140:143], v[168:171], a[172:175], v[140:143]\n\t"
  "v_mul_f32 v83, v83, v211\n\t"
  "v_cvt_pk_bf16_f32 v185, v82, v83\n\t"
  "v_mfma_f32_16x16x32_bf16 v[140:143], v[172:175], a[188:191], v[140:143]\n\t"
  "v_fma_f32 v212, v212, v205, v201\n\t"
  "v_fma_f32 v213, v213, v205, v201\n\t"
  "s_waitcnt lgkmcnt(2)\n\t"
  "v_mfma_f32_16x16x32_bf16 v[144:147], v[176:179], a[160:163], v[144:147]\n\t"
  "ds_read_b128 v[168:171], v220 offset:21504\n\t"
  "v_fma_f32 v214, v214, v205, v201\n\t"
  "v_mfma_f32_16x16x32_bf16 v[144:147], v[180:183], a[176:179], v[144:147]\n\t"
  "ds_read_b128 v[172:175], v220 offset:23552\n\t"
  "v_fma_f32 v215, v215, v205, v201\n\t"
  "v_mfma_f32_16x16x32_bf16 v[148:151], v[176:179], a[164:167], v[148:151]\n\t"
  "global_load_lds_dwordx4 v221, s[44:45] offset:0 sc1\n\t"
  "v_mul_f32 v212, v68, v212\n\t"
  "v_mfma_f32_16x16x32_bf16 v[148:151], v[180:183], a[180:183], v[148:151]\n\t"
  "v_exp_f32 v212, v212\n\t"
  "v_mul_f32 v213, v69, v213\n\t"
  "v_mfma_f32_16x16x32_bf16 v[152:155], v[176:179], a[168:171], v[152:155]\n\t"
  "v_exp_f32 v213, v213\n\t"
  "v_mfma_f32_16x16x32_bf16 v[152:155], v[180:183], a[184:187], v[152:155]\n\t"
  "global_load_lds_dwordx4 v221, s[44:45] offset:1024 sc1\n\t"
  "v_mul_f32 v214, v70, v214\n\t"
  "v_mfma_f32_16x16x32_bf16 v[156:159], v[176:179], a[172:175], v[156:159]\n\t"
  "v_exp_f32 v214, v214\n\t"
  "v_mfma_f32_16x16x32_bf16 v[156:159], v[180:183], a[188:191], v[156:159]\n\t"
  "v_mul_f32 v215, v71, v215\n\t"
  "v_mul_f32 v84, v68, v84\n\t"
  "s_waitcnt lgkmcnt(2)\n\t"
  "v_mfma_f32_16x16x32_bf16 v[128:131], v[160:163], a[192:195], v[128:131]\n\t"
  "ds_read_b128 v[176:179], v220 offset:24576\n\t"
  "v_mul_f32 v85, v69, v85\n\t"
  "v_mfma_f32_16x16x32_bf16 v[128:131], v[164:167], a[208:211], v[128:131]\n\t"
  "ds_read_b128 v[180:183], v220 offset:26624\n\t"
  "v_mul_f32 v86, v70, v86\n\t"
  "v_mfma_f32_16x16x32_bf16 v[132:135], v[160:163], a[196:199], v[132:135]\n\t"
  "global_load_lds_dwordx4 v221, s[44:45] offset:2048 sc1\n\t"
  "v_mul_f32 v87, v71, v87\n\t"
  "v_mfma_f32_16x16x32_bf16 v[132:135], v[164:167], a[212:215], v[132:135]\n\t"
  "v_exp_f32 v215, v215\n\t"
  "v_add_f32 v212, 1.0, v212\n\t"
  "v_mfma_f32_16x16x32_bf16 v[136:139], v[160:163], a[200:203], v[136:139]\n\t"
  "v_rcp_f32 v212, v212\n\t"
  "v_mfma_f32_16x16x32_bf16 v[136:139], v[164:167], a[216:219], v[136:139]\n\t"
  "v_add_f32 v213, 1.0, v213\n\t"
  "v_add_f32 v214, 1.0, v214\n\t"
  "v_mfma_f32_16x16x32_bf16 v[140:143], v[160:163], a[204:207], v[140:143]\n\t"
  "v_rcp_f32 v213, v213\n\t"
  "v_add_f32 v215, 1.0, v215\n\t"
  "v_mfma_f32_16x16x32_bf16 v[140:143], v[164:167], a[220:223], v[140:143]\n\t"
  "v_mul_f32 v84, v84, v212\n\t"
  "s_waitcnt lgkmcnt(2)\n\t"
  "v_mfma_f32_16x16x32_bf16 v[144:147], v[168:171], a[192:195], v[144:147]\n\t"
  "ds_read_b128 v[160:163], v220 offset:25600\n\t"
  "v_mul_f32 v85, v85, v213\n\t"
  "v_mfma_f32_16x16x32_bf16 v[144:147], v[172:175], a[208:211], v[144:147]\n\t"
  "ds_read_b128 v[164:167], v220 offset:27648\n\t"
  "v_cvt_pk_bf16_f32 v188, v84, v85\n\t"
  "v_mfma_f32_16x16x32_bf16 v[148:151], v[168:171], a[196:199], v[148:151]\n\t"
  "v_rcp_f32 v214, v214\n\t"
  "v_mul_f32 v208, v72, v72\n\t"
  "v_mfma_f32_16x16x32_bf16 v[148:151], v[172:175], a[212:215], v[148:151]\n\t"
  "v_rcp_f32 v215, v215\n\t"
  "v_mfma_f32_16x16x32_bf16 v[152:155], v[168:171], a[200:203], v[152:155]\n\t"
  "v_mul_f32 v86, v86, v214\n\t"
  "v_mul_f32 v87, v87, v215\n\t"
  "v_mfma_f32_16x16x32_bf16 v[152:155], v[172:175], a[216:219], v[152:155]\n\t"
  "v_cvt_pk_bf16_f32 v189, v86, v87\n\t"
  "v_mul_f32 v209, v73, v73\n\t"
  "v_mfma_f32_16x16x32_bf16 v[156:159], v[168:171], a[204:207], v[156:159]\n\t"
  "v_mul_f32 v210, v74, v74\n\t"
  "v_mul_f32 v211, v75, v75\n\t"
  "v_fma_f32 v208, v208, v206, v202\n\t"
  "v_mfma_f32_16x16x32_bf16 v[156:159], v[172:175], a[220:223], v[156:159]\n\t"
  "v_fma_f32 v209, v209, v206, v202\n\t"
  "s_waitcnt lgkmcnt(2)\n\t"
  "v_mfma_f32_16x16x32_bf16 v[128:131], v[176:179], a[224:227], v[128:131]\n\t"
  "ds_read_b128 v[168:171], v220 offset:28672\n\t"
  "v_fma_f32 v210, v210, v206, v202\n\t"
  "v_mfma_f32_16x16x32_bf16 v[128:131], v[180:183], a[240:243], v[128:131]\n\t"
  "ds_read_b128 v[172:175], v220 offset:30720\n\t"
  "v_fma_f32 v211, v211, v206, v202\n\t"
  "v_mfma_f32_16x16x32_bf16 v[132:135], v[176:179], a[228:231], v[132:135]\n\t"
  "v_mul_f32 v208, v72, v208\n\t"
  "v_mul_f32 v209, v73, v209\n\t"
  "v_mul_f32 v210, v74, v210\n\t"
  "v_mfma_f32_16x16x32_bf16 v[132:135], v[180:183], a[244:247], v[132:135]\n\t"
  "v_exp_f32 v208, v208\n\t"
  "v_mfma_f32_16x16x32_bf16 v[136:139], v[176:179], a[232:235], v[136:139]\n\t"
  "v_exp_f32 v209, v209\n\t"
  "v_mfma_f32_16x16x32_bf16 v[136:139], v[180:183], a[248:251], v[136:139]\n\t"
  "v_exp_f32 v210, v210\n\t"
  "v_mul_f32 v211, v75, v211\n\t"
  "v_mfma_f32_16x16x32_bf16 v[140:143], v[176:179], a[236:239], v[140:143]\n\t"
  "v_exp_f32 v211, v211\n\t"
  "v_mfma_f32_16x16x32_bf16 v[140:143], v[180:183], a[252:255], v[140:143]\n\t"
  "v_mul_f32 v88, v72, v88\n\t"
  "s_waitcnt lgkmcnt(2)\n\t"
  "v_mfma_f32_16x16x32_bf16 v[144:147], v[160:163], a[224:227], v[144:147]\n\t"
  "ds_read_b128 v[176:179], v220 offset:29696\n\t"
  "v_mul_f32 v89, v73, v89\n\t"
  "v_mfma_f32_16x16x32_bf16 v[144:147], v[164:167], a[240:243], v[144:147]\n\t"
  "ds_read_b128 v[180:183], v220 offset:31744\n\t"
  "v_mul_f32 v90, v74, v90\n\t"
  "v_mul_f32 v91, v75, v91\n\t"
  "v_mfma_f32_16x16x32_bf16 v[148:151], v[160:163], a[228:231], v[148:151]\n\t"
  "v_add_f32 v208, 1.0, v208\n\t"
  "v_add_f32 v209, 1.0, v209\n\t"
  "v_mfma_f32_16x16x32_bf16 v[148:151], v[164:167], a[244:247], v[148:151]\n\t"
  "v_rcp_f32 v208, v208\n\t"
  "v_mfma_f32_16x16x32_bf16 v[152:155], v[160:163], a[232:235], v[152:155]\n\t"
  "v_rcp_f32 v209, v209\n\t"
  "v_mfma_f32_16x16x32_bf16 v[152:155], v[164:167], a[248:251], v[152:155]\n\t"
  "v_add_f32 v210, 1.0, v210\n\t"
  "v_add_f32 v211, 1.0, v211\n\t"
  "v_mul_f32 v88, v88, v208\n\t"
  "v_mfma_f32_16x16x32_bf16 v[156:159], v[160:163], a[236:239], v[156:159]\n\t"
  "v_rcp_f32 v210, v210\n\t"
  "v_mfma_f32_16x16x32_bf16 v[156:159], v[164:167], a[252:255], v[156:159]\n\t"
  "s_waitcnt vmcnt(25) lgkmcnt(2)\n\t"
  "s_barrier\n\t"
  "v_mfma_f32_16x16x32_bf16 v[128:131], v[168:171], v[0:3], v[128:131]\n\t"
  "ds_read_b128 v[160:163], v219 offset:24576\n\t"
  "v_mul_f32 v89, v89, v209\n\t"
  "v_mfma_f32_16x16x32_bf16 v[128:131], v[172:175], v[16:19], v[128:131]\n\t"
  "ds_read_b128 v[164:167], v219 offset:26624\n\t"
  "v_mul_f32 v90, v90, v210\n\t"
  "v_cvt_pk_bf16_f32 v192, v88, v89\n\t"
  "v_mfma_f32_16x16x32_bf16 v[132:135], v[168:171], v[4:7], v[132:135]\n\t"
  "s_add_u32 m0, s46, 139264\n\t"
  "v_mul_f32 v212, v76, v76\n\t"
  "v_mfma_f32_16x16x32_bf16 v[132:135], v[172:175], v[20:23], v[132:135]\n\t"
  "v_rcp_f32 v211, v211\n\t"
  "v_mfma_f32_16x16x32_bf16 v[136:139], v[168:171], v[8:11], v[136:139]\n\t"
  "v_mul_f32 v91, v91, v211\n\t"
  "v_cvt_pk_bf16_f32 v193, v90, v91\n\t"
  "v_mfma_f32_16x16x32_bf16 v[136:139], v[172:175], v[24:27], v[136:139]\n\t"
  "s_add_u32 s44, s30, 233472\n\t"
  "s_addc_u32 s45, s31, 0\n\t"
  "v_mul_f32 v213, v77, v77\n\t"
  "v_mfma_f32_16x16x32_bf16 v[140:143], v[168:171], v[12:15], v[140:143]\n\t"
  "v_mul_f32 v214, v78, v78\n\t"
  "v_mul_f32 v215, v79, v79\n\t"
  "v_mfma_f32_16x16x32_bf16 v[140:143], v[172:175], v[28:31], v[140:143]\n\t"
  "v_fma_f32 v212, v212, v207, v203\n\t"
  "s_waitcnt lgkmcnt(2)\n\t"
  "v_mfma_f32_16x16x32_bf16 v[144:147], v[176:179], v[0:3], v[144:147]\n\t"
  "ds_read_b128 v[168:171], v219 offset:25600\n\t"
  "v_fma_f32 v213, v213, v207, v203\n\t"
  "v_fma_f32 v214, v214, v207, v203\n\t"
  "v_mfma_f32_16x16x32_bf16 v[144:147], v[180:183], v[16:19], v[144:147]\n\t"
  "ds_read_b128 v[172:175], v219 offset:27648\n\t"
  "v_fma_f32 v215, v215, v207, v203\n\t"
  "v_mfma_f32_16x16x32_bf16 v[148:151], v[176:179], v[4:7], v[148:151]\n\t"
  "global_load_lds_dwordx4 v221, s[44:45] offset:0 sc1\n\t"
  "v_mul_f32 v212, v76, v212\n\t"
  "v_mfma_f32_16x16x32_bf16 v[148:151], v[180:183], v[20:23], v[148:151]\n\t"
  "v_exp_f32 v212, v212\n\t"
  "v_mfma_f32_16x16x32_bf16 v[152:155], v[176:179], v[8:11], v[152:155]\n\t"
  "v_mul_f32 v213, v77, v213\n\t"
  "v_mul_f32 v214, v78, v214\n\t"
  "v_mul_f32 v215, v79, v215\n\t"
  "v_mfma_f32_16x16x32_bf16 v[152:155], v[180:183], v[24:27], v[152:155]\n\t"
  "global_load_lds_dwordx4 v221, s[44:45] offset:1024 sc1\n\t"
  "v_mul_f32 v92, v76, v92\n\t"
  "v_mfma_f32_16x16x32_bf16 v[156:159], v[176:179], v[12:15], v[156:159]\n\t"
  "v_exp_f32 v213, v213\n\t"
  "v_mfma_f32_16x16x32_bf16 v[156:159], v[180:183], v[28:31], v[156:159]\n\t"
  "v_mul_f32 v93, v77, v93\n\t"
  "s_waitcnt lgkmcnt(2)\n\t"
  "v_mfma_f32_16x16x32_bf16 v[32:35], v[160:163], a[0:3], 0\n\t"
  "ds_read_b128 v[176:179], v219 offset:28672\n\t"
  "v_mul_f32 v94, v78, v94\n\t"
  "v_mul_f32 v95, v79, v95\n\t"
  "v_mfma_f32_16x16x32_bf16 v[32:35], v[164:167], a[16:19], v[32:35]\n\t"
  "ds_read_b128 v[180:183], v219 offset:30720\n\t"
  "v_add_f32 v212, 1.0, v212\n\t"
  "v_mfma_f32_16x16x32_bf16 v[36:39], v[160:163], a[4:7], 0\n\t"
  "global_load_lds_dwordx4 v221, s[44:45] offset:2048 sc1\n\t"
  "v_add_f32 v213, 1.0, v213\n\t"
  "v_mfma_f32_16x16x32_bf16 v[36:39], v[164:167], a[20:23], v[36:39]\n\t"
  "v_exp_f32 v214, v214\n\t"
  "v_mfma_f32_16x16x32_bf16 v[40:43], v[160:163], a[8:11], 0\n\t"
  "v_exp_f32 v215, v215\n\t"
  "v_add_f32 v214, 1.0, v214\n\t"
  "v_mfma_f32_16x16x32_bf16 v[40:43], v[164:167], a[24:27], v[40:43]\n\t"
  "v_rcp_f32 v212, v212\n\t"
  "v_mfma_f32_16x16x32_bf16 v[44:47], v[160:163], a[12:15], 0\n\t"
  "v_rcp_f32 v213, v213\n\t"
  "v_mfma_f32_16x16x32_bf16 v[44:47], v[164:167], a[28:31], v[44:47]\n\t"
  "v_add_f32 v215, 1.0, v215\n\t"
  "s_waitcnt lgkmcnt(2)\n\t"
  "v_mfma_f32_16x16x32_bf16 v[48:51], v[168:171], a[0:3], 0\n\t"
  "ds_read_b128 v[160:163], v219 offset:29696\n\t"
  "v_mul_f32 v92, v92, v212\n\t"
  "v_mul_f32 v93, v93, v213\n\t"
  "v_mfma_f32_16x16x32_bf16 v[48:51], v[172:175], a[16:19], v[48:51]\n\t"
  "ds_read_b128 v[164:167], v219 offset:31744\n\t"
  "v_cvt_pk_bf16_f32 v196, v92, v93\n\t"
  "v_mfma_f32_16x16x32_bf16 v[52:55], v[168:171], a[4:7], 0\n\t"
  "v_rcp_f32 v214, v214\n\t"
  "v_mfma_f32_16x16x32_bf16 v[52:55], v[172:175], a[20:23], v[52:55]\n\t"
  "v_rcp_f32 v215, v215\n\t"
  "v_mul_f32 v94, v94, v214\n\t"
  "v_mfma_f32_16x16x32_bf16 v[56:59], v[168:171], a[8:11], 0\n\t"
  "v_mul_f32 v95, v95, v215\n\t"
  "v_cvt_pk_bf16_f32 v197, v94, v95\n\t"
  "v_mfma_f32_16x16x32_bf16 v[56:59], v[172:175], a[24:27], v[56:59]\n\t"
  "v_mul_f32 v208, v96, v96\n\t"
  "v_mul_f32 v209, v97, v97\n\t"
  "v_mfma_f32_16x16x32_bf16 v[60:63], v[168:171], a[12:15], 0\n\t"
  "v_mul_f32 v210, v98, v98\n\t"
  "v_mul_f32 v211, v99, v99\n\t"
  "v_mfma_f32_16x16x32_bf16 v[60:63], v[172:175], a[28:31], v[60:63]\n\t"
  "v_fma_f32 v208, v208, v204, v200\n\t"
  "v_fma_f32 v209, v209, v204, v200\n\t"
  "s_waitcnt lgkmcnt(2)\n\t"
  "v_mfma_f32_16x16x32_bf16 v[32:35], v[176:179], a[32:35], v[32:35]\n\t"
  "ds_read_b128 v[168:171], v219 offset:32768\n\t"
  "v_fma_f32 v210, v210, v204, v200\n\t"
  "v_mfma_f32_16x16x32_bf16 v[32:35], v[180:183], a[48:51], v[32:35]\n\t"
  "ds_read_b128 v[172:175], v219 offset:34816\n\t"
  "v_fma_f32 v211, v211, v204, v200\n\t"
  "v_mfma_f32_16x16x32_bf16 v[36:39], v[176:179], a[36:39], v[36:39]\n\t"
  "v_mul_f32 v208, v96, v208\n\t"
  "v_mul_f32 v209, v97, v209\n\t"
  "v_mfma_f32_16x16x32_bf16 v[36:39], v[180:183], a[52:55], v[36:39]\n\t"
  "v_exp_f32 v208, v208\n\t"
  "v_mul_f32 v210, v98, v210\n\t"
  "v_mfma_f32_16x16x32_bf16 v[40:43], v[176:179], a[40:43], v[40:43]\n\t"
  "v_exp_f32 v209, v209\n\t"
  "v_mfma_f32_16x16x32_bf16 v[40:43], v[180:183], a[56:59], v[40:43]\n\t"
  "v_exp_f32 v210, v210\n\t"
  "v_mfma_f32_16x16x32_bf16 v[44:47], v[176:179], a[44:47], v[44:47]\n\t"
  "v_mul_f32 v211, v99, v211\n\t"
  "v_mul_f32 v112, v96, v112\n\t"
  "v_mfma_f32_16x16x32_bf16 v[44:47], v[180:183], a[60:63], v[44:47]\n\t"
  "v_mul_f32 v113, v97, v113\n\t"
  "v_mul_f32 v114, v98, v114\n\t"
  "s_waitcnt lgkmcnt(2)\n\t"
  "v_mfma_f32_16x16x32_bf16 v[48:51], v[160:163], a[32:35], v[48:51]\n\t"
  "ds_read_b128 v[176:179], v219 offset:33792\n\t"
  "v_mul_f32 v115, v99, v115\n\t"
  "v_mfma_f32_16x16x32_bf16 v[48:51], v[164:167], a[48:51], v[48:51]\n\t"
  "ds_read_b128 v[180:183], v219 offset:35840\n\t"
  "v_add_f32 v208, 1.0, v208\n\t"
  "v_mfma_f32_16x16x32_bf16 v[52:55], v[160:163], a[36:39], v[52:55]\n\t"
  "v_exp_f32 v211, v211\n\t"
  "v_mfma_f32_16x16x32_bf16 v[52:55], v[164:167], a[52:55], v[52:55]\n\t"
  "v_rcp_f32 v208, v208\n\t"
  "v_add_f32 v209, 1.0, v209\n\t"
  "v_mfma_f32_16x16x32_bf16 v[56:59], v[160:163], a[40:43], v[56:59]\n\t"
  "v_rcp_f32 v209, v209\n\t"
  "v_mfma_f32_16x16x32_bf16 v[56:59], v[164:167], a[56:59], v[56:59]\n\t"
  "v_add_f32 v210, 1.0, v210\n\t"
  "v_add_f32 v211, 1.0, v211\n\t"
  "v_mfma_f32_16x16x32_bf16 v[60:63], v[160:163], a[44:47], v[60:63]\n\t"
  "v_rcp_f32 v210, v210\n\t"
  "v_mfma_f32_16x16x32_bf16 v[60:63], v[164:167], a[60:63], v[60:63]\n\t"
  "v_mul_f32 v112, v112, v208\n\t"
  "s_waitcnt vmcnt(22) lgkmcnt(2)\n\t"
  "s_barrier\n\t"
  "v_mfma_f32_16x16x32_bf16 v[32:35], v[168:171], a[64:67], v[32:35]\n\t"
  "ds_read_b128 v[160:163], v219 offset:36864\n\t"
  "v_mul_f32 v113, v113, v209\n\t"
  "v_mfma_f32_16x16x32_bf16 v[32:35], v[172:175], a[80:83], v[32:35]\n\t"
  "ds_read_b128 v[164:167], v219 offset:38912\n\t"
  "v_mul_f32 v114, v114, v210\n\t"
  "v_mfma_f32_16x16x32_bf16 v[36:39], v[168:171], a[68:71], v[36:39]\n\t"
  "s_add_u32 m0, s46, 151552\n\t"
  "v_cvt_pk_bf16_f32 v186, v112, v113\n\t"
  "v_mul_f32 v212, v100, v100\n\t"
  "v_mfma_f32_16x16x32_bf16 v[36:39], v[172:175], a[84:87], v[36:39]\n\t"
  "v_rcp_f32 v211, v211\n\t"
  "v_mfma_f32_16x16x32_bf16 v[40:43], v[168:171], a[72:75], v[40:43]\n\t"
  "s_add_u32 s44, s30, 245760\n\t"
  "s_addc_u32 s45, s31, 0\n\t"
  "v_mfma_f32_16x16x32_bf16 v[40:43], v[172:175], a[88:91], v[40:43]\n\t"
  "v_mul_f32 v115, v115, v211\n\t"
  "v_cvt_pk_bf16_f32 v187, v114, v115\n\t"
  "v_mfma_f32_16x16x32_bf16 v[44:47], v[168:171], a[76:79], v[44:47]\n\t"
  "global_load_lds_dwordx4 v221, s[44:45] offset:0 sc1\n\t"
  "global_store_dwordx4 v216, v[184:187], s[34:35] offset:0 sc1\n\t"
  "v_mul_f32 v213, v101, v101\n\t"
  "v_mfma_f32_16x16x32_bf16 v[44:47], v[172:175], a[92:95], v[44:47]\n\t"
  "v_mul_f32 v214, v102, v102\n\t"
  "s_waitcnt lgkmcnt(2)\n\t"
  "v_mfma_f32_16x16x32_bf16 v[48:51], v[176:179], a[64:67], v[48:51]\n\t"
  "ds_read_b128 v[168:171], v219 offset:37888\n\t"
  "v_mul_f32 v215, v103, v103\n\t"
  "v_mfma_f32_16x16x32_bf16 v[48:51], v[180:183], a[80:83], v[48:51]\n\t"
  "ds_read_b128 v[172:175], v219 offset:39936\n\t"
  "v_fma_f32 v212, v212, v205, v201\n\t"
  "v_mfma_f32_16x16x32_bf16 v[52:55], v[176:179], a[68:71], v[52:55]\n\t"
  "v_fma_f32 v213, v213, v205, v201\n\t"
  "v_fma_f32 v214, v214, v205, v201\n\t"
  "v_fma_f32 v215, v215, v205, v201\n\t"
  "v_mfma_f32_16x16x32_bf16 v[52:55], v[180:183], a[84:87], v[52:55]\n\t"
  "global_load_lds_dwordx4 v221, s[44:45] offset:1024 sc1\n\t"
  "v_mul_f32 v212, v100, v212\n\t"
  "v_mfma_f32_16x16x32_bf16 v[56:59], v[176:179], a[72:75], v[56:59]\n\t"
  "v_exp_f32 v212, v212\n\t"
  "v_mfma_f32_16x16x32_bf16 v[56:59], v[180:183], a[88:91], v[56:59]\n\t"
  "global_load_lds_dwordx4 v221, s[44:45] offset:2048 sc1\n\t"
  "v_mul_f32 v213, v101, v213\n\t"
  "v_mfma_f32_16x16x32_bf16 v[60:63], v[176:179], a[76:79], v[60:63]\n\t"
  "v_exp_f32 v213, v213\n\t"
  "v_mul_f32 v214, v102, v214\n\t"
  "v_mfma_f32_16x16x32_bf16 v[60:63], v[180:183], a[92:95], v[60:63]\n\t"
  "s_add_u32 s44, s54, 28672\n\t"
  "s_addc_u32 s45, s55, 0\n\t"
  "s_waitcnt lgkmcnt(2)\n\t"
  "v_mfma_f32_16x16x32_bf16 v[32:35], v[160:163], a[96:99], v[32:35]\n\t"
  "ds_read_b128 v[176:179], v219 offset:40960\n\t"
  "v_mul_f32 v215, v103, v215\n\t"
  "v_mfma_f32_16x16x32_bf16 v[32:35], v[164:167], a[112:115], v[32:35]\n\t"
  "ds_read_b128 v[180:183], v219 offset:43008\n\t"
  "v_mul_f32 v116, v100, v116\n\t"
  "v_mfma_f32_16x16x32_bf16 v[36:39], v[160:163], a[100:103], v[36:39]\n\t"
  "v_exp_f32 v214, v214\n\t"
  "v_mul_f32 v117, v101, v117\n\t"
  "v_mfma_f32_16x16x32_bf16 v[36:39], v[164:167], a[116:119], v[36:39]\n\t"
  "v_exp_f32 v215, v215\n\t"
  "v_mfma_f32_16x16x32_bf16 v[40:43], v[160:163], a[104:107], v[40:43]\n\t"
  "s_add_u32 m0, s48, 28672\n\t"
  "v_mul_f32 v118, v102, v118\n\t"
  "v_mfma_f32_16x16x32_bf16 v[40:43], v[164:167], a[120:123], v[40:43]\n\t"
  "v_mul_f32 v119, v103, v119\n\t"
  "v_add_f32 v212, 1.0, v212\n\t"
  "v_mfma_f32_16x16x32_bf16 v[44:47], v[160:163], a[108:111], v[44:47]\n\t"
  "global_load_lds_dwordx4 v230, s[44:45] offset:0 sc1\n\t"
  "v_add_f32 v213, 1.0, v213\n\t"
  "v_add_f32 v214, 1.0, v214\n\t"
  "v_mfma_f32_16x16x32_bf16 v[44:47], v[164:167], a[124:127], v[44:47]\n\t"
  "v_add_f32 v215, 1.0, v215\n\t"
  "s_waitcnt lgkmcnt(2)\n\t"
  "v_mfma_f32_16x16x32_bf16 v[48:51], v[168:171], a[96:99], v[48:51]\n\t"
  "ds_read_b128 v[160:163], v219 offset:41984\n\t"
  "v_mul_f32 v208, v104, v104\n\t"
  "v_mfma_f32_16x16x32_bf16 v[48:51], v[172:175], a[112:115], v[48:51]\n\t"
  "ds_read_b128 v[164:167], v219 offset:44032\n\t"
  "v_mul_f32 v209, v105, v105\n\t"
  "v_mfma_f32_16x16x32_bf16 v[52:55], v[168:171], a[100:103], v[52:55]\n\t"
  "global_load_lds_dwordx4 v230, s[44:45] offset:1024 sc1\n\t"
  "v_mul_f32 v210, v106, v106\n\t"
  "v_mul_f32 v211, v107, v107\n\t"
  "v_mfma_f32_16x16x32_bf16 v[52:55], v[172:175], a[116:119], v[52:55]\n\t"
  "v_rcp_f32 v212, v212\n\t"
  "v_mfma_f32_16x16x32_bf16 v[56:59], v[168:171], a[104:107], v[56:59]\n\t"
  "v_rcp_f32 v213, v213\n\t"
  "v_mfma_f32_16x16x32_bf16 v[56:59], v[172:175], a[120:123], v[56:59]\n\t"
  "global_load_lds_dwordx4 v230, s[44:45] offset:2048 sc1\n\t"
  "v_mul_f32 v116, v116, v212\n\t"
  "v_mfma_f32_16x16x32_bf16 v[60:63], v[168:171], a[108:111], v[60:63]\n\t"
  "v_rcp_f32 v214, v214\n\t"
  "v_mul_f32 v117, v117, v213\n\t"
  "v_mfma_f32_16x16x32_bf16 v[60:63], v[172:175], a[124:127], v[60:63]\n\t"
  "global_load_lds_dwordx4 v230, s[44:45] offset:3072 sc1\n\t"
  "s_waitcnt lgkmcnt(2)\n\t"
  "v_mfma_f32_16x16x32_bf16 v[32:35], v[176:179], a[128:131], v[32:35]\n\t"
  "ds_read_b128 v[168:171], v219 offset:45056\n\t"
  "v_mul_f32 v118, v118, v214\n\t"
  "v_mfma_f32_16x16x32_bf16 v[32:35], v[180:183], a[144:147], v[32:35]\n\t"
  "ds_read_b128 v[172:175], v219 offset:47104\n\t"
  "v_cvt_pk_bf16_f32 v190, v116, v117\n\t"
  "v_fma_f32 v208, v208, v206, v202\n\t"
  "v_mfma_f32_16x16x32_bf16 v[36:39], v[176:179], a[132:135], v[36:39]\n\t"
  "v_rcp_f32 v215, v215\n\t"
  "v_mfma_f32_16x16x32_bf16 v[36:39], v[180:183], a[148:151], v[36:39]\n\t"
  "s_add_u32 m0, s48, 32768\n\t"
  "v_mul_f32 v119, v119, v215\n\t"
  "v_mfma_f32_16x16x32_bf16 v[40:43], v[176:179], a[136:139], v[40:43]\n\t"
  "v_cvt_pk_bf16_f32 v191, v118, v119\n\t"
  "v_fma_f32 v209, v209, v206, v202\n\t"
  "v_mfma_f32_16x16x32_bf16 v[40:43], v[180:183], a[152:155], v[40:43]\n\t"
  "global_store_dwordx4 v216, v[188:191], s[36:37] offset:0 sc1\n\t"
  "v_fma_f32 v210, v210, v206, v202\n\t"
  "v_fma_f32 v211, v211, v206, v202\n\t"
  "v_mfma_f32_16x16x32_bf16 v[44:47], v[176:179], a[140:143], v[44:47]\n\t"
  "global_load_lds_dwordx4 v231, s[44:45] offset:0 sc1\n\t"
  "v_mul_f32 v208, v104, v208\n\t"
  "v_mfma_f32_16x16x32_bf16 v[44:47], v[180:183], a[156:159], v[44:47]\n\t"
  "v_mul_f32 v209, v105, v209\n\t"
  "s_waitcnt lgkmcnt(2)\n\t"
  "v_mfma_f32_16x16x32_bf16 v[48:51], v[160:163], a[128:131], v[48:51]\n\t"
  "ds_read_b128 v[176:179], v219 offset:46080\n\t"
  "v_mul_f32 v210, v106, v210\n\t"
  "v_mfma_f32_16x16x32_bf16 v[48:51], v[164:167], a[144:147], v[48:51]\n\t"
  "ds_read_b128 v[180:183], v219 offset:48128\n\t"
  "v_mul_f32 v211, v107, v211\n\t"
  "v_mul_f32 v120, v104, v120\n\t"
  "v_mfma_f32_16x16x32_bf16 v[52:55], v[160:163], a[132:135], v[52:55]\n\t"
  "global_load_lds_dwordx4 v231, s[44:45] offset:1024 sc1\n\t"
  "v_mul_f32 v121, v105, v121\n\t"
  "v_mfma_f32_16x16x32_bf16 v[52:55], v[164:167], a[148:151], v[52:55]\n\t"
  "v_exp_f32 v208, v208\n\t"
  "v_mfma_f32_16x16x32_bf16 v[56:59], v[160:163], a[136:139], v[56:59]\n\t"
  "global_load_lds_dwordx4 v231, s[44:45] offset:2048 sc1\n\t"
  "v_mul_f32 v122, v106, v122\n\t"
  "v_mfma_f32_16x16x32_bf16 v[56:59], v[164:167], a[152:155], v[56:59]\n\t"
  "v_exp_f32 v209, v209\n\t"
  "v_mul_f32 v123, v107, v123\n\t"
  "v_mfma_f32_16x16x32_bf16 v[60:63], v[160:163], a[140:143], v[60:63]\n\t"
  "v_exp_f32 v210, v210\n\t"
  "v_mfma_f32_16x16x32_bf16 v[60:63], v[164:167], a[156:159], v[60:63]\n\t"
  "s_waitcnt vmcnt(26) lgkmcnt(2)\n\t"
  "s_barrier\n\t"
  "v_mfma_f32_16x16x32_bf16 v[32:35], v[168:171], a[160:163], v[32:35]\n\t"
  "ds_read_b128 v[160:163], v219 offset:49152\n\t"
  "v_add_f32 v208, 1.0, v208\n\t"
  "v_mfma_f32_16x16x32_bf16 v[32:35], v[172:175], a[176:179], v[32:35]\n\t"
  "ds_read_b128 v[164:167], v219 offset:51200\n\t"
  "v_add_f32 v209, 1.0, v209\n\t"
  "v_add_f32 v210, 1.0, v210\n\t"
  "v_mfma_f32_16x16x32_bf16 v[36:39], v[168:171], a[164:167], v[36:39]\n\t"
  "s_add_u32 m0, s46, 90112\n\t"
  "v_mul_f32 v212, v108, v108\n\t"
  "v_mfma_f32_16x16x32_bf16 v[36:39], v[172:175], a[180:183], v[36:39]\n\t"
  "v_exp_f32 v211, v211\n\t"
  "v_mfma_f32_16x16x32_bf16 v[40:43], v[168:171], a[168:171], v[40:43]\n\t"
  "v_rcp_f32 v208, v208\n\t"
  "v_mfma_f32_16x16x32_bf16 v[40:43], v[172:175], a[184:187], v[40:43]\n\t"
  "s_add_u32 s44, s30, 258048\n\t"
  "s_addc_u32 s45, s31, 0\n\t"
  "v_add_f32 v211, 1.0, v211\n\t"
  "v_mfma_f32_16x16x32_bf16 v[44:47], v[168:171], a[172:175], v[44:47]\n\t"
  "v_rcp_f32 v209, v209\n\t"
  "v_mfma_f32_16x16x32_bf16 v[44:47], v[172:175], a[188:191], v[44:47]\n\t"
  "v_mul_f32 v120, v120, v208\n\t"
  "s_waitcnt lgkmcnt(2)\n\t"
  "v_mfma_f32_16x16x32_bf16 v[48:51], v[176:179], a[160:163], v[48:51]\n\t"
  "ds_read_b128 v[168:171], v219 offset:50176\n\t"
  "v_mul_f32 v121, v121, v209\n\t"
  "v_mfma_f32_16x16x32_bf16 v[48:51], v[180:183], a[176:179], v[48:51]\n\t"
  "ds_read_b128 v[172:175], v219 offset:52224\n\t"
  "v_cvt_pk_bf16_f32 v194, v120, v121\n\t"
  "v_mul_f32 v213, v109, v109\n\t"
  "v_mfma_f32_16x16x32_bf16 v[52:55], v[176:179], a[164:167], v[52:55]\n\t"
  "global_load_lds_dwordx4 v221, s[44:45] offset:0 sc1\n\t"
  "v_mul_f32 v214, v110, v110\n\t"
  "v_mfma_f32_16x16x32_bf16 v[52:55], v[180:183], a[180:183], v[52:55]\n\t"
  "v_rcp_f32 v210, v210\n\t"
  "v_mfma_f32_16x16x32_bf16 v[56:59], v[176:179], a[168:171], v[56:59]\n\t"
  "v_rcp_f32 v211, v211\n\t"
  "v_mfma_f32_16x16x32_bf16 v[56:59], v[180:183], a[184:187], v[56:59]\n\t"
  "global_load_lds_dwordx4 v221, s[44:45] offset:1024 sc1\n\t"
  "v_mul_f32 v122, v122, v210\n\t"
  "v_mul_f32 v123, v123, v211\n\t"
  "v_mfma_f32_16x16x32_bf16 v[60:63], v[176:179], a[172:175], v[60:63]\n\t"
  "v_cvt_pk_bf16_f32 v195, v122, v123\n\t"
  "v_mul_f32 v215, v111, v111\n\t"
  "v_mfma_f32_16x16x32_bf16 v[60:63], v[180:183], a[188:191], v[60:63]\n\t"
  "global_store_dwordx4 v216, v[192:195], s[38:39] offset:0 sc1\n\t"
  "s_waitcnt lgkmcnt(2)\n\t"
  "v_mfma_f32_16x16x32_bf16 v[32:35], v[160:163], a[192:195], v[32:35]\n\t"
  "ds_read_b128 v[176:179], v219 offset:53248\n\t"
  "v_fma_f32 v212, v212, v207, v203\n\t"
  "v_mfma_f32_16x16x32_bf16 v[32:35], v[164:167], a[208:211], v[32:35]\n\t"
  "ds_read_b128 v[180:183], v219 offset:55296\n\t"
  "v_fma_f32 v213, v213, v207, v203\n\t"
  "v_fma_f32 v214, v214, v207, v203\n\t"
  "v_mfma_f32_16x16x32_bf16 v[36:39], v[160:163], a[196:199], v[36:39]\n\t"
  "global_load_lds_dwordx4 v221, s[44:45] offset:2048 sc1\n\t"
  "v_fma_f32 v215, v215, v207, v203\n\t"
  "v_mfma_f32_16x16x32_bf16 v[36:39], v[164:167], a[212:215], v[36:39]\n\t"
  "v_mul_f32 v212, v108, v212\n\t"
  "v_mul_f32 v213, v109, v213\n\t"
  "v_mfma_f32_16x16x32_bf16 v[40:43], v[160:163], a[200:203], v[40:43]\n\t"
  "v_exp_f32 v212, v212\n\t"
  "v_mfma_f32_16x16x32_bf16 v[40:43], v[164:167], a[216:219], v[40:43]\n\t"
  "v_exp_f32 v213, v213\n\t"
  "v_mul_f32 v214, v110, v214\n\t"
  "v_mfma_f32_16x16x32_bf16 v[44:47], v[160:163], a[204:207], v[44:47]\n\t"
  "v_exp_f32 v214, v214\n\t"
  "v_mfma_f32_16x16x32_bf16 v[44:47], v[164:167], a[220:223], v[44:47]\n\t"
  "v_mul_f32 v215, v111, v215\n\t"
  "s_waitcnt lgkmcnt(2)\n\t"
  "v_mfma_f32_16x16x32_bf16 v[48:51], v[168:171], a[192:195], v[48:51]\n\t"
  "ds_read_b128 v[160:163], v219 offset:54272\n\t"
  "v_mul_f32 v124, v108, v124\n\t"
  "v_mfma_f32_16x16x32_bf16 v[48:51], v[172:175], a[208:211], v[48:51]\n\t"
  "ds_read_b128 v[164:167], v219 offset:56320\n\t"
  "v_mul_f32 v125, v109, v125\n\t"
  "v_mul_f32 v126, v110, v126\n\t"
  "v_mfma_f32_16x16x32_bf16 v[52:55], v[168:171], a[196:199], v[52:55]\n\t"
  "v_exp_f32 v215, v215\n\t"
  "v_mfma_f32_16x16x32_bf16 v[52:55], v[172:175], a[212:215], v[52:55]\n\t"
  "v_mul_f32 v127, v111, v127\n\t"
  "v_add_f32 v212, 1.0, v212\n\t"
  "v_mfma_f32_16x16x32_bf16 v[56:59], v[168:171], a[200:203], v[56:59]\n\t"
  "v_rcp_f32 v212, v212\n\t"
  "v_mfma_f32_16x16x32_bf16 v[56:59], v[172:175], a[216:219], v[56:59]\n\t"
  "v_add_f32 v213, 1.0, v213\n\t"
  "v_add_f32 v214, 1.0, v214\n\t"
  "v_add_f32 v215, 1.0, v215\n\t"
  "v_mfma_f32_16x16x32_bf16 v[60:63], v[168:171], a[204:207], v[60:63]\n\t"
  "v_rcp_f32 v213, v213\n\t"
  "v_mfma_f32_16x16x32_bf16 v[60:63], v[172:175], a[220:223], v[60:63]\n\t"
  "v_mul_f32 v124, v124, v212\n\t"
  "s_waitcnt lgkmcnt(2)\n\t"
  "v_mfma_f32_16x16x32_bf16 v[32:35], v[176:179], a[224:227], v[32:35]\n\t"
  "ds_read_b128 v[168:171], v219 offset:57344\n\t"
  "v_mul_f32 v125, v125, v213\n\t"
  "v_mfma_f32_16x16x32_bf16 v[32:35], v[180:183], a[240:243], v[32:35]\n\t"
  "ds_read_b128 v[172:175], v219 offset:59392\n\t"
  "v_cvt_pk_bf16_f32 v198, v124, v125\n\t"
  "v_mul_f32 v208, v128, v128\n\t"
  "v_mfma_f32_16x16x32_bf16 v[36:39], v[176:179], a[228:231], v[36:39]\n\t"
  "v_rcp_f32 v214, v214\n\t"
  "v_mfma_f32_16x16x32_bf16 v[36:39], v[180:183], a[244:247], v[36:39]\n\t"
  "v_rcp_f32 v215, v215\n\t"
  "v_mfma_f32_16x16x32_bf16 v[40:43], v[176:179], a[232:235], v[40:43]\n\t"
  "v_mul_f32 v126, v126, v214\n\t"
  "v_mul_f32 v127, v127, v215\n\t"
  "v_mfma_f32_16x16x32_bf16 v[40:43], v[180:183], a[248:251], v[40:43]\n\t"
  "v_cvt_pk_bf16_f32 v199, v126, v127\n\t"
  "v_mul_f32 v209, v129, v129\n\t"
  "v_mul_f32 v210, v130, v130\n\t"
  "v_mfma_f32_16x16x32_bf16 v[44:47], v[176:179], a[236:239], v[44:47]\n\t"
  "global_store_dwordx4 v216, v[196:199], s[40:41] offset:0 sc1\n\t"
  "v_mul_f32 v211, v131, v131\n\t"
  "v_mfma_f32_16x16x32_bf16 v[44:47], v[180:183], a[252:255], v[44:47]\n\t"
  "v_fma_f32 v208, v208, v204, v200\n\t"
  "s_waitcnt lgkmcnt(2)\n\t"
  "v_mfma_f32_16x16x32_bf16 v[48:51], v[160:163], a[224:227], v[48:51]\n\t"
  "ds_read_b128 v[176:179], v219 offset:58368\n\t"
  "v_fma_f32 v209, v209, v204, v200\n\t"
  "v_mfma_f32_16x16x32_bf16 v[48:51], v[164:167], a[240:243], v[48:51]\n\t"
  "ds_read_b128 v[180:183], v219 offset:60416\n\t"
  "v_fma_f32 v210, v210, v204, v200\n\t"
  "v_fma_f32 v211, v211, v204, v200\n\t"
  "v_mfma_f32_16x16x32_bf16 v[52:55], v[160:163], a[228:231], v[52:55]\n\t"
  "v_mul_f32 v208, v128, v208\n\t"
  "v_mul_f32 v209, v129, v209\n\t"
  "v_mfma_f32_16x16x32_bf16 v[52:55], v[164:167], a[244:247], v[52:55]\n\t"
  "v_exp_f32 v208, v208\n\t"
  "v_mfma_f32_16x16x32_bf16 v[56:59], v[160:163], a[232:235], v[56:59]\n\t"
  "v_exp_f32 v209, v209\n\t"
  "v_mfma_f32_16x16x32_bf16 v[56:59], v[164:167], a[248:251], v[56:59]\n\t"
  "v_mul_f32 v210, v130, v210\n\t"
  "v_mul_f32 v211, v131, v211\n\t"
  "v_mul_f32 v144, v128, v144\n\t"
  "v_mfma_f32_16x16x32_bf16 v[60:63], v[160:163], a[236:239], v[60:63]\n\t"
  "v_exp_f32 v210, v210\n\t"
  "v_mfma_f32_16x16x32_bf16 v[60:63], v[164:167], a[252:255], v[60:63]\n\t"
  "s_waitcnt vmcnt(20) lgkmcnt(2)\n\t"
  "s_barrier\n\t"
  "v_mfma_f32_16x16x32_bf16 v[32:35], v[168:171], v[0:3], v[32:35]\n\t"
  "ds_read_b128 v[160:163], v219 offset:61440\n\t"
  "v_mul_f32 v145, v129, v145\n\t"
  "v_mfma_f32_16x16x32_bf16 v[32:35], v[172:175], v[16:19], v[32:35]\n\t"
  "ds_read_b128 v[164:167], v219 offset:63488\n\t"
  "v_mul_f32 v146, v130, v146\n\t"
  "v_mul_f32 v147, v131, v147\n\t"
  "v_mfma_f32_16x16x32_bf16 v[36:39], v[168:171], v[4:7], v[36:39]\n\t"
  "s_add_u32 m0, s46, 102400\n\t"
  "v_add_f32 v208, 1.0, v208\n\t"
  "v_mfma_f32_16x16x32_bf16 v[36:39], v[172:175], v[20:23], v[36:39]\n\t"
  "v_exp_f32 v211, v211\n\t"
  "v_mfma_f32_16x16x32_bf16 v[40:43], v[168:171], v[8:11], v[40:43]\n\t"
  "v_rcp_f32 v208, v208\n\t"
  "v_mfma_f32_16x16x32_bf16 v[40:43], v[172:175], v[24:27], v[40:43]\n\t"
  "s_add_u32 s44, s30, 270336\n\t"
  "s_addc_u32 s45, s31, 0\n\t"
  "v_add_f32 v209, 1.0, v209\n\t"
  "v_mfma_f32_16x16x32_bf16 v[44:47], v[168:171], v[12:15], v[44:47]\n\t"
  "v_rcp_f32 v209, v209\n\t"
  "v_mfma_f32_16x16x32_bf16 v[44:47], v[172:175], v[28:31], v[44:47]\n\t"
  "v_add_f32 v210, 1.0, v210\n\t"
  "s_waitcnt lgkmcnt(2)\n\t"
  "v_mfma_f32_16x16x32_bf16 v[48:51], v[176:179], v[0:3], v[48:51]\n\t"
  "ds_read_b128 v[168:171], v219 offset:62464\n\t"
  "v_add_f32 v211, 1.0, v211\n\t"
  "v_mfma_f32_16x16x32_bf16 v[48:51], v[180:183], v[16:19], v[48:51]\n\t"
  "ds_read_b128 v[172:175], v219 offset:64512\n\t"
  "v_mul_f32 v144, v144, v208\n\t"
  "v_mul_f32 v145, v145, v209\n\t"
  "v_mfma_f32_16x16x32_bf16 v[52:55], v[176:179], v[4:7], v[52:55]\n\t"
  "global_load_lds_dwordx4 v221, s[44:45] offset:0 sc1\n\t"
  "v_cvt_pk_bf16_f32 v184, v144, v145\n\t"
  "v_mfma_f32_16x16x32_bf16 v[52:55], v[180:183], v[20:23], v[52:55]\n\t"
  "v_rcp_f32 v210, v210\n\t"
  "v_mfma_f32_16x16x32_bf16 v[56:59], v[176:179], v[8:11], v[56:59]\n\t"
  "v_rcp_f32 v211, v211\n\t"
  "v_mfma_f32_16x16x32_bf16 v[56:59], v[180:183], v[24:27], v[56:59]\n\t"
  "global_load_lds_dwordx4 v221, s[44:45] offset:1024 sc1\n\t"
  "v_mul_f32 v146, v146, v210\n\t"
  "v_mul_f32 v147, v147, v211\n\t"
  "v_mfma_f32_16x16x32_bf16 v[60:63], v[176:179], v[12:15], v[60:63]\n\t"
  "v_cvt_pk_bf16_f32 v185, v146, v147\n\t"
  "v_mul_f32 v212, v132, v132\n\t"
  "v_mfma_f32_16x16x32_bf16 v[60:63], v[180:183], v[28:31], v[60:63]\n\t"
  "v_mul_f32 v213, v133, v133\n\t"
  "s_waitcnt lgkmcnt(2)\n\t"
  "v_mfma_f32_16x16x32_bf16 v[64:67], v[160:163], a[0:3], 0\n\t"
  "ds_read_b128 v[176:179], v220 offset:0\n\t"
  "v_mul_f32 v214, v134, v134\n\t"
  "v_mfma_f32_16x16x32_bf16 v[64:67], v[164:167], a[16:19], v[64:67]\n\t"
  "ds_read_b128 v[180:183], v220 offset:2048\n\t"
  "v_mul_f32 v215, v135, v135\n\t"
  "v_fma_f32 v212, v212, v205, v201\n\t"
  "v_mfma_f32_16x16x32_bf16 v[68:71], v[160:163], a[4:7], 0\n\t"
  "PG3_%=:\n\t"
  "s_cbranch_execz PG7_%=\n\t"
  "global_load_lds_dwordx4 v221, s[44:45] offset:2048 sc1\n\t"
  "v_fma_f32 v213, v213, v205, v201\n\t"
  "v_mfma_f32_16x16x32_bf16 v[68:71], v[164:167], a[20:23], v[68:71]\n\t"
  "v_fma_f32 v214, v214, v205, v201\n\t"
  "v_fma_f32 v215, v215, v205, v201\n\t"
  "v_mfma_f32_16x16x32_bf16 v[72:75], v[160:163], a[8:11], 0\n\t"
  "v_mul_f32 v212, v132, v212\n\t"
  "v_mul_f32 v213, v133, v213\n\t"
  "v_mfma_f32_16x16x32_bf16 v[72:75], v[164:167], a[24:27], v[72:75]\n\t"
  "v_exp_f32 v212, v212\n\t"
  "v_mul_f32 v214, v134, v214\n\t"
  "v_mfma_f32_16x16x32_bf16 v[76:79], v[160:163], a[12:15], 0\n\t"
  "v_exp_f32 v213, v213\n\t"
  "v_mfma_f32_16x16x32_bf16 v[76:79], v[164:167], a[28:31], v[76:79]\n\t"
  "v_mul_f32 v215, v135, v215\n\t"
  "s_waitcnt lgkmcnt(2)\n\t"
  "v_mfma_f32_16x16x32_bf16 v[80:83], v[168:171], a[0:3], 0\n\t"
  "ds_read_b128 v[160:163], v220 offset:1024\n\t"
  "v_mul_f32 v148, v132, v148\n\t"
  "v_mfma_f32_16x16x32_bf16 v[80:83], v[172:175], a[16:19], v[80:83]\n\t"
  "ds_read_b128 v[164:167], v220 offset:3072\n\t"
  "v_mul_f32 v149, v133, v149\n\t"
  "v_mul_f32 v150, v134, v150\n\t"
  "v_mfma_f32_16x16x32_bf16 v[84:87], v[168:171], a[4:7], 0\n\t"
  "v_exp_f32 v214, v214\n\t"
  "v_mfma_f32_16x16x32_bf16 v[84:87], v[172:175], a[20:23], v[84:87]\n\t"
  "v_exp_f32 v215, v215\n\t"
  "v_mfma_f32_16x16x32_bf16 v[88:91], v[168:171], a[8:11], 0\n\t"
  "v_mul_f32 v151, v135, v151\n\t"
  "v_add_f32 v212, 1.0, v212\n\t"
  "v_mfma_f32_16x16x32_bf16 v[88:91], v[172:175], a[24:27], v[88:91]\n\t"
  "v_rcp_f32 v212, v212\n\t"
  "v_add_f32 v213, 1.0, v213\n\t"
  "v_mfma_f32_16x16x32_bf16 v[92:95], v[168:171], a[12:15], 0\n\t"
  "v_rcp_f32 v213, v213\n\t"
  "v_mfma_f32_16x16x32_bf16 v[92:95], v[172:175], a[28:31], v[92:95]\n\t"
  "v_add_f32 v214, 1.0, v214\n\t"
  "s_waitcnt lgkmcnt(2)\n\t"
  "v_mfma_f32_16x16x32_bf16 v[64:67], v[176:179], a[32:35], v[64:67]\n\t"
  "ds_read_b128 v[168:171], v220 offset:4096\n\t"
  "v_add_f32 v215, 1.0, v215\n\t"
  "v_mfma_f32_16x16x32_bf16 v[64:67], v[180:183], a[48:51], v[64:67]\n\t"
  "ds_read_b128 v[172:175], v220 offset:6144\n\t"
  "v_mul_f32 v148, v148, v212\n\t"
  "v_mul_f32 v149, v149, v213\n\t"
  "v_mfma_f32_16x16x32_bf16 v[68:71], v[176:179], a[36:39], v[68:71]\n\t"
  "v_rcp_f32 v214, v214\n\t"
  "v_mfma_f32_16x16x32_bf16 v[68:71], v[180:183], a[52:55], v[68:71]\n\t"
  "v_rcp_f32 v215, v215\n\t"
  "v_mfma_f32_16x16x32_bf16 v[72:75], v[176:179], a[40:43], v[72:75]\n\t"
  "v_mul_f32 v150, v150, v214\n\t"
  "v_mul_f32 v151, v151, v215\n\t"
  "v_mfma_f32_16x16x32_bf16 v[72:75], v[180:183], a[56:59], v[72:75]\n\t"
  "v_cvt_pk_bf16_f32 v188, v148, v149\n\t"
  "v_cvt_pk_bf16_f32 v189, v150, v151\n\t"
  "v_mul_f32 v208, v136, v136\n\t"
  "v_mfma_f32_16x16x32_bf16 v[76:79], v[176:179], a[44:47], v[76:79]\n\t"
  "v_mul_f32 v209, v137, v137\n\t"
  "v_mul_f32 v210, v138, v138\n\t"
  "v_mfma_f32_16x16x32_bf16 v[76:79], v[180:183], a[60:63], v[76:79]\n\t"
  "v_mul_f32 v211, v139, v139\n\t"
  "s_waitcnt lgkmcnt(2)\n\t"
  "v_mfma_f32_16x16x32_bf16 v[80:83], v[160:163], a[32:35], v[80:83]\n\t"
  "ds_read_b128 v[176:179], v220 offset:5120\n\t"
  "v_fma_f32 v208, v208, v206, v202\n\t"
  "v_mfma_f32_16x16x32_bf16 v[80:83], v[164:167], a[48:51], v[80:83]\n\t"
  "ds_read_b128 v[180:183], v220 offset:7168\n\t"
  "v_fma_f32 v209, v209, v206, v202\n\t"
  "v_fma_f32 v210, v210, v206, v202\n\t"
  "v_mfma_f32_16x16x32_bf16 v[84:87], v[160:163], a[36:39], v[84:87]\n\t"
  "v_fma_f32 v211, v211, v206, v202\n\t"
  "v_mul_f32 v208, v136, v208\n\t"
  "v_mfma_f32_16x16x32_bf16 v[84:87], v[164:167], a[52:55], v[84:87]\n\t"
  "v_exp_f32 v208, v208\n\t"
  "v_mfma_f32_16x16x32_bf16 v[88:91], v[160:163], a[40:43], v[88:91]\n\t"
  "v_mul_f32 v209, v137, v209\n\t"
  "v_mul_f32 v210, v138, v210\n\t"
  "v_mfma_f32_16x16x32_bf16 v[88:91], v[164:167], a[56:59], v[88:91]\n\t"
  "v_exp_f32 v209, v209\n\t"
  "v_mul_f32 v211, v139, v211\n\t"
  "v_mfma_f32_16x16x32_bf16 v[92:95], v[160:163], a[44:47], v[92:95]\n\t"
  "v_exp_f32 v210, v210\n\t"
  "v_mfma_f32_16x16x32_bf16 v[92:95], v[164:167], a[60:63], v[92:95]\n\t"
  "s_waitcnt vmcnt(20) lgkmcnt(2)\n\t"
  "s_barrier\n\t"
  "v_mfma_f32_16x16x32_bf16 v[64:67], v[168:171], a[64:67], v[64:67]\n\t"
  "ds_read_b128 v[160:163], v220 offset:8192\n\t"
  "v_mul_f32 v152, v136, v152\n\t"
  "v_mfma_f32_16x16x32_bf16 v[64:67], v[172:175], a[80:83], v[64:67]\n\t"
  "ds_read_b128 v[164:167], v220 offset:10240\n\t"
  "v_mul_f32 v153, v137, v153\n\t"
  "v_mul_f32 v154, v138, v154\n\t"
  "v_mfma_f32_16x16x32_bf16 v[68:71], v[168:171], a[68:71], v[68:71]\n\t"
  "s_add_u32 m0, s46, 114688\n\t"
  "v_mul_f32 v155, v139, v155\n\t"
  "v_mfma_f32_16x16x32_bf16 v[68:71], v[172:175], a[84:87], v[68:71]\n\t"
  "v_exp_f32 v211, v211\n\t"
  "v_mfma_f32_16x16x32_bf16 v[72:75], v[168:171], a[72:75], v[72:75]\n\t"
  "s_add_u32 s44, s30, 282624\n\t"
  "s_addc_u32 s45, s31, 0\n\t"
  "v_mfma_f32_16x16x32_bf16 v[72:75], v[172:175], a[88:91], v[72:75]\n\t"
  "v_add_f32 v208, 1.0, v208\n\t"
  "v_add_f32 v209, 1.0, v209\n\t"
  "v_add_f32 v210, 1.0, v210\n\t"
  "v_mfma_f32_16x16x32_bf16 v[76:79], v[168:171], a[76:79], v[76:79]\n\t"
  "global_load_lds_dwordx4 v221, s[44:45] offset:0 sc1\n\t"
  "v_add_f32 v211, 1.0, v211\n\t"
  "v_mfma_f32_16x16x32_bf16 v[76:79], v[172:175], a[92:95], v[76:79]\n\t"
  "v_mul_f32 v212, v140, v140\n\t"
  "s_waitcnt lgkmcnt(2)\n\t"
  "v_mfma_f32_16x16x32_bf16 v[80:83], v[176:179], a[64:67], v[80:83]\n\t"
  "ds_read_b128 v[168:171], v220 offset:9216\n\t"
  "v_mul_f32 v213, v141, v141\n\t"
  "v_mfma_f32_16x16x32_bf16 v[80:83], v[180:183], a[80:83], v[80:83]\n\t"
  "ds_read_b128 v[172:175], v220 offset:11264\n\t"
  "v_mul_f32 v214, v142, v142\n\t"
  "v_mul_f32 v215, v143, v143\n\t"
  "v_mfma_f32_16x16x32_bf16 v[84:87], v[176:179], a[68:71], v[84:87]\n\t"
  "v_rcp_f32 v208, v208\n\t"
  "v_mfma_f32_16x16x32_bf16 v[84:87], v[180:183], a[84:87], v[84:87]\n\t"
  "global_load_lds_dwordx4 v221, s[44:45] offset:1024 sc1\n\t"
  "v_mul_f32 v152, v152, v208\n\t"
  "v_mfma_f32_16x16x32_bf16 v[88:91], v[176:179], a[72:75], v[88:91]\n\t"
  "v_rcp_f32 v209, v209\n\t"
  "v_mfma_f32_16x16x32_bf16 v[88:91], v[180:183], a[88:91], v[88:91]\n\t"
  "global_load_lds_dwordx4 v221, s[44:45] offset:2048 sc1\n\t"
  "v_mul_f32 v153, v153, v209\n\t"
  "v_cvt_pk_bf16_f32 v192, v152, v153\n\t"
  "v_mfma_f32_16x16x32_bf16 v[92:95], v[176:179], a[76:79], v[92:95]\n\t"
  "v_rcp_f32 v210, v210\n\t"
  "v_mfma_f32_16x16x32_bf16 v[92:95], v[180:183], a[92:95], v[92:95]\n\t"
  "s_add_u32 s44, s54, 57344\n\t"
  "s_addc_u32 s45, s55, 0\n\t"
  "s_waitcnt lgkmcnt(2)\n\t"
  "v_mfma_f32_16x16x32_bf16 v[64:67], v[160:163], a[96:99], v[64:67]\n\t"
  "ds_read_b128 v[176:179], v220 offset:12288\n\t"
  "v_mul_f32 v154, v154, v210\n\t"
  "v_mfma_f32_16x16x32_bf16 v[64:67], v[164:167], a[112:115], v[64:67]\n\t"
  "ds_read_b128 v[180:183], v220 offset:14336\n\t"
  "v_fma_f32 v212, v212, v207, v203\n\t"
  "v_fma_f32 v213, v213, v207, v203\n\t"
  "v_mfma_f32_16x16x32_bf16 v[68:71], v[160:163], a[100:103], v[68:71]\n\t"
  "v_rcp_f32 v211, v211\n\t"
  "v_mfma_f32_16x16x32_bf16 v[68:71], v[164:167], a[116:119], v[68:71]\n\t"
  "v_mul_f32 v155, v155, v211\n\t"
  "v_cvt_pk_bf16_f32 v193, v154, v155\n\t"
  "v_mfma_f32_16x16x32_bf16 v[72:75], v[160:163], a[104:107], v[72:75]\n\t"
  "s_add_u32 m0, s48, 57344\n\t"
  "v_fma_f32 v214, v214, v207, v203\n\t"
  "v_mfma_f32_16x16x32_bf16 v[72:75], v[164:167], a[120:123], v[72:75]\n\t"
  "v_fma_f32 v215, v215, v207, v203\n\t"
  "v_mul_f32 v212, v140, v212\n\t"
  "v_mfma_f32_16x16x32_bf16 v[76:79], v[160:163], a[108:111], v[76:79]\n\t"
  "global_load_lds_dwordx4 v230, s[44:45] offset:0 sc1\n\t"
  "v_mul_f32 v213, v141, v213\n\t"
  "v_mul_f32 v214, v142, v214\n\t"
  "v_mfma_f32_16x16x32_bf16 v[76:79], v[164:167], a[124:127], v[76:79]\n\t"
  "v_mul_f32 v215, v143, v215\n\t"
  "s_waitcnt lgkmcnt(2)\n\t"
  "v_mfma_f32_16x16x32_bf16 v[80:83], v[168:171], a[96:99], v[80:83]\n\t"
  "ds_read_b128 v[160:163], v220 offset:13312\n\t"
  "v_mul_f32 v156, v140, v156\n\t"
  "v_mfma_f32_16x16x32_bf16 v[80:83], v[172:175], a[112:115], v[80:83]\n\t"
  "ds_read_b128 v[164:167], v220 offset:15360\n\t"
  "v_mul_f32 v157, v141, v157\n\t"
  "v_mfma_f32_16x16x32_bf16 v[84:87], v[168:171], a[100:103], v[84:87]\n\t"
  "global_load_lds_dwordx4 v230, s[44:45] offset:1024 sc1\n\t"
  "v_mul_f32 v158, v142, v158\n\t"
  "v_mul_f32 v159, v143, v159\n\t"
  "v_mfma_f32_16x16x32_bf16 v[84:87], v[172:175], a[116:119], v[84:87]\n\t"
  "v_exp_f32 v212, v212\n\t"
  "v_mfma_f32_16x16x32_bf16 v[88:91], v[168:171], a[104:107], v[88:91]\n\t"
  "v_exp_f32 v213, v213\n\t"
  "v_mfma_f32_16x16x32_bf16 v[88:91], v[172:175], a[120:123], v[88:91]\n\t"
  "global_load_lds_dwordx4 v230, s[44:45] offset:2048 sc1\n\t"
  "v_add_f32 v212, 1.0, v212\n\t"
  "v_mfma_f32_16x16x32_bf16 v[92:95], v[168:171], a[108:111], v[92:95]\n\t"
  "v_exp_f32 v214, v214\n\t"
  "v_add_f32 v213, 1.0, v213\n\t"
  "v_mfma_f32_16x16x32_bf16 v[92:95], v[172:175], a[124:127], v[92:95]\n\t"
  "global_load_lds_dwordx4 v230, s[44:45] offset:3072 sc1\n\t"
  "s_waitcnt lgkmcnt(2)\n\t"
  "v_mfma_f32_16x16x32_bf16 v[64:67], v[176:179], a[128:131], v[64:67]\n\t"
  "ds_read_b128 v[168:171], v220 offset:16384\n\t"
  "v_add_f32 v214, 1.0, v214\n\t"
  "v_mfma_f32_16x16x32_bf16 v[64:67], v[180:183], a[144:147], v[64:67]\n\t"
  "ds_read_b128 v[172:175], v220 offset:18432\n\t"
  "v_mul_f32 v208, v32, v32\n\t"
  "v_mfma_f32_16x16x32_bf16 v[68:71], v[176:179], a[132:135], v[68:71]\n\t"
  "v_exp_f32 v215, v215\n\t"
  "v_mul_f32 v209, v33, v33\n\t"
  "v_mfma_f32_16x16x32_bf16 v[68:71], v[180:183], a[148:151], v[68:71]\n\t"
  "s_add_u32 m0, s48, 61440\n\t"
  "v_add_f32 v215, 1.0, v215\n\t"
  "v_mfma_f32_16x16x32_bf16 v[72:75], v[176:179], a[136:139], v[72:75]\n\t"
  "v_rcp_f32 v212, v212\n\t"
  "v_mfma_f32_16x16x32_bf16 v[72:75], v[180:183], a[152:155], v[72:75]\n\t"
  "v_rcp_f32 v213, v213\n\t"
  "v_mfma_f32_16x16x32_bf16 v[76:79], v[176:179], a[140:143], v[76:79]\n\t"
  "global_load_lds_dwordx4 v231, s[44:45] offset:0 sc1\n\t"
  "v_mul_f32 v156, v156, v212\n\t"
  "v_mul_f32 v157, v157, v213\n\t"
  "v_mfma_f32_16x16x32_bf16 v[76:79], v[180:183], a[156:159], v[76:79]\n\t"
  "v_cvt_pk_bf16_f32 v196, v156, v157\n\t"
  "s_waitcnt lgkmcnt(2)\n\t"
  "v_mfma_f32_16x16x32_bf16 v[80:83], v[160:163], a[128:131], v[80:83]\n\t"
  "ds_read_b128 v[176:179], v220 offset:17408\n\t"
  "v_mul_f32 v210, v34, v34\n\t"
  "v_mfma_f32_16x16x32_bf16 v[80:83], v[164:167], a[144:147], v[80:83]\n\t"
  "ds_read_b128 v[180:183], v220 offset:19456\n\t"
  "v_mul_f32 v211, v35, v35\n\t"
  "v_mfma_f32_16x16x32_bf16 v[84:87], v[160:163], a[132:135], v[84:87]\n\t"
  "global_load_lds_dwordx4 v231, s[44:45] offset:1024 sc1\n\t"
  "v_fma_f32 v208, v208, v204, v200\n\t"
  "v_fma_f32 v209, v209, v204, v200\n\t"
  "v_mfma_f32_16x16x32_bf16 v[84:87], v[164:167], a[148:151], v[84:87]\n\t"
  "v_rcp_f32 v214, v214\n\t"
  "v_mfma_f32_16x16x32_bf16 v[88:91], v[160:163], a[136:139], v[88:91]\n\t"
  "global_load_lds_dwordx4 v231, s[44:45] offset:2048 sc1\n\t"
  "v_mul_f32 v158, v158, v214\n\t"
  "v_mfma_f32_16x16x32_bf16 v[88:91], v[164:167], a[152:155], v[88:91]\n\t"
  "v_rcp_f32 v215, v215\n\t"
  "v_mfma_f32_16x16x32_bf16 v[92:95], v[160:163], a[140:143], v[92:95]\n\t"
  "v_mul_f32 v159, v159, v215\n\t"
  "v_cvt_pk_bf16_f32 v197, v158, v159\n\t"
  "v_mfma_f32_16x16x32_bf16 v[92:95], v[164:167], a[156:159], v[92:95]\n\t"
  "v_fma_f32 v210, v210, v204, v200\n\t"
  "s_waitcnt vmcnt(26) lgkmcnt(2)\n\t"
  "s_barrier\n\t"
  "v_mfma_f32_16x16x32_bf16 v[64:67], v[168:171], a[160:163], v[64:67]\n\t"
  "ds_read_b128 v[160:163], v220 offset:20480\n\t"
  "v_fma_f32 v211, v211, v204, v200\n\t"
  "v_mfma_f32_16x16x32_bf16 v[64:67], v[172:175], a[176:179], v[64:67]\n\t"
  "ds_read_b128 v[164:167], v220 offset:22528\n\t"
  "v_mul_f32 v208, v32, v208\n\t"
  "v_mfma_f32_16x16x32_bf16 v[68:71], v[168:171], a[164:167], v[68:71]\n\t"
  "s_add_u32 m0, s46, 126976\n\t"
  "v_mul_f32 v209, v33, v209\n\t"
  "v_mfma_f32_16x16x32_bf16 v[68:71], v[172:175], a[180:183], v[68:71]\n\t"
  "v_exp_f32 v208, v208\n\t"
  "v_mul_f32 v210, v34, v210\n\t"
  "v_mfma_f32_16x16x32_bf16 v[72:75], v[168:171], a[168:171], v[72:75]\n\t"
  "v_exp_f32 v209, v209\n\t"
  "v_mfma_f32_16x16x32_bf16 v[72:75], v[172:175], a[184:187], v[72:75]\n\t"
  "s_add_u32 s44, s94, 294912\n\t"
  "s_addc_u32 s45, s95, 0\n\t"
  "v_mfma_f32_16x16x32_bf16 v[76:79], v[168:171], a[172:175], v[76:79]\n\t"
  "v_exp_f32 v210, v210\n\t"
  "v_mfma_f32_16x16x32_bf16 v[76:79], v[172:175], a[188:191], v[76:79]\n\t"
  "v_mul_f32 v211, v35, v211\n\t"
  "v_mul_f32 v48, v32, v48\n\t"
  "s_waitcnt lgkmcnt(2)\n\t"
  "v_mfma_f32_16x16x32_bf16 v[80:83], v[176:179], a[160:163], v[80:83]\n\t"
  "ds_read_b128 v[168:171], v220 offset:21504\n\t"
  "v_mul_f32 v49, v33, v49\n\t"
  "v_mfma_f32_16x16x32_bf16 v[80:83], v[180:183], a[176:179], v[80:83]\n\t"
  "ds_read_b128 v[172:175], v220 offset:23552\n\t"
  "v_mul_f32 v50, v34, v50\n\t"
  "v_mfma_f32_16x16x32_bf16 v[84:87], v[176:179], a[164:167], v[84:87]\n\t"
  "global_load_lds_dwordx4 v221, s[44:45] offset:0 sc1\n\t"
  "v_mul_f32 v51, v35, v51\n\t"
  "v_mfma_f32_16x16x32_bf16 v[84:87], v[180:183], a[180:183], v[84:87]\n\t"
  "v_exp_f32 v211, v211\n\t"
  "v_add_f32 v208, 1.0, v208\n\t"
  "v_mfma_f32_16x16x32_bf16 v[88:91], v[176:179], a[168:171], v[88:91]\n\t"
  "v_rcp_f32 v208, v208\n\t"
  "v_mfma_f32_16x16x32_bf16 v[88:91], v[180:183], a[184:187], v[88:91]\n\t"
  "global_load_lds_dwordx4 v221, s[44:45] offset:1024 sc1\n\t"
  "v_add_f32 v209, 1.0, v209\n\t"
  "v_mfma_f32_16x16x32_bf16 v[92:95], v[176:179], a[172:175], v[92:95]\n\t"
  "v_rcp_f32 v209, v209\n\t"
  "v_mfma_f32_16x16x32_bf16 v[92:95], v[180:183], a[188:191], v[92:95]\n\t"
  "v_add_f32 v210, 1.0, v210\n\t"
  "v_add_f32 v211, 1.0, v211\n\t"
  "s_waitcnt lgkmcnt(2)\n\t"
  "v_mfma_f32_16x16x32_bf16 v[64:67], v[160:163], a[192:195], v[64:67]\n\t"
  "ds_read_b128 v[176:179], v220 offset:24576\n\t"
  "v_mul_f32 v48, v48, v208\n\t"
  "v_mfma_f32_16x16x32_bf16 v[64:67], v[164:167], a[208:211], v[64:67]\n\t"
  "ds_read_b128 v[180:183], v220 offset:26624\n\t"
  "v_mul_f32 v49, v49, v209\n\t"
  "v_mfma_f32_16x16x32_bf16 v[68:71], v[160:163], a[196:199], v[68:71]\n\t"
  "global_load_lds_dwordx4 v221, s[44:45] offset:2048 sc1\n\t"
  "v_cvt_pk_bf16_f32 v186, v48, v49\n\t"
  "v_mfma_f32_16x16x32_bf16 v[68:71], v[164:167], a[212:215], v[68:71]\n\t"
  "v_rcp_f32 v210, v210\n\t"
  "v_mfma_f32_16x16x32_bf16 v[72:75], v[160:163], a[200:203], v[72:75]\n\t"
  "v_rcp_f32 v211, v211\n\t"
  "v_mul_f32 v50, v50, v210\n\t"
  "v_mfma_f32_16x16x32_bf16 v[72:75], v[164:167], a[216:219], v[72:75]\n\t"
  "v_mul_f32 v51, v51, v211\n\t"
  "v_cvt_pk_bf16_f32 v187, v50, v51\n\t"
  "v_mfma_f32_16x16x32_bf16 v[76:79], v[160:163], a[204:207], v[76:79]\n\t"
  "global_store_dwordx4 v216, v[184:187], s[34:35] offset:1024 sc1\n\t"
  "v_mul_f32 v212, v36, v36\n\t"
  "v_mfma_f32_16x16x32_bf16 v[76:79], v[164:167], a[220:223], v[76:79]\n\t"
  "v_mul_f32 v213, v37, v37\n\t"
  "s_waitcnt lgkmcnt(2)\n\t"
  "v_mfma_f32_16x16x32_bf16 v[80:83], v[168:171], a[192:195], v[80:83]\n\t"
  "ds_read_b128 v[160:163], v220 offset:25600\n\t"
  "v_mul_f32 v214, v38, v38\n\t"
  "v_mul_f32 v215, v39, v39\n\t"
  "v_mfma_f32_16x16x32_bf16 v[80:83], v[172:175], a[208:211], v[80:83]\n\t"
  "ds_read_b128 v[164:167], v220 offset:27648\n\t"
  "v_fma_f32 v212, v212, v205, v201\n\t"
  "v_mfma_f32_16x16x32_bf16 v[84:87], v[168:171], a[196:199], v[84:87]\n\t"
  "v_fma_f32 v213, v213, v205, v201\n\t"
  "v_fma_f32 v214, v214, v205, v201\n\t"
  "v_mfma_f32_16x16x32_bf16 v[84:87], v[172:175], a[212:215], v[84:87]\n\t"
  "v_fma_f32 v215, v215, v205, v201\n\t"
  "v_mul_f32 v212, v36, v212\n\t"
  "v_mfma_f32_16x16x32_bf16 v[88:91], v[168:171], a[200:203], v[88:91]\n\t"
  "v_exp_f32 v212, v212\n\t"
  "v_mul_f32 v213, v37, v213\n\t"
  "v_mfma_f32_16x16x32_bf16 v[88:91], v[172:175], a[216:219], v[88:91]\n\t"
  "v_exp_f32 v213, v213\n\t"
  "v_mfma_f32_16x16x32_bf16 v[92:95], v[168:171], a[204:207], v[92:95]\n\t"
  "v_mul_f32 v214, v38, v214\n\t"
  "v_mul_f32 v215, v39, v215\n\t"
  "v_mfma_f32_16x16x32_bf16 v[92:95], v[172:175], a[220:223], v[92:95]\n\t"
  "v_mul_f32 v52, v36, v52\n\t"
  "s_waitcnt lgkmcnt(2)\n\t"
  "v_mfma_f32_16x16x32_bf16 v[64:67], v[176:179], a[224:227], v[64:67]\n\t"
  "ds_read_b128 v[168:171], v220 offset:28672\n\t"
  "v_mul_f32 v53, v37, v53\n\t"
  "v_mul_f32 v54, v38, v54\n\t"
  "v_mfma_f32_16x16x32_bf16 v[64:67], v[180:183], a[240:243], v[64:67]\n\t"
  "ds_read_b128 v[172:175], v220 offset:30720\n\t"
  "v_mul_f32 v55, v39, v55\n\t"
  "v_mfma_f32_16x16x32_bf16 v[68:71], v[176:179], a[228:231], v[68:71]\n\t"
  "v_exp_f32 v214, v214\n\t"
  "v_mfma_f32_16x16x32_bf16 v[68:71], v[180:183], a[244:247], v[68:71]\n\t"
  "v_exp_f32 v215, v215\n\t"
  "v_mfma_f32_16x16x32_bf16 v[72:75], v[176:179], a[232:235], v[72:75]\n\t"
  "v_add_f32 v212, 1.0, v212\n\t"
  "v_add_f32 v213, 1.0, v213\n\t"
  "v_mfma_f32_16x16x32_bf16 v[72:75], v[180:183], a[248:251], v[72:75]\n\t"
  "v_rcp_f32 v212, v212\n\t"
  "v_add_f32 v214, 1.0, v214\n\t"
  "v_mfma_f32_16x16x32_bf16 v[76:79], v[176:179], a[236:239], v[76:79]\n\t"
  "v_rcp_f32 v213, v213\n\t"
  "v_mfma_f32_16x16x32_bf16 v[76:79], v[180:183], a[252:255], v[76:79]\n\t"
  "v_add_f32 v215, 1.0, v215\n\t"
  "s_waitcnt lgkmcnt(2)\n\t"
  "v_mfma_f32_16x16x32_bf16 v[80:83], v[160:163], a[224:227], v[80:83]\n\t"
  "ds_read_b128 v[176:179], v220 offset:29696\n\t"
  "v_mul_f32 v52, v52, v212\n\t"
  "v_mfma_f32_16x16x32_bf16 v[80:83], v[164:167], a[240:243], v[80:83]\n\t"
  "ds_read_b128 v[180:183], v220 offset:31744\n\t"
  "v_mul_f32 v53, v53, v213\n\t"
  "v_cvt_pk_bf16_f32 v190, v52, v53\n\t"
  "v_mfma_f32_16x16x32_bf16 v[84:87], v[160:163], a[228:231], v[84:87]\n\t"
  "v_rcp_f32 v214, v214\n\t"
  "v_mfma_f32_16x16x32_bf16 v[84:87], v[164:167], a[244:247], v[84:87]\n\t"
  "v_rcp_f32 v215, v215\n\t"
  "v_mfma_f32_16x16x32_bf16 v[88:91], v[160:163], a[232:235], v[88:91]\n\t"
  "v_mul_f32 v54, v54, v214\n\t"
  "v_mul_f32 v55, v55, v215\n\t"
  "v_mfma_f32_16x16x32_bf16 v[88:91], v[164:167], a[248:251], v[88:91]\n\t"
  "s_mul_i32 s14, s16, 1\n\t"
  "s_add_u32 s26, s58, s14\n\t"
  "s_addc_u32 s27, s59, 0\n\t"
  "v_mfma_f32_16x16x32_bf16 v[92:95], v[160:163], a[236:239], v[92:95]\n\t"
  "v_cvt_pk_bf16_f32 v191, v54, v55\n\t"
  "v_mul_f32 v208, v40, v40\n\t"
  "v_mul_f32 v209, v41, v41\n\t"
  "v_mfma_f32_16x16x32_bf16 v[92:95], v[164:167], a[252:255], v[92:95]\n\t"
  "s_waitcnt vmcnt(17) lgkmcnt(2)\n\t"
  "s_barrier\n\t"
  "v_mfma_f32_16x16x32_bf16 v[64:67], v[168:171], v[0:3], v[64:67]\n\t"
  "ds_read_b128 v[160:163], v219 offset:24576\n\t"
  "global_store_dwordx4 v216, v[188:191], s[36:37] offset:1024 sc1\n\t"
  "v_mfma_f32_16x16x32_bf16 v[64:67], v[172:175], v[16:19], v[64:67]\n\t"
  "ds_read_b128 v[164:167], v219 offset:26624\n\t"
  "v_mul_f32 v210, v42, v42\n\t"
  "v_mfma_f32_16x16x32_bf16 v[68:71], v[168:171], v[4:7], v[68:71]\n\t"
  "global_store_dword v236, v244, s[26:27] offset:0 sc1\n\t"
  "s_add_u32 m0, s46, 139264\n\t"
  "v_mul_f32 v211, v43, v43\n\t"
  "v_mfma_f32_16x16x32_bf16 v[68:71], v[172:175], v[20:23], v[68:71]\n\t"
  "v_fma_f32 v208, v208, v206, v202\n\t"
  "v_fma_f32 v209, v209, v206, v202\n\t"
  "v_mfma_f32_16x16x32_bf16 v[72:75], v[168:171], v[8:11], v[72:75]\n\t"
  "v_fma_f32 v210, v210, v206, v202\n\t"
  "v_fma_f32 v211, v211, v206, v202\n\t"
  "v_mfma_f32_16x16x32_bf16 v[72:75], v[172:175], v[24:27], v[72:75]\n\t"
  "s_add_u32 s44, s94, 307200\n\t"
  "s_addc_u32 s45, s95, 0\n\t"
  "v_mfma_f32_16x16x32_bf16 v[76:79], v[168:171], v[12:15], v[76:79]\n\t"
  "v_mul_f32 v208, v40, v208\n\t"
  "v_mul_f32 v209, v41, v209\n\t"
  "v_mul_f32 v210, v42, v210\n\t"
  "v_mfma_f32_16x16x32_bf16 v[76:79], v[172:175], v[28:31], v[76:79]\n\t"
  "v_mul_f32 v211, v43, v211\n\t"
  "s_waitcnt lgkmcnt(2)\n\t"
  "v_mfma_f32_16x16x32_bf16 v[80:83], v[176:179], v[0:3], v[80:83]\n\t"
  "ds_read_b128 v[168:171], v219 offset:25600\n\t"
  "v_mul_f32 v56, v40, v56\n\t"
  "v_mfma_f32_16x16x32_bf16 v[80:83], v[180:183], v[16:19], v[80:83]\n\t"
  "ds_read_b128 v[172:175], v219 offset:27648\n\t"
  "v_mul_f32 v57, v41, v57\n\t"
  "v_mfma_f32_16x16x32_bf16 v[84:87], v[176:179], v[4:7], v[84:87]\n\t"
  "global_load_lds_dwordx4 v221, s[44:45] offset:0 sc1\n\t"
  "v_mul_f32 v58, v42, v58\n\t"
  "v_mul_f32 v59, v43, v59\n\t"
  "v_mfma_f32_16x16x32_bf16 v[84:87], v[180:183], v[20:23], v[84:87]\n\t"
  "v_exp_f32 v208, v208\n\t"
  "v_mfma_f32_16x16x32_bf16 v[88:91], v[176:179], v[8:11], v[88:91]\n\t"
  "v_exp_f32 v209, v209\n\t"
  "v_mfma_f32_16x16x32_bf16 v[88:91], v[180:183], v[24:27], v[88:91]\n\t"
  "global_load_lds_dwordx4 v221, s[44:45] offset:1024 sc1\n\t"
  "v_add_f32 v208, 1.0, v208\n\t"
  "v_mfma_f32_16x16x32_bf16 v[92:95], v[176:179], v[12:15], v[92:95]\n\t"
  "v_exp_f32 v210, v210\n\t"
  "v_mfma_f32_16x16x32_bf16 v[92:95], v[180:183], v[28:31], v[92:95]\n\t"
  "v_add_f32 v209, 1.0, v209\n\t"
  "v_add_f32 v210, 1.0, v210\n\t"
  "s_waitcnt lgkmcnt(2)\n\t"
  "v_mfma_f32_16x16x32_bf16 v[96:99], v[160:163], a[0:3], 0\n\t"
  "ds_read_b128 v[176:179], v219 offset:28672\n\t"
  "v_mul_f32 v212, v44, v44\n\t"
  "v_mfma_f32_16x16x32_bf16 v[96:99], v[164:167], a[16:19], v[96:99]\n\t"
  "ds_read_b128 v[180:183], v219 offset:30720\n\t"
  "v_mul_f32 v213, v45, v45\n\t"
  "v_mfma_f32_16x16x32_bf16 v[100:103], v[160:163], a[4:7], 0\n\t"
  "global_load_lds_dwordx4 v221, s[44:45] offset:2048 sc1\n\t"
  "v_mul_f32 v214, v46, v46\n\t"
  "v_mfma_f32_16x16x32_bf16 v[100:103], v[164:167], a[20:23], v[100:103]\n\t"
  "v_exp_f32 v211, v211\n\t"
  "v_mul_f32 v215, v47, v47\n\t"
  "v_mfma_f32_16x16x32_bf16 v[104:107], v[160:163], a[8:11], 0\n\t"
  "v_rcp_f32 v208, v208\n\t"
  "v_mfma_f32_16x16x32_bf16 v[104:107], v[164:167], a[24:27], v[104:107]\n\t"
  "v_rcp_f32 v209, v209\n\t"
  "v_mfma_f32_16x16x32_bf16 v[108:111], v[160:163], a[12:15], 0\n\t"
  "v_rcp_f32 v210, v210\n\t"
  "v_mfma_f32_16x16x32_bf16 v[108:111], v[164:167], a[28:31], v[108:111]\n\t"
  "v_add_f32 v211, 1.0, v211\n\t"
  "v_mul_f32 v56, v56, v208\n\t"
  "s_waitcnt lgkmcnt(2)\n\t"
  "v_mfma_f32_16x16x32_bf16 v[112:115], v[168:171], a[0:3], 0\n\t"
  "ds_read_b128 v[160:163], v219 offset:29696\n\t"
  "v_mul_f32 v57, v57, v209\n\t"
  "v_mfma_f32_16x16x32_bf16 v[112:115], v[172:175], a[16:19], v[112:115]\n\t"
  "ds_read_b128 v[164:167], v219 offset:31744\n\t"
  "v_mul_f32 v58, v58, v210\n\t"
  "v_mfma_f32_16x16x32_bf16 v[116:119], v[168:171], a[4:7], 0\n\t"
  "v_rcp_f32 v211, v211\n\t"
  "v_mfma_f32_16x16x32_bf16 v[116:119], v[172:175], a[20:23], v[116:119]\n\t"
  "v_mul_f32 v59, v59, v211\n\t"
  "v_cvt_pk_bf16_f32 v194, v56, v57\n\t"
  "v_mfma_f32_16x16x32_bf16 v[120:123], v[168:171], a[8:11], 0\n\t"
  "v_cvt_pk_bf16_f32 v195, v58, v59\n\t"
  "v_fma_f32 v212, v212, v207, v203\n\t"
  "v_fma_f32 v213, v213, v207, v203\n\t"
  "v_mfma_f32_16x16x32_bf16 v[120:123], v[172:175], a[24:27], v[120:123]\n\t"
  "global_store_dwordx4 v216, v[192:195], s[38:39] offset:1024 sc1\n\t"
  "v_fma_f32 v214, v214, v207, v203\n\t"
  "v_mfma_f32_16x16x32_bf16 v[124:127], v[168:171], a[12:15], 0\n\t"
  "v_fma_f32 v215, v215, v207, v203\n\t"
  "v_mul_f32 v212, v44, v212\n\t"
  "v_mfma_f32_16x16x32_bf16 v[124:127], v[172:175], a[28:31], v[124:127]\n\t"
  "v_mul_f32 v213, v45, v213\n\t"
  "s_waitcnt lgkmcnt(2)\n\t"
  "v_mfma_f32_16x16x32_bf16 v[96:99], v[176:179], a[32:35], v[96:99]\n\t"
  "ds_read_b128 v[168:171], v219 offset:32768\n\t"
  "v_mul_f32 v214, v46, v214\n\t"
  "v_mul_f32 v215, v47, v215\n\t"
  "v_mfma_f32_16x16x32_bf16 v[96:99], v[180:183], a[48:51], v[96:99]\n\t"
  "ds_read_b128 v[172:175], v219 offset:34816\n\t"
  "v_mul_f32 v60, v44, v60\n\t"
  "v_mfma_f32_16x16x32_bf16 v[100:103], v[176:179], a[36:39], v[100:103]\n\t"
  "v_exp_f32 v212, v212\n\t"
  "v_mfma_f32_16x16x32_bf16 v[100:103], v[180:183], a[52:55], v[100:103]\n\t"
  "v_exp_f32 v213, v213\n\t"
  "v_mfma_f32_16x16x32_bf16 v[104:107], v[176:179], a[40:43], v[104:107]\n\t"
  "v_exp_f32 v214, v214\n\t"
  "v_mul_f32 v61, v45, v61\n\t"
  "v_mfma_f32_16x16x32_bf16 v[104:107], v[180:183], a[56:59], v[104:107]\n\t"
  "v_exp_f32 v215, v215\n\t"
  "v_mfma_f32_16x16x32_bf16 v[108:111], v[176:179], a[44:47], v[108:111]\n\t"
  "v_mul_f32 v62, v46, v62\n\t"
  "v_mul_f32 v63, v47, v63\n\t"
  "v_mfma_f32_16x16x32_bf16 v[108:111], v[180:183], a[60:63], v[108:111]\n\t"
  "v_add_f32 v212, 1.0, v212\n\t"
  "s_waitcnt lgkmcnt(2)\n\t"
  "v_mfma_f32_16x16x32_bf16 v[112:115], v[160:163], a[32:35], v[112:115]\n\t"
  "ds_read_b128 v[176:179], v219 offset:33792\n\t"
  "v_add_f32 v213, 1.0, v213\n\t"
  "v_mfma_f32_16x16x32_bf16 v[112:115], v[164:167], a[48:51], v[112:115]\n\t"
  "ds_read_b128 v[180:183], v219 offset:35840\n\t"
  "v_add_f32 v214, 1.0, v214\n\t"
  "v_add_f32 v215, 1.0, v215\n\t"
  "v_mfma_f32_16x16x32_bf16 v[116:119], v[160:163], a[36:39], v[116:119]\n\t"
  "v_rcp_f32 v212, v212\n\t"
  "v_mfma_f32_16x16x32_bf16 v[116:119], v[164:167], a[52:55], v[116:119]\n\t"
  "v_rcp_f32 v213, v213\n\t"
  "v_mfma_f32_16x16x32_bf16 v[120:123], v[160:163], a[40:43], v[120:123]\n\t"
  "v_rcp_f32 v214, v214\n\t"
  "v_mfma_f32_16x16x32_bf16 v[120:123], v[164:167], a[56:59], v[120:123]\n\t"
  "v_rcp_f32 v215, v215\n\t"
  "v_mul_f32 v60, v60, v212\n\t"
  "v_mfma_f32_16x16x32_bf16 v[124:127], v[160:163], a[44:47], v[124:127]\n\t"
  "v_mul_f32 v61, v61, v213\n\t"
  "v_mul_f32 v62, v62, v214\n\t"
  "v_mfma_f32_16x16x32_bf16 v[124:127], v[164:167], a[60:63], v[124:127]\n\t"
  "s_waitcnt vmcnt(20) lgkmcnt(2)\n\t"
  "s_barrier\n\t"
  "v_mfma_f32_16x16x32_bf16 v[96:99], v[168:171], a[64:67], v[96:99]\n\t"
  "ds_read_b128 v[160:163], v219 offset:36864\n\t"
  "v_mul_f32 v63, v63, v215\n\t"
  "v_mfma_f32_16x16x32_bf16 v[96:99], v[172:175], a[80:83], v[96:99]\n\t"
  "ds_read_b128 v[164:167], v219 offset:38912\n\t"
  "v_cvt_pk_bf16_f32 v198, v60, v61\n\t"
  "v_mfma_f32_16x16x32_bf16 v[100:103], v[168:171], a[68:71], v[100:103]\n\t"
  "s_add_u32 m0, s46, 151552\n\t"
  "v_cvt_pk_bf16_f32 v199, v62, v63\n\t"
  "v_mul_f32 v208, v64, v64\n\t"
  "v_mfma_f32_16x16x32_bf16 v[100:103], v[172:175], a[84:87], v[100:103]\n\t"
  "global_store_dwordx4 v216, v[196:199], s[40:41] offset:1024 sc1\n\t"
  "v_mul_f32 v209, v65, v65\n\t"
  "v_mfma_f32_16x16x32_bf16 v[104:107], v[168:171], a[72:75], v[104:107]\n\t"
  "v_mul_f32 v210, v66, v66\n\t"
  "v_mul_f32 v211, v67, v67\n\t"
  "v_mfma_f32_16x16x32_bf16 v[104:107], v[172:175], a[88:91], v[104:107]\n\t"
  "s_add_u32 s44, s94, 319488\n\t"
  "s_addc_u32 s45, s95, 0\n\t"
  "v_mfma_f32_16x16x32_bf16 v[108:111], v[168:171], a[76:79], v[108:111]\n\t"
  "v_fma_f32 v208, v208, v204, v200\n\t"
  "v_fma_f32 v209, v209, v204, v200\n\t"
  "v_fma_f32 v210, v210, v204, v200\n\t"
  "v_mfma_f32_16x16x32_bf16 v[108:111], v[172:175], a[92:95], v[108:111]\n\t"
  "v_fma_f32 v211, v211, v204, v200\n\t"
  "s_waitcnt lgkmcnt(2)\n\t"
  "v_mfma_f32_16x16x32_bf16 v[112:115], v[176:179], a[64:67], v[112:115]\n\t"
  "ds_read_b128 v[168:171], v219 offset:37888\n\t"
  "v_mul_f32 v208, v64, v208\n\t"
  "v_mfma_f32_16x16x32_bf16 v[112:115], v[180:183], a[80:83], v[112:115]\n\t"
  "ds_read_b128 v[172:175], v219 offset:39936\n\t"
  "v_mul_f32 v209, v65, v209\n\t"
  "v_mfma_f32_16x16x32_bf16 v[116:119], v[176:179], a[68:71], v[116:119]\n\t"
  "global_load_lds_dwordx4 v221, s[44:45] offset:0 sc1\n\t"
  "v_mul_f32 v210, v66, v210\n\t"
  "v_mfma_f32_16x16x32_bf16 v[116:119], v[180:183], a[84:87], v[116:119]\n\t"
  "v_exp_f32 v208, v208\n\t"
  "v_mul_f32 v211, v67, v211\n\t"
  "v_mfma_f32_16x16x32_bf16 v[120:123], v[176:179], a[72:75], v[120:123]\n\t"
  "v_exp_f32 v209, v209\n\t"
  "v_mfma_f32_16x16x32_bf16 v[120:123], v[180:183], a[88:91], v[120:123]\n\t"
  "global_load_lds_dwordx4 v221, s[44:45] offset:1024 sc1\n\t"
  "v_mul_f32 v80, v64, v80\n\t"
  "v_mfma_f32_16x16x32_bf16 v[124:127], v[176:179], a[76:79], v[124:127]\n\t"
  "v_exp_f32 v210, v210\n\t"
  "v_mfma_f32_16x16x32_bf16 v[124:127], v[180:183], a[92:95], v[124:127]\n\t"
  "v_mul_f32 v81, v65, v81\n\t"
  "v_mul_f32 v82, v66, v82\n\t"
  "s_waitcnt lgkmcnt(2)\n\t"
  "v_mfma_f32_16x16x32_bf16 v[96:99], v[160:163], a[96:99], v[96:99]\n\t"
  "ds_read_b128 v[176:179], v219 offset:40960\n\t"
  "v_mul_f32 v83, v67, v83\n\t"
  "v_mfma_f32_16x16x32_bf16 v[96:99], v[164:167], a[112:115], v[96:99]\n\t"
  "ds_read_b128 v[180:183], v219 offset:43008\n\t"
  "v_add_f32 v208, 1.0, v208\n\t"
  "v_mfma_f32_16x16x32_bf16 v[100:103], v[160:163], a[100:103], v[100:103]\n\t"
  "global_load_lds_dwordx4 v221, s[44:45] offset:2048 sc1\n\t"
  "v_add_f32 v209, 1.0, v209\n\t"
  "v_mfma_f32_16x16x32_bf16 v[100:103], v[164:167], a[116:119], v[100:103]\n\t"
  "v_exp_f32 v211, v211\n\t"
  "v_mfma_f32_16x16x32_bf16 v[104:107], v[160:163], a[104:107], v[104:107]\n\t"
  "v_rcp_f32 v208, v208\n\t"
  "v_add_f32 v210, 1.0, v210\n\t"
  "v_mfma_f32_16x16x32_bf16 v[104:107], v[164:167], a[120:123], v[104:107]\n\t"
  "v_rcp_f32 v209, v209\n\t"
  "v_mfma_f32_16x16x32_bf16 v[108:111], v[160:163], a[108:111], v[108:111]\n\t"
  "v_rcp_f32 v210, v210\n\t"
  "v_mfma_f32_16x16x32_bf16 v[108:111], v[164:167], a[124:127], v[108:111]\n\t"
  "v_add_f32 v211, 1.0, v211\n\t"
  "s_waitcnt lgkmcnt(2)\n\t"
  "v_mfma_f32_16x16x32_bf16 v[112:115], v[168:171], a[96:99], v[112:115]\n\t"
  "ds_read_b128 v[160:163], v219 offset:41984\n\t"
  "v_mul_f32 v80, v80, v208\n\t"
  "v_mul_f32 v81, v81, v209\n\t"
  "v_mfma_f32_16x16x32_bf16 v[112:115], v[172:175], a[112:115], v[112:115]\n\t"
  "ds_read_b128 v[164:167], v219 offset:44032\n\t"
  "v_mul_f32 v82, v82, v210\n\t"
  "v_mfma_f32_16x16x32_bf16 v[116:119], v[168:171], a[100:103], v[116:119]\n\t"
  "v_rcp_f32 v211, v211\n\t"
  "v_mfma_f32_16x16x32_bf16 v[116:119], v[172:175], a[116:119], v[116:119]\n\t"
  "v_mul_f32 v83, v83, v211\n\t"
  "v_cvt_pk_bf16_f32 v184, v80, v81\n\t"
  "v_mfma_f32_16x16x32_bf16 v[120:123], v[168:171], a[104:107], v[120:123]\n\t"
  "v_cvt_pk_bf16_f32 v185, v82, v83\n\t"
  "v_mul_f32 v212, v68, v68\n\t"
  "v_mfma_f32_16x16x32_bf16 v[120:123], v[172:175], a[120:123], v[120:123]\n\t"
  "v_mul_f32 v213, v69, v69\n\t"
  "v_mul_f32 v214, v70, v70\n\t"
  "v_mul_f32 v215, v71, v71\n\t"
  "v_mfma_f32_16x16x32_bf16 v[124:127], v[168:171], a[108:111], v[124:127]\n\t"
  "v_fma_f32 v212, v212, v205, v201\n\t"
  "v_fma_f32 v213, v213, v205, v201\n\t"
  "v_mfma_f32_16x16x32_bf16 v[124:127], v[172:175], a[124:127], v[124:127]\n\t"
  "v_fma_f32 v214, v214, v205, v201\n\t"
  "s_waitcnt lgkmcnt(2)\n\t"
  "v_mfma_f32_16x16x32_bf16 v[96:99], v[176:179], a[128:131], v[96:99]\n\t"
  "ds_read_b128 v[168:171], v219 offset:45056\n\t"
  "v_fma_f32 v215, v215, v205, v201\n\t"
  "v_mfma_f32_16x16x32_bf16 v[96:99], v[180:183], a[144:147], v[96:99]\n\t"
  "ds_read_b128 v[172:175], v219 offset:47104\n\t"
  "v_mul_f32 v212, v68, v212\n\t"
  "v_mul_f32 v213, v69, v213\n\t"
  "v_mfma_f32_16x16x32_bf16 v[100:103], v[176:179], a[132:135], v[100:103]\n\t"
  "v_exp_f32 v212, v212\n\t"
  "v_mfma_f32_16x16x32_bf16 v[100:103], v[180:183], a[148:151], v[100:103]\n\t"
  "v_exp_f32 v213, v213\n\t"
  "v_mfma_f32_16x16x32_bf16 v[104:107], v[176:179], a[136:139], v[104:107]\n\t"
  "v_mul_f32 v214, v70, v214\n\t"
  "v_mul_f32 v215, v71, v215\n\t"
  "v_mfma_f32_16x16x32_bf16 v[104:107], v[180:183], a[152:155], v[104:107]\n\t"
  "v_exp_f32 v214, v214\n\t"
  "v_mfma_f32_16x16x32_bf16 v[108:111], v[176:179], a[140:143], v[108:111]\n\t"
  "v_exp_f32 v215, v215\n\t"
  "v_mul_f32 v84, v68, v84\n\t"
  "v_mfma_f32_16x16x32_bf16 v[108:111], v[180:183], a[156:159], v[108:111]\n\t"
  "v_mul_f32 v85, v69, v85\n\t"
  "s_waitcnt lgkmcnt(2)\n\t"
  "v_mfma_f32_16x16x32_bf16 v[112:115], v[160:163], a[128:131], v[112:115]\n\t"
  "ds_read_b128 v[176:179], v219 offset:46080\n\t"
  "v_mul_f32 v86, v70, v86\n\t"
  "v_mfma_f32_16x16x32_bf16 v[112:115], v[164:167], a[144:147], v[112:115]\n\t"
  "ds_read_b128 v[180:183], v219 offset:48128\n\t"
  "v_mul_f32 v87, v71, v87\n\t"
  "v_mfma_f32_16x16x32_bf16 v[116:119], v[160:163], a[132:135], v[116:119]\n\t"
  "v_add_f32 v212, 1.0, v212\n\t"
  "v_add_f32 v213, 1.0, v213\n\t"
  "v_mfma_f32_16x16x32_bf16 v[116:119], v[164:167], a[148:151], v[116:119]\n\t"
  "v_rcp_f32 v212, v212\n\t"
  "v_add_f32 v214, 1.0, v214\n\t"
  "v_mfma_f32_16x16x32_bf16 v[120:123], v[160:163], a[136:139], v[120:123]\n\t"
  "v_rcp_f32 v213, v213\n\t"
  "v_mfma_f32_16x16x32_bf16 v[120:123], v[164:167], a[152:155], v[120:123]\n\t"
  "v_rcp_f32 v214, v214\n\t"
  "v_mfma_f32_16x16x32_bf16 v[124:127], v[160:163], a[140:143], v[124:127]\n\t"
  "v_add_f32 v215, 1.0, v215\n\t"
  "v_mul_f32 v84, v84, v212\n\t"
  "v_mfma_f32_16x16x32_bf16 v[124:127], v[164:167], a[156:159], v[124:127]\n\t"
  "v_mul_f32 v85, v85, v213\n\t"
  "s_waitcnt vmcnt(21) lgkmcnt(2)\n\t"
  "s_barrier\n\t"
  "v_mfma_f32_16x16x32_bf16 v[96:99], v[168:171], a[160:163], v[96:99]\n\t"
  "ds_read_b128 v[160:163], v219 offset:49152\n\t"
  "v_mul_f32 v86, v86, v214\n\t"
  "v_mfma_f32_16x16x32_bf16 v[96:99], v[172:175], a[176:179], v[96:99]\n\t"
  "ds_read_b128 v[164:167], v219 offset:51200\n\t"
  "v_cvt_pk_bf16_f32 v188, v84, v85\n\t"
  "v_mfma_f32_16x16x32_bf16 v[100:103], v[168:171], a[164:167], v[100:103]\n\t"
  "PG4_%=:\n\t"
  "s_cbranch_execz PGBACK_%=\n\t"
  "v_rcp_f32 v215, v215\n\t"
  "v_mfma_f32_16x16x32_bf16 v[100:103], v[172:175], a[180:183], v[100:103]\n\t"
  "v_mul_f32 v87, v87, v215\n\t"
  "v_cvt_pk_bf16_f32 v189, v86, v87\n\t"
  "v_mfma_f32_16x16x32_bf16 v[104:107], v[168:171], a[168:171], v[104:107]\n\t"
  "v_mul_f32 v208, v72, v72\n\t"
  "v_mul_f32 v209, v73, v73\n\t"
  "v_mul_f32 v210, v74, v74\n\t"
  "v_mfma_f32_16x16x32_bf16 v[104:107], v[172:175], a[184:187], v[104:107]\n\t"
  "v_mul_f32 v211, v75, v75\n\t"
  "v_fma_f32 v208, v208, v206, v202\n\t"
  "v_mfma_f32_16x16x32_bf16 v[108:111], v[168:171], a[172:175], v[108:111]\n\t"
  "v_fma_f32 v209, v209, v206, v202\n\t"
  "v_fma_f32 v210, v210, v206, v202\n\t"
  "v_mfma_f32_16x16x32_bf16 v[108:111], v[172:175], a[188:191], v[108:111]\n\t"
  "v_fma_f32 v211, v211, v206, v202\n\t"
  "s_waitcnt lgkmcnt(2)\n\t"
  "v_mfma_f32_16x16x32_bf16 v[112:115], v[176:179], a[160:163], v[112:115]\n\t"
  "ds_read_b128 v[168:171], v219 offset:50176\n\t"
  "v_mul_f32 v208, v72, v208\n\t"
  "v_mfma_f32_16x16x32_bf16 v[112:115], v[180:183], a[176:179], v[112:115]\n\t"
  "ds_read_b128 v[172:175], v219 offset:52224\n\t"
  "v_mul_f32 v209, v73, v209\n\t"
  "v_mul_f32 v210, v74, v210\n\t"
  "v_mfma_f32_16x16x32_bf16 v[116:119], v[176:179], a[164:167], v[116:119]\n\t"
  "v_exp_f32 v208, v208\n\t"
  "v_mfma_f32_16x16x32_bf16 v[116:119], v[180:183], a[180:183], v[116:119]\n\t"
  "v_exp_f32 v209, v209\n\t"
  "v_mfma_f32_16x16x32_bf16 v[120:123], v[176:179], a[168:171], v[120:123]\n\t"
  "v_exp_f32 v210, v210\n\t"
  "v_mfma_f32_16x16x32_bf16 v[120:123], v[180:183], a[184:187], v[120:123]\n\t"
  "v_mul_f32 v211, v75, v211\n\t"
  "v_mul_f32 v88, v72, v88\n\t"
  "v_mul_f32 v89, v73, v89\n\t"
  "v_mfma_f32_16x16x32_bf16 v[124:127], v[176:179], a[172:175], v[124:127]\n\t"
  "v_exp_f32 v211, v211\n\t"
  "v_mfma_f32_16x16x32_bf16 v[124:127], v[180:183], a[188:191], v[124:127]\n\t"
  "v_mul_f32 v90, v74, v90\n\t"
  "s_waitcnt lgkmcnt(2)\n\t"
  "v_mfma_f32_16x16x32_bf16 v[96:99], v[160:163], a[192:195], v[96:99]\n\t"
  "ds_read_b128 v[176:179], v219 offset:53248\n\t"
  "v_mul_f32 v91, v75, v91\n\t"
  "v_mfma_f32_16x16x32_bf16 v[96:99], v[164:167], a[208:211], v[96:99]\n\t"
  "ds_read_b128 v[180:183], v219 offset:55296\n\t"
  "v_add_f32 v208, 1.0, v208\n\t"
  "v_mfma_f32_16x16x32_bf16 v[100:103], v[160:163], a[196:199], v[100:103]\n\t"
  "v_rcp_f32 v208, v208\n\t"
  "v_add_f32 v209, 1.0, v209\n\t"
  "v_mfma_f32_16x16x32_bf16 v[100:103], v[164:167], a[212:215], v[100:103]\n\t"
  "v_rcp_f32 v209, v209\n\t"
  "v_mfma_f32_16x16x32_bf16 v[104:107], v[160:163], a[200:203], v[104:107]\n\t"
  "v_add_f32 v210, 1.0, v210\n\t"
  "v_add_f32 v211, 1.0, v211\n\t"
  "v_mfma_f32_16x16x32_bf16 v[104:107], v[164:167], a[216:219], v[104:107]\n\t"
  "v_rcp_f32 v210, v210\n\t"
  "v_mfma_f32_16x16x32_bf16 v[108:111], v[160:163], a[204:207], v[108:111]\n\t"
  "v_rcp_f32 v211, v211\n\t"
  "v_mfma_f32_16x16x32_bf16 v[108:111], v[164:167], a[220:223], v[108:111]\n\t"
  "v_mul_f32 v88, v88, v208\n\t"
  "v_mul_f32 v89, v89, v209\n\t"
  "s_waitcnt lgkmcnt(2)\n\t"
  "v_mfma_f32_16x16x32_bf16 v[112:115], v[168:171], a[192:195], v[112:115]\n\t"
  "ds_read_b128 v[160:163], v219 offset:54272\n\t"
  "v_mul_f32 v90, v90, v210\n\t"
  "v_mfma_f32_16x16x32_bf16 v[112:115], v[172:175], a[208:211], v[112:115]\n\t"
  "ds_read_b128 v[164:167], v219 offset:56320\n\t"
  "v_mul_f32 v91, v91, v211\n\t"
  "v_mfma_f32_16x16x32_bf16 v[116:119], v[168:171], a[196:199], v[116:119]\n\t"
  "v_cvt_pk_bf16_f32 v192, v88, v89\n\t"
  "v_cvt_pk_bf16_f32 v193, v90, v91\n\t"
  "v_mfma_f32_16x16x32_bf16 v[116:119], v[172:175], a[212:215], v[116:119]\n\t"
  "v_mul_f32 v212, v76, v76\n\t"
  "v_mul_f32 v213, v77, v77\n\t"
  "v_mul_f32 v214, v78, v78\n\t"
  "v_mfma_f32_16x16x32_bf16 v[120:123], v[168:171], a[200:203], v[120:123]\n\t"
  "v_mul_f32 v215, v79, v79\n\t"
  "v_fma_f32 v212, v212, v207, v203\n\t"
  "v_mfma_f32_16x16x32_bf16 v[120:123], v[172:175], a[216:219], v[120:123]\n\t"
  "v_fma_f32 v213, v213, v207, v203\n\t"
  "v_fma_f32 v214, v214, v207, v203\n\t"
  "v_mfma_f32_16x16x32_bf16 v[124:127], v[168:171], a[204:207], v[124:127]\n\t"
  "v_fma_f32 v215, v215, v207, v203\n\t"
  "v_mul_f32 v212, v76, v212\n\t"
  "v_mfma_f32_16x16x32_bf16 v[124:127], v[172:175], a[220:223], v[124:127]\n\t"
  "v_mul_f32 v213, v77, v213\n\t"
  "s_waitcnt lgkmcnt(2)\n\t"
  "v_mfma_f32_16x16x32_bf16 v[96:99], v[176:179], a[224:227], v[96:99]\n\t"
  "ds_read_b128 v[168:171], v219 offset:57344\n\t"
  "v_mul_f32 v214, v78, v214\n\t"
  "v_mul_f32 v215, v79, v215\n\t"
  "v_mfma_f32_16x16x32_bf16 v[96:99], v[180:183], a[240:243], v[96:99]\n\t"
  "ds_read_b128 v[172:175], v219 offset:59392\n\t"
  "v_mul_f32 v92, v76, v92\n\t"
  "v_mfma_f32_16x16x32_bf16 v[100:103], v[176:179], a[228:231], v[100:103]\n\t"
  "v_exp_f32 v212, v212\n\t"
  "v_mfma_f32_16x16x32_bf16 v[100:103], v[180:183], a[244:247], v[100:103]\n\t"
  "v_exp_f32 v213, v213\n\t"
  "v_mfma_f32_16x16x32_bf16 v[104:107], v[176:179], a[232:235], v[104:107]\n\t"
  "v_exp_f32 v214, v214\n\t"
  "v_mfma_f32_16x16x32_bf16 v[104:107], v[180:183], a[248:251], v[104:107]\n\t"
  "v_exp_f32 v215, v215\n\t"
  "v_mul_f32 v93, v77, v93\n\t"
  "v_mfma_f32_16x16x32_bf16 v[108:111], v[176:179], a[236:239], v[108:111]\n\t"
  "v_mul_f32 v94, v78, v94\n\t"
  "v_mul_f32 v95, v79, v95\n\t"
  "v_mfma_f32_16x16x32_bf16 v[108:111], v[180:183], a[252:255], v[108:111]\n\t"
  "v_add_f32 v212, 1.0, v212\n\t"
  "s_waitcnt lgkmcnt(2)\n\t"
  "v_mfma_f32_16x16x32_bf16 v[112:115], v[160:163], a[224:227], v[112:115]\n\t"
  "ds_read_b128 v[176:179], v219 offset:58368\n\t"
  "v_add_f32 v213, 1.0, v213\n\t"
  "v_mfma_f32_16x16x32_bf16 v[112:115], v[164:167], a[240:243], v[112:115]\n\t"
  "ds_read_b128 v[180:183], v219 offset:60416\n\t"
  "v_add_f32 v214, 1.0, v214\n\t"
  "v_mfma_f32_16x16x32_bf16 v[116:119], v[160:163], a[228:231], v[116:119]\n\t"
  "v_rcp_f32 v212, v212\n\t"
  "v_add_f32 v215, 1.0, v215\n\t"
  "v_mfma_f32_16x16x32_bf16 v[116:119], v[164:167], a[244:247], v[116:119]\n\t"
  "s_add_u32 s28, s58, 32768\n\t"
  "s_addc_u32 s29, s59, 0\n\t"
  "v_mfma_f32_16x16x32_bf16 v[120:123], v[160:163], a[232:235], v[120:123]\n\t"
  "v_rcp_f32 v213, v213\n\t"
  "v_mfma_f32_16x16x32_bf16 v[120:123], v[164:167], a[248:251], v[120:123]\n\t"
  "s_mul_i32 s14, s16, 2\n\t"
  "s_add_u32 s26, s58, s14\n\t"
  "s_addc_u32 s27, s59, 0\n\t"
  "v_mfma_f32_16x16x32_bf16 v[124:127], v[160:163], a[236:239], v[124:127]\n\t"
  "v_rcp_f32 v214, v214\n\t"
  "v_mfma_f32_16x16x32_bf16 v[124:127], v[164:167], a[252:255], v[124:127]\n\t"
  "v_mul_f32 v92, v92, v212\n\t"
  "s_waitcnt vmcnt(3) lgkmcnt(2)\n\t"
  "s_barrier\n\t"
  "v_mfma_f32_16x16x32_bf16 v[96:99], v[168:171], v[0:3], v[96:99]\n\t"
  "ds_read_b128 v[160:163], v219 offset:61440\n\t"
  "v_mul_f32 v93, v93, v213\n\t"
  "v_mfma_f32_16x16x32_bf16 v[96:99], v[172:175], v[16:19], v[96:99]\n\t"
  "ds_read_b128 v[164:167], v219 offset:63488\n\t"
  "v_mul_f32 v94, v94, v214\n\t"
  "v_mfma_f32_16x16x32_bf16 v[100:103], v[168:171], v[4:7], v[100:103]\n\t"
  "global_store_dword v236, v244, s[26:27] offset:0 sc1\n\t"
  "v_cvt_pk_bf16_f32 v196, v92, v93\n\t"
  "v_mfma_f32_16x16x32_bf16 v[100:103], v[172:175], v[20:23], v[100:103]\n\t"
  "v_rcp_f32 v215, v215\n\t"
  "v_mfma_f32_16x16x32_bf16 v[104:107], v[168:171], v[8:11], v[104:107]\n\t"
  "global_load_dword v243, v234, s[28:29] offset:0 sc1\n\t"
  "v_mul_f32 v95, v95, v215\n\t"
  "v_cvt_pk_bf16_f32 v197, v94, v95\n\t"
  "v_mfma_f32_16x16x32_bf16 v[104:107], v[172:175], v[24:27], v[104:107]\n\t"
  "s_add_u32 s44, s54, 86016\n\t"
  "s_addc_u32 s45, s55, 0\n\t"
  "v_mfma_f32_16x16x32_bf16 v[108:111], v[168:171], v[12:15], v[108:111]\n\t"
  "v_mfma_f32_16x16x32_bf16 v[108:111], v[172:175], v[28:31], v[108:111]\n\t"
  "s_waitcnt lgkmcnt(2)\n\t"
  "v_mfma_f32_16x16x32_bf16 v[112:115], v[176:179], v[0:3], v[112:115]\n\t"
  "ds_read_b128 v[168:171], v219 offset:62464\n\t"
  "v_mfma_f32_16x16x32_bf16 v[112:115], v[180:183], v[16:19], v[112:115]\n\t"
  "ds_read_b128 v[172:175], v219 offset:64512\n\t"
  "v_mfma_f32_16x16x32_bf16 v[116:119], v[176:179], v[4:7], v[116:119]\n\t"
  "s_add_u32 m0, s48, 86016\n\t"
  "v_mfma_f32_16x16x32_bf16 v[116:119], v[180:183], v[20:23], v[116:119]\n\t"
  "v_mfma_f32_16x16x32_bf16 v[120:123], v[176:179], v[8:11], v[120:123]\n\t"
  "v_mfma_f32_16x16x32_bf16 v[120:123], v[180:183], v[24:27], v[120:123]\n\t"
  "global_load_lds_dwordx4 v230, s[44:45] offset:0 sc1\n\t"
  "v_mfma_f32_16x16x32_bf16 v[124:127], v[176:179], v[12:15], v[124:127]\n\t"
  "v_mfma_f32_16x16x32_bf16 v[124:127], v[180:183], v[28:31], v[124:127]\n\t"
  "s_waitcnt lgkmcnt(2)\n\t"
  "v_mfma_f32_16x16x32_bf16 v[128:131], v[160:163], a[0:3], 0\n\t"
  "ds_read_b128 v[176:179], v220 offset:0\n\t"
  "v_mfma_f32_16x16x32_bf16 v[128:131], v[164:167], a[16:19], v[128:131]\n\t"
  "ds_read_b128 v[180:183], v220 offset:2048\n\t"
  "v_mfma_f32_16x16x32_bf16 v[132:135], v[160:163], a[4:7], 0\n\t"
  "global_load_lds_dwordx4 v230, s[44:45] offset:1024 sc1\n\t"
  "v_mfma_f32_16x16x32_bf16 v[132:135], v[164:167], a[20:23], v[132:135]\n\t"
  "v_mul_f32 v208, v96, v96\n\t"
  "v_mul_f32 v209, v97, v97\n\t"
  "v_mul_f32 v210, v98, v98\n\t"
  "v_mfma_f32_16x16x32_bf16 v[136:139], v[160:163], a[8:11], 0\n\t"
  "v_mul_f32 v211, v99, v99\n\t"
  "v_fma_f32 v208, v208, v204, v200\n\t"
  "v_fma_f32 v209, v209, v204, v200\n\t"
  "v_mfma_f32_16x16x32_bf16 v[136:139], v[164:167], a[24:27], v[136:139]\n\t"
  "global_load_lds_dwordx4 v230, s[44:45] offset:2048 sc1\n\t"
  "v_fma_f32 v210, v210, v204, v200\n\t"
  "v_mfma_f32_16x16x32_bf16 v[140:143], v[160:163], a[12:15], 0\n\t"
  "v_fma_f32 v211, v211, v204, v200\n\t"
  "v_mul_f32 v208, v96, v208\n\t"
  "v_mfma_f32_16x16x32_bf16 v[140:143], v[164:167], a[28:31], v[140:143]\n\t"
  "v_mul_f32 v209, v97, v209\n\t"
  "v_mul_f32 v210, v98, v210\n\t"
  "s_waitcnt lgkmcnt(2)\n\t"
  "v_mfma_f32_16x16x32_bf16 v[144:147], v[168:171], a[0:3], 0\n\t"
  "ds_read_b128 v[160:163], v220 offset:1024\n\t"
  "v_mul_f32 v211, v99, v211\n\t"
  "v_mfma_f32_16x16x32_bf16 v[144:147], v[172:175], a[16:19], v[144:147]\n\t"
  "ds_read_b128 v[164:167], v220 offset:3072\n\t"
  "v_mul_f32 v112, v96, v112\n\t"
  "v_mfma_f32_16x16x32_bf16 v[148:151], v[168:171], a[4:7], 0\n\t"
  "global_load_lds_dwordx4 v230, s[44:45] offset:3072 sc1\n\t"
  "v_mul_f32 v113, v97, v113\n\t"
  "v_mfma_f32_16x16x32_bf16 v[148:151], v[172:175], a[20:23], v[148:151]\n\t"
  "v_exp_f32 v208, v208\n\t"
  "v_mul_f32 v114, v98, v114\n\t"
  "v_mfma_f32_16x16x32_bf16 v[152:155], v[168:171], a[8:11], 0\n\t"
  "v_exp_f32 v209, v209\n\t"
  "v_mfma_f32_16x16x32_bf16 v[152:155], v[172:175], a[24:27], v[152:155]\n\t"
  "s_add_u32 m0, s48, 90112\n\t"
  "v_mul_f32 v115, v99, v115\n\t"
  "v_mfma_f32_16x16x32_bf16 v[156:159], v[168:171], a[12:15], 0\n\t"
  "v_exp_f32 v210, v210\n\t"
  "v_add_f32 v208, 1.0, v208\n\t"
  "v_mfma_f32_16x16x32_bf16 v[156:159], v[172:175], a[28:31], v[156:159]\n\t"
  "v_add_f32 v209, 1.0, v209\n\t"
  "s_waitcnt lgkmcnt(2)\n\t"
  "v_mfma_f32_16x16x32_bf16 v[128:131], v[176:179], a[32:35], v[128:131]\n\t"
  "ds_read_b128 v[168:171], v220 offset:4096\n\t"
  "v_add_f32 v210, 1.0, v210\n\t"
  "v_mfma_f32_16x16x32_bf16 v[128:131], v[180:183], a[48:51], v[128:131]\n\t"
  "ds_read_b128 v[172:175], v220 offset:6144\n\t"
  "v_mul_f32 v212, v100, v100\n\t"
  "v_mfma_f32_16x16x32_bf16 v[132:135], v[176:179], a[36:39], v[132:135]\n\t"
  "global_load_lds_dwordx4 v231, s[44:45] offset:0 sc1\n\t"
  "v_mul_f32 v213, v101, v101\n\t"
  "v_mul_f32 v214, v102, v102\n\t"
  "v_mfma_f32_16x16x32_bf16 v[132:135], v[180:183], a[52:55], v[132:135]\n\t"
  "v_exp_f32 v211, v211\n\t"
  "v_mfma_f32_16x16x32_bf16 v[136:139], v[176:179], a[40:43], v[136:139]\n\t"
  "v_rcp_f32 v208, v208\n\t"
  "v_mfma_f32_16x16x32_bf16 v[136:139], v[180:183], a[56:59], v[136:139]\n\t"
  "global_load_lds_dwordx4 v231, s[44:45] offset:1024 sc1\n\t"
  "v_add_f32 v211, 1.0, v211\n\t"
  "v_mfma_f32_16x16x32_bf16 v[140:143], v[176:179], a[44:47], v[140:143]\n\t"
  "v_rcp_f32 v209, v209\n\t"
  "v_mul_f32 v112, v112, v208\n\t"
  "v_mfma_f32_16x16x32_bf16 v[140:143], v[180:183], a[60:63], v[140:143]\n\t"
  "v_mul_f32 v113, v113, v209\n\t"
  "s_waitcnt lgkmcnt(2)\n\t"
  "v_mfma_f32_16x16x32_bf16 v[144:147], v[160:163], a[32:35], v[144:147]\n\t"
  "ds_read_b128 v[176:179], v220 offset:5120\n\t"
  "v_cvt_pk_bf16_f32 v186, v112, v113\n\t"
  "v_mfma_f32_16x16x32_bf16 v[144:147], v[164:167], a[48:51], v[144:147]\n\t"
  "ds_read_b128 v[180:183], v220 offset:7168\n\t"
  "v_mul_f32 v215, v103, v103\n\t"
  "v_fma_f32 v212, v212, v205, v201\n\t"
  "v_mfma_f32_16x16x32_bf16 v[148:151], v[160:163], a[36:39], v[148:151]\n\t"
  "global_load_lds_dwordx4 v231, s[44:45] offset:2048 sc1\n\t"
  "v_fma_f32 v213, v213, v205, v201\n\t"
  "v_mfma_f32_16x16x32_bf16 v[148:151], v[164:167], a[52:55], v[148:151]\n\t"
  "v_rcp_f32 v210, v210\n\t"
  "v_mfma_f32_16x16x32_bf16 v[152:155], v[160:163], a[40:43], v[152:155]\n\t"
  "v_rcp_f32 v211, v211\n\t"
  "v_mfma_f32_16x16x32_bf16 v[152:155], v[164:167], a[56:59], v[152:155]\n\t"
  "v_mul_f32 v114, v114, v210\n\t"
  "v_mul_f32 v115, v115, v211\n\t"
  "v_cvt_pk_bf16_f32 v187, v114, v115\n\t"
  "v_mfma_f32_16x16x32_bf16 v[156:159], v[160:163], a[44:47], v[156:159]\n\t"
  "global_store_dwordx4 v216, v[184:187], s[34:35] offset:2048 sc1\n\t"
  "v_fma_f32 v214, v214, v205, v201\n\t"
  "v_mfma_f32_16x16x32_bf16 v[156:159], v[164:167], a[60:63], v[156:159]\n\t"
  "s_waitcnt vmcnt(8) lgkmcnt(2)\n\t"
  "s_barrier\n\t"
  "v_mfma_f32_16x16x32_bf16 v[128:131], v[168:171], a[64:67], v[128:131]\n\t"
  "ds_read_b128 v[160:163], v220 offset:8192\n\t"
  "v_fma_f32 v215, v215, v205, v201\n\t"
  "v_mfma_f32_16x16x32_bf16 v[128:131], v[172:175], a[80:83], v[128:131]\n\t"
  "ds_read_b128 v[164:167], v220 offset:10240\n\t"
  "v_mul_f32 v212, v100, v212\n\t"
  "v_mul_f32 v213, v101, v213\n\t"
  "v_mfma_f32_16x16x32_bf16 v[132:135], v[168:171], a[68:71], v[132:135]\n\t"
  "v_cmp_gt_u32 vcc, 1, v243\n\t"
  "v_mul_f32 v214, v102, v214\n\t"
  "v_mfma_f32_16x16x32_bf16 v[132:135], v[172:175], a[84:87], v[132:135]\n\t"
  "v_exp_f32 v212, v212\n\t"
  "v_mfma_f32_16x16x32_bf16 v[136:139], v[168:171], a[72:75], v[136:139]\n\t"
  "v_exp_f32 v213, v213\n\t"
  "v_mfma_f32_16x16x32_bf16 v[136:139], v[172:175], a[88:91], v[136:139]\n\t"
  "s_cbranch_vccnz RETRYA_%=\n\t"
  "BACKA_%=:\n\t"
  "v_mul_f32 v215, v103, v215\n\t"
  "v_mul_f32 v116, v100, v116\n\t"
  "v_mfma_f32_16x16x32_bf16 v[140:143], v[168:171], a[76:79], v[140:143]\n\t"
  "v_exp_f32 v214, v214\n\t"
  "v_mfma_f32_16x16x32_bf16 v[140:143], v[172:175], a[92:95], v[140:143]\n\t"
  "v_mul_f32 v117, v101, v117\n\t"
  "s_waitcnt lgkmcnt(2)\n\t"
  "v_mfma_f32_16x16x32_bf16 v[144:147], v[176:179], a[64:67], v[144:147]\n\t"
  "ds_read_b128 v[168:171], v220 offset:9216\n\t"
  "v_mul_f32 v118, v102, v118\n\t"
  "v_mul_f32 v119, v103, v119\n\t"
  "v_mfma_f32_16x16x32_bf16 v[144:147], v[180:183], a[80:83], v[144:147]\n\t"
  "ds_read_b128 v[172:175], v220 offset:11264\n\t"
  "v_add_f32 v212, 1.0, v212\n\t"
  "v_mfma_f32_16x16x32_bf16 v[148:151], v[176:179], a[68:71], v[148:151]\n\t"
  "v_exp_f32 v215, v215\n\t"
  "v_mfma_f32_16x16x32_bf16 v[148:151], v[180:183], a[84:87], v[148:151]\n\t"
  "v_rcp_f32 v212, v212\n\t"
  "v_mfma_f32_16x16x32_bf16 v[152:155], v[176:179], a[72:75], v[152:155]\n\t"
  "v_add_f32 v213, 1.0, v213\n\t"
  "v_add_f32 v214, 1.0, v214\n\t"
  "v_add_f32 v215, 1.0, v215\n\t"
  "v_mfma_f32_16x16x32_bf16 v[152:155], v[180:183], a[88:91], v[152:155]\n\t"
  "v_rcp_f32 v213, v213\n\t"
  "v_mfma_f32_16x16x32_bf16 v[156:159], v[176:179], a[76:79], v[156:159]\n\t"
  "v_rcp_f32 v214, v214\n\t"
  "v_mfma_f32_16x16x32_bf16 v[156:159], v[180:183], a[92:95], v[156:159]\n\t"
  "v_mul_f32 v116, v116, v212\n\t"
  "s_waitcnt lgkmcnt(2)\n\t"
  "v_mfma_f32_16x16x32_bf16 v[128:131], v[160:163], a[96:99], v[128:131]\n\t"
  "ds_read_b128 v[176:179], v220 offset:12288\n\t"
  "v_mul_f32 v117, v117, v213\n\t"
  "v_mul_f32 v118, v118, v214\n\t"
  "v_mfma_f32_16x16x32_bf16 v[128:131], v[164:167], a[112:115], v[128:131]\n\t"
  "ds_read_b128 v[180:183], v220 offset:14336\n\t"
  "v_cvt_pk_bf16_f32 v190, v116, v117\n\t"
  "v_mfma_f32_16x16x32_bf16 v[132:135], v[160:163], a[100:103], v[132:135]\n\t"
  "global_load_dwordx4 a[0:3], v216, s[50:51] offset:-4096 sc1\n\t"
  "v_mul_f32 v208, v104, v104\n\t"
  "v_mfma_f32_16x16x32_bf16 v[132:135], v[164:167], a[116:119], v[132:135]\n\t"
  "v_rcp_f32 v215, v215\n\t"
  "v_mfma_f32_16x16x32_bf16 v[136:139], v[160:163], a[104:107], v[136:139]\n\t"
  "global_load_dwordx4 a[4:7], v216, s[52:53] offset:-4096 sc1\n\t"
  "v_mul_f32 v119, v119, v215\n\t"
  "v_cvt_pk_bf16_f32 v191, v118, v119\n\t"
  "v_mfma_f32_16x16x32_bf16 v[136:139], v[164:167], a[120:123], v[136:139]\n\t"
  "global_store_dwordx4 v216, v[188:191], s[36:37] offset:2048 sc1\n\t"
  "v_mul_f32 v209, v105, v105\n\t"
  "v_mfma_f32_16x16x32_bf16 v[140:143], v[160:163], a[108:111], v[140:143]\n\t"
  "global_load_dwordx4 a[8:11], v216, s[50:51] offset:1024 sc1\n\t"
  "v_mul_f32 v210, v106, v106\n\t"
  "v_mfma_f32_16x16x32_bf16 v[140:143], v[164:167], a[124:127], v[140:143]\n\t"
  "v_mul_f32 v211, v107, v107\n\t"
  "v_fma_f32 v208, v208, v206, v202\n\t"
  "s_waitcnt lgkmcnt(2)\n\t"
  "v_mfma_f32_16x16x32_bf16 v[144:147], v[168:171], a[96:99], v[144:147]\n\t"
  "ds_read_b128 v[160:163], v220 offset:13312\n\t"
  "v_fma_f32 v209, v209, v206, v202\n\t"
  "v_mfma_f32_16x16x32_bf16 v[144:147], v[172:175], a[112:115], v[144:147]\n\t"
  "ds_read_b128 v[164:167], v220 offset:15360\n\t"
  "v_fma_f32 v210, v210, v206, v202\n\t"
  "v_mfma_f32_16x16x32_bf16 v[148:151], v[168:171], a[100:103], v[148:151]\n\t"
  "global_load_dwordx4 a[12:15], v216, s[52:53] offset:1024 sc1\n\t"
  "v_fma_f32 v211, v211, v206, v202\n\t"
  "v_mfma_f32_16x16x32_bf16 v[148:151], v[172:175], a[116:119], v[148:151]\n\t"
  "v_mul_f32 v208, v104, v208\n\t"
  "v_mul_f32 v209, v105, v209\n\t"
  "v_mul_f32 v210, v106, v210\n\t"
  "v_mfma_f32_16x16x32_bf16 v[152:155], v[168:171], a[104:107], v[152:155]\n\t"
  "v_exp_f32 v208, v208\n\t"
  "v_mfma_f32_16x16x32_bf16 v[152:155], v[172:175], a[120:123], v[152:155]\n\t"
  "global_load_dwordx4 a[16:19], v217, s[50:51] offset:-3072 sc1\n\t"
  "v_mul_f32 v211, v107, v211\n\t"
  "v_mfma_f32_16x16x32_bf16 v[156:159], v[168:171], a[108:111], v[156:159]\n\t"
  "v_exp_f32 v209, v209\n\t"
  "v_mfma_f32_16x16x32_bf16 v[156:159], v[172:175], a[124:127], v[156:159]\n\t"
  "global_load_dwordx4 a[20:23], v217, s[52:53] offset:-3072 sc1\n\t"
  "v_mul_f32 v120, v104, v120\n\t"
  "s_waitcnt lgkmcnt(2)\n\t"
  "v_mfma_f32_16x16x32_bf16 v[128:131], v[176:179], a[128:131], v[128:131]\n\t"
  "ds_read_b128 v[168:171], v220 offset:16384\n\t"
  "v_mul_f32 v121, v105, v121\n\t"
  "v_mfma_f32_16x16x32_bf16 v[128:131], v[180:183], a[144:147], v[128:131]\n\t"
  "ds_read_b128 v[172:175], v220 offset:18432\n\t"
  "v_mul_f32 v122, v106, v122\n\t"
  "v_mfma_f32_16x16x32_bf16 v[132:135], v[176:179], a[132:135], v[132:135]\n\t"
  "v_exp_f32 v210, v210\n\t"
  "v_mfma_f32_16x16x32_bf16 v[132:135], v[180:183], a[148:151], v[132:135]\n\t"
  "global_load_dwordx4 a[24:27], v217, s[50:51] offset:2048 sc1\n\t"
  "v_mul_f32 v123, v107, v123\n\t"
  "v_add_f32 v208, 1.0, v208\n\t"
  "v_mfma_f32_16x16x32_bf16 v[136:139], v[176:179], a[136:139], v[136:139]\n\t"
  "v_exp_f32 v211, v211\n\t"
  "v_mfma_f32_16x16x32_bf16 v[136:139], v[180:183], a[152:155], v[136:139]\n\t"
  "v_rcp_f32 v208, v208\n\t"
  "v_mfma_f32_16x16x32_bf16 v[140:143], v[176:179], a[140:143], v[140:143]\n\t"
  "global_load_dwordx4 a[28:31], v217, s[52:53] offset:2048 sc1\n\t"
  "v_add_f32 v209, 1.0, v209\n\t"
  "v_mfma_f32_16x16x32_bf16 v[140:143], v[180:183], a[156:159], v[140:143]\n\t"
  "v_add_f32 v210, 1.0, v210\n\t"
  "v_add_f32 v211, 1.0, v211\n\t"
  "s_waitcnt lgkmcnt(2)\n\t"
  "v_mfma_f32_16x16x32_bf16 v[144:147], v[160:163], a[128:131], v[144:147]\n\t"
  "ds_read_b128 v[176:179], v220 offset:17408\n\t"
  "v_mul_f32 v120, v120, v208\n\t"
  "v_mfma_f32_16x16x32_bf16 v[144:147], v[164:167], a[144:147], v[144:147]\n\t"
  "ds_read_b128 v[180:183], v220 offset:19456\n\t"
  "v_mul_f32 v212, v108, v108\n\t"
  "v_mfma_f32_16x16x32_bf16 v[148:151], v[160:163], a[132:135], v[148:151]\n\t"
  "global_load_dwordx4 a[32:35], v218, s[50:51] offset:-2048 sc1\n\t"
  "v_mul_f32 v213, v109, v109\n\t"
  "v_mfma_f32_16x16x32_bf16 v[148:151], v[164:167], a[148:151], v[148:151]\n\t"
  "s_add_u32 s28, s58, 65536\n\t"
  "s_addc_u32 s29, s59, 0\n\t"
  "v_mul_f32 v214, v110, v110\n\t"
  "v_mfma_f32_16x16x32_bf16 v[152:155], v[160:163], a[136:139], v[152:155]\n\t"
  "global_load_dwordx4 a[36:39], v218, s[52:53] offset:-2048 sc1\n\t"
  "v_mul_f32 v215, v111, v111\n\t"
  "v_mfma_f32_16x16x32_bf16 v[152:155], v[164:167], a[152:155], v[152:155]\n\t"
  "v_rcp_f32 v209, v209\n\t"
  "v_mfma_f32_16x16x32_bf16 v[156:159], v[160:163], a[140:143], v[156:159]\n\t"
  "v_rcp_f32 v210, v210\n\t"
  "v_mfma_f32_16x16x32_bf16 v[156:159], v[164:167], a[156:159], v[156:159]\n\t"
  "v_mul_f32 v121, v121, v209\n\t"
  "s_waitcnt lgkmcnt(2)\n\t"
  "s_barrier\n\t"
  "v_mfma_f32_16x16x32_bf16 v[128:131], v[168:171], a[160:163], v[128:131]\n\t"
  "ds_read_b128 v[160:163], v220 offset:20480\n\t"
  "v_mul_f32 v122, v122, v210\n\t"
  "v_mfma_f32_16x16x32_bf16 v[128:131], v[172:175], a[176:179], v[128:131]\n\t"
  "ds_read_b128 v[164:167], v220 offset:22528\n\t"
  "v_cvt_pk_bf16_f32 v194, v120, v121\n\t"
  "v_mfma_f32_16x16x32_bf16 v[132:135], v[168:171], a[164:167], v[132:135]\n\t"
  "v_rcp_f32 v211, v211\n\t"
  "v_mfma_f32_16x16x32_bf16 v[132:135], v[172:175], a[180:183], v[132:135]\n\t"
  "v_mul_f32 v123, v123, v211\n\t"
  "v_cvt_pk_bf16_f32 v195, v122, v123\n\t"
  "v_fma_f32 v212, v212, v207, v203\n\t"
  "v_mfma_f32_16x16x32_bf16 v[136:139], v[168:171], a[168:171], v[136:139]\n\t"
  "global_load_dword v243, v235, s[28:29] offset:0 sc1\n\t"
  "global_store_dwordx4 v216, v[192:195], s[38:39] offset:2048 sc1\n\t"
  "v_mfma_f32_16x16x32_bf16 v[136:139], v[172:175], a[184:187], v[136:139]\n\t"
  "global_load_dwordx4 a[40:43], v218, s[50:51] offset:3072 sc1\n\t"
  "v_fma_f32 v213, v213, v207, v203\n\t"
  "v_mfma_f32_16x16x32_bf16 v[140:143], v[168:171], a[172:175], v[140:143]\n\t"
  "v_fma_f32 v214, v214, v207, v203\n\t"
  "v_fma_f32 v215, v215, v207, v203\n\t"
  "v_mfma_f32_16x16x32_bf16 v[140:143], v[172:175], a[188:191], v[140:143]\n\t"
  "v_mul_f32 v212, v108, v212\n\t"
  "v_mul_f32 v213, v109, v213\n\t"
  "s_waitcnt lgkmcnt(2)\n\t"
  "v_mfma_f32_16x16x32_bf16 v[144:147], v[176:179], a[160:163], v[144:147]\n\t"
  "ds_read_b128 v[168:171], v220 offset:21504\n\t"
  "v_mul_f32 v214, v110, v214\n\t"
  "v_mfma_f32_16x16x32_bf16 v[144:147], v[180:183], a[176:179], v[144:147]\n\t"
  "ds_read_b128 v[172:175], v220 offset:23552\n\t"
  "v_mul_f32 v215, v111, v215\n\t"
  "v_mfma_f32_16x16x32_bf16 v[148:151], v[176:179], a[164:167], v[148:151]\n\t"
  "global_load_dwordx4 a[44:47], v218, s[52:53] offset:3072 sc1\n\t"
  "v_mul_f32 v124, v108, v124\n\t"
  "v_mfma_f32_16x16x32_bf16 v[148:151], v[180:183], a[180:183], v[148:151]\n\t"
  "v_exp_f32 v212, v212\n\t"
  "v_mul_f32 v125, v109, v125\n\t"
  "v_mfma_f32_16x16x32_bf16 v[152:155], v[176:179], a[168:171], v[152:155]\n\t"
  "v_exp_f32 v213, v213\n\t"
  "v_mfma_f32_16x16x32_bf16 v[152:155], v[180:183], a[184:187], v[152:155]\n\t"
  "global_load_dwordx4 a[48:51], v228, s[50:51] offset:-1024 sc1\n\t"
  "v_mul_f32 v126, v110, v126\n\t"
  "v_mfma_f32_16x16x32_bf16 v[156:159], v[176:179], a[172:175], v[156:159]\n\t"
  "v_exp_f32 v214, v214\n\t"
  "v_mfma_f32_16x16x32_bf16 v[156:159], v[180:183], a[188:191], v[156:159]\n\t"
  "v_mul_f32 v127, v111, v127\n\t"
  "v_add_f32 v212, 1.0, v212\n\t"
  "s_waitcnt lgkmcnt(2)\n\t"
  "v_mfma_f32_16x16x32_bf16 v[128:131], v[160:163], a[192:195], v[128:131]\n\t"
  "ds_read_b128 v[176:179], v220 offset:24576\n\t"
  "v_add_f32 v213, 1.0, v213\n\t"
  "v_mfma_f32_16x16x32_bf16 v[128:131], v[164:167], a[208:211], v[128:131]\n\t"
  "ds_read_b128 v[180:183], v220 offset:26624\n\t"
  "v_add_f32 v214, 1.0, v214\n\t"
  "v_mfma_f32_16x16x32_bf16 v[132:135], v[160:163], a[196:199], v[132:135]\n\t"
  "global_load_dwordx4 a[52:55], v228, s[52:53] offset:-1024 sc1\n\t"
  "v_mfma_f32_16x16x32_bf16 v[132:135], v[164:167], a[212:215], v[132:135]\n\t"
  "v_exp_f32 v215, v215\n\t"
  "v_mfma_f32_16x16x32_bf16 v[136:139], v[160:163], a[200:203], v[136:139]\n\t"
  "v_rcp_f32 v212, v212\n\t"
  "v_add_f32 v215, 1.0, v215\n\t"
  "v_mfma_f32_16x16x32_bf16 v[136:139], v[164:167], a[216:219], v[136:139]\n\t"
  "global_load_dwordx4 a[56:59], v229, s[50:51] offset:-4096 sc1\n\t"
  "v_mul_f32 v124, v124, v212\n\t"
  "v_mfma_f32_16x16x32_bf16 v[140:143], v[160:163], a[204:207], v[140:143]\n\t"
  "v_rcp_f32 v213, v213\n\t"
  "v_mfma_f32_16x16x32_bf16 v[140:143], v[164:167], a[220:223], v[140:143]\n\t"
  "v_mul_f32 v125, v125, v213\n\t"
  "v_cvt_pk_bf16_f32 v198, v124, v125\n\t"
  "s_waitcnt lgkmcnt(2)\n\t"
  "v_mfma_f32_16x16x32_bf16 v[144:147], v[168:171], a[192:195], v[144:147]\n\t"
  "ds_read_b128 v[160:163], v220 offset:25600\n\t"
  "v_mfma_f32_16x16x32_bf16 v[144:147], v[172:175], a[208:211], v[144:147]\n\t"
  "ds_read_b128 v[164:167], v220 offset:27648\n\t"
  "v_mfma_f32_16x16x32_bf16 v[148:151], v[168:171], a[196:199], v[148:151]\n\t"
  "global_load_dwordx4 a[60:63], v229, s[52:53] offset:-4096 sc1\n\t"
  "v_mfma_f32_16x16x32_bf16 v[148:151], v[172:175], a[212:215], v[148:151]\n\t"
  "v_rcp_f32 v214, v214\n\t"
  "v_mfma_f32_16x16x32_bf16 v[152:155], v[168:171], a[200:203], v[152:155]\n\t"
  "v_rcp_f32 v215, v215\n\t"
  "v_mul_f32 v126, v126, v214\n\t"
  "v_mfma_f32_16x16x32_bf16 v[152:155], v[172:175], a[216:219], v[152:155]\n\t"
  "global_load_dwordx4 a[64:67], v216, s[50:51] offset:-3072 sc1\n\t"
  "v_mul_f32 v127, v127, v215\n\t"
  "v_mfma_f32_16x16x32_bf16 v[156:159], v[168:171], a[204:207], v[156:159]\n\t"
  "v_cvt_pk_bf16_f32 v199, v126, v127\n\t"
  "v_mfma_f32_16x16x32_bf16 v[156:159], v[172:175], a[220:223], v[156:159]\n\t"
  "global_store_dwordx4 v216, v[196:199], s[40:41] offset:2048 sc1\n\t"
  "s_waitcnt lgkmcnt(2)\n\t"
  "v_mfma_f32_16x16x32_bf16 v[128:131], v[176:179], a[224:227], v[128:131]\n\t"
  "ds_read_b128 v[168:171], v220 offset:28672\n\t"
  "v_mfma_f32_16x16x32_bf16 v[128:131], v[180:183], a[240:243], v[128:131]\n\t"
  "ds_read_b128 v[172:175], v220 offset:30720\n\t"
  "v_mfma_f32_16x16x32_bf16 v[132:135], v[176:179], a[228:231], v[132:135]\n\t"
  "global_load_dwordx4 a[68:71], v216, s[52:53] offset:-3072 sc1\n\t"
  "v_mfma_f32_16x16x32_bf16 v[132:135], v[180:183], a[244:247], v[132:135]\n\t"
  "v_mfma_f32_16x16x32_bf16 v[136:139], v[176:179], a[232:235], v[136:139]\n\t"
  "v_mfma_f32_16x16x32_bf16 v[136:139], v[180:183], a[248:251], v[136:139]\n\t"
  "global_load_dwordx4 a[72:75], v216, s[50:51] offset:2048 sc1\n\t"
  "v_mfma_f32_16x16x32_bf16 v[140:143], v[176:179], a[236:239], v[140:143]\n\t"
  "v_mfma_f32_16x16x32_bf16 v[140:143], v[180:183], a[252:255], v[140:143]\n\t"
  "s_waitcnt lgkmcnt(2)\n\t"
  "v_mfma_f32_16x16x32_bf16 v[144:147], v[160:163], a[224:227], v[144:147]\n\t"
  "ds_read_b128 v[176:179], v220 offset:29696\n\t"
  "v_mfma_f32_16x16x32_bf16 v[144:147], v[164:167], a[240:243], v[144:147]\n\t"
  "ds_read_b128 v[180:183], v220 offset:31744\n\t"
  "v_mfma_f32_16x16x32_bf16 v[148:151], v[160:163], a[228:231], v[148:151]\n\t"
  "global_load_dwordx4 a[76:79], v216, s[52:53] offset:2048 sc1\n\t"
  "v_mfma_f32_16x16x32_bf16 v[148:151], v[164:167], a[244:247], v[148:151]\n\t"
  "v_mfma_f32_16x16x32_bf16 v[152:155], v[160:163], a[232:235], v[152:155]\n\t"
  "v_mfma_f32_16x16x32_bf16 v[152:155], v[164:167], a[248:251], v[152:155]\n\t"
  "v_mfma_f32_16x16x32_bf16 v[156:159], v[160:163], a[236:239], v[156:159]\n\t"
  "v_mfma_f32_16x16x32_bf16 v[156:159], v[164:167], a[252:255], v[156:159]\n\t"
  "s_waitcnt vmcnt(12) lgkmcnt(2)\n\t"
  "s_barrier\n\t"
  "v_mfma_f32_16x16x32_bf16 v[128:131], v[168:171], v[0:3], v[128:131]\n\t"
  "ds_read_b128 v[32:35], v216 offset:0\n\t"
  "v_mfma_f32_16x16x32_bf16 v[128:131], v[172:175], v[16:19], v[128:131]\n\t"
  "ds_read_b128 v[36:39], v216 offset:7168\n\t"
  "v_mfma_f32_16x16x32_bf16 v[132:135], v[168:171], v[4:7], v[132:135]\n\t"
  "ds_read_b128 v[40:43], v216 offset:1024\n\t"
  "v_cmp_gt_u32 vcc, 1, v243\n\t"
  "v_mfma_f32_16x16x32_bf16 v[132:135], v[172:175], v[20:23], v[132:135]\n\t"
  "ds_read_b128 v[44:47], v216 offset:8192\n\t"
  "v_mfma_f32_16x16x32_bf16 v[136:139], v[168:171], v[8:11], v[136:139]\n\t"
  "v_mfma_f32_16x16x32_bf16 v[136:139], v[172:175], v[24:27], v[136:139]\n\t"
  "s_cbranch_vccnz RETRYB_%=\n\t"
  "BACKB_%=:\n\t"
  "v_mfma_f32_16x16x32_bf16 v[140:143], v[168:171], v[12:15], v[140:143]\n\t"
  "global_load_dwordx4 a[80:83], v217, s[50:51] offset:-2048 sc1\n\t"
  "v_mfma_f32_16x16x32_bf16 v[140:143], v[172:175], v[28:31], v[140:143]\n\t"
  "s_waitcnt lgkmcnt(4)\n\t"
  "v_mfma_f32_16x16x32_bf16 v[144:147], v[176:179], v[0:3], v[144:147]\n\t"
  "ds_read_b128 v[48:51], v216 offset:2048\n\t"
  "v_mfma_f32_16x16x32_bf16 v[144:147], v[180:183], v[16:19], v[144:147]\n\t"
  "ds_read_b128 v[52:55], v216 offset:9216\n\t"
  "v_mfma_f32_16x16x32_bf16 v[148:151], v[176:179], v[4:7], v[148:151]\n\t"
  "ds_read_b128 v[56:59], v216 offset:3072\n\t"
  "v_mfma_f32_16x16x32_bf16 v[148:151], v[180:183], v[20:23], v[148:151]\n\t"
  "ds_read_b128 v[60:63], v216 offset:10240\n\t"
  "v_mfma_f32_16x16x32_bf16 v[152:155], v[176:179], v[8:11], v[152:155]\n\t"
  "global_load_dwordx4 a[84:87], v217, s[52:53] offset:-2048 sc1\n\t"
  "v_mfma_f32_16x16x32_bf16 v[152:155], v[180:183], v[24:27], v[152:155]\n\t"
  "v_mfma_f32_16x16x32_bf16 v[156:159], v[176:179], v[12:15], v[156:159]\n\t"
  "global_load_dwordx4 a[88:91], v217, s[50:51] offset:3072 sc1\n\t"
  "v_mfma_f32_16x16x32_bf16 v[156:159], v[180:183], v[28:31], v[156:159]\n\t"
  "s_waitcnt lgkmcnt(4)\n\t"
  "v_mfma_f32_16x16x32_bf16 v[64:67], v[32:35], a[0:3], 0\n\t"
  "ds_read_b128 v[160:163], v216 offset:4096\n\t"
  "v_mfma_f32_16x16x32_bf16 v[64:67], v[36:39], a[8:11], v[64:67]\n\t"
  "ds_read_b128 v[164:167], v216 offset:11264\n\t"
  "v_mfma_f32_16x16x32_bf16 v[68:71], v[32:35], a[4:7], 0\n\t"
  "ds_read_b128 v[168:171], v216 offset:5120\n\t"
  "v_mfma_f32_16x16x32_bf16 v[68:71], v[36:39], a[12:15], v[68:71]\n\t"
  "ds_read_b128 v[172:175], v216 offset:12288\n\t"
  "v_mul_f32 v208, v128, v128\n\t"
  "v_mul_f32 v209, v129, v129\n\t"
  "v_mfma_f32_16x16x32_bf16 v[72:75], v[40:43], a[0:3], 0\n\t"
  "global_load_dwordx4 a[92:95], v217, s[52:53] offset:3072 sc1\n\t"
  "v_mul_f32 v210, v130, v130\n\t"
  "v_mul_f32 v211, v131, v131\n\t"
  "v_mfma_f32_16x16x32_bf16 v[72:75], v[44:47], a[8:11], v[72:75]\n\t"
  "v_fma_f32 v208, v208, v204, v200\n\t"
  "v_fma_f32 v209, v209, v204, v200\n\t"
  "v_fma_f32 v210, v210, v204, v200\n\t"
  "v_mfma_f32_16x16x32_bf16 v[76:79], v[40:43], a[4:7], 0\n\t"
  "global_load_dwordx4 a[96:99], v218, s[50:51] offset:-1024 sc1\n\t"
  "v_fma_f32 v211, v211, v204, v200\n\t"
  "v_mul_f32 v208, v128, v208\n\t"
  "v_mfma_f32_16x16x32_bf16 v[76:79], v[44:47], a[12:15], v[76:79]\n\t"
  "v_mul_f32 v209, v129, v209\n\t"
  "v_mul_f32 v210, v130, v210\n\t"
  "s_waitcnt lgkmcnt(4)\n\t"
  "v_mfma_f32_16x16x32_bf16 v[80:83], v[48:51], a[0:3], 0\n\t"
  "ds_read_b128 v[32:35], v216 offset:6144\n\t"
  "v_mul_f32 v211, v131, v211\n\t"
  "v_mfma_f32_16x16x32_bf16 v[80:83], v[52:55], a[8:11], v[80:83]\n\t"
  "ds_read_b128 v[36:39], v216 offset:13312\n\t"
  "v_mul_f32 v144, v128, v144\n\t"
  "v_mul_f32 v145, v129, v145\n\t"
  "v_mfma_f32_16x16x32_bf16 v[84:87], v[48:51], a[4:7], 0\n\t"
  "ds_read_b128 v[40:43], v216 offset:14336\n\t"
  "v_mul_f32 v146, v130, v146\n\t"
  "v_mul_f32 v147, v131, v147\n\t"
  "v_mfma_f32_16x16x32_bf16 v[84:87], v[52:55], a[12:15], v[84:87]\n\t"
  "ds_read_b128 v[44:47], v216 offset:21504\n\t"
  "v_exp_f32 v208, v208\n\t"
  "v_mfma_f32_16x16x32_bf16 v[88:91], v[56:59], a[0:3], 0\n\t"
  "global_load_dwordx4 a[100:103], v218, s[52:53] offset:-1024 sc1\n\t"
  "v_add_f32 v208, 1.0, v208\n\t"
  "v_mfma_f32_16x16x32_bf16 v[88:91], v[60:63], a[8:11], v[88:91]\n\t"
  "v_exp_f32 v209, v209\n\t"
  "v_mfma_f32_16x16x32_bf16 v[92:95], v[56:59], a[4:7], 0\n\t"
  "global_load_dwordx4 a[104:107], v228, s[50:51] offset:-4096 sc1\n\t"
  "v_add_f32 v209, 1.0, v209\n\t"
  "v_mfma_f32_16x16x32_bf16 v[92:95], v[60:63], a[12:15], v[92:95]\n\t"
  "v_exp_f32 v210, v210\n\t"
  "s_waitcnt lgkmcnt(4)\n\t"
  "v_mfma_f32_16x16x32_bf16 v[96:99], v[160:163], a[0:3], 0\n\t"
  "ds_read_b128 v[48:51], v216 offset:15360\n\t"
  "v_add_f32 v210, 1.0, v210\n\t"
  "v_mul_f32 v212, v132, v132\n\t"
  "v_mfma_f32_16x16x32_bf16 v[96:99], v[164:167], a[8:11], v[96:99]\n\t"
  "ds_read_b128 v[52:55], v216 offset:22528\n\t"
  "v_mul_f32 v213, v133, v133\n\t"
  "v_mul_f32 v214, v134, v134\n\t"
  "v_mfma_f32_16x16x32_bf16 v[100:103], v[160:163], a[4:7], 0\n\t"
  "ds_read_b128 v[56:59], v216 offset:16384\n\t"
  "v_mul_f32 v215, v135, v135\n\t"
  "v_fma_f32 v212, v212, v205, v201\n\t"
  "v_mfma_f32_16x16x32_bf16 v[100:103], v[164:167], a[12:15], v[100:103]\n\t"
  "ds_read_b128 v[60:63], v216 offset:23552\n\t"
  "v_fma_f32 v213, v213, v205, v201\n\t"
  "v_fma_f32 v214, v214, v205, v201\n\t"
  "v_mfma_f32_16x16x32_bf16 v[104:107], v[168:171], a[0:3], 0\n\t"
  "global_load_dwordx4 a[108:111], v228, s[52:53] offset:-4096 sc1\n\t"
  "v_fma_f32 v215, v215, v205, v201\n\t"
  "v_mfma_f32_16x16x32_bf16 v[104:107], v[172:175], a[8:11], v[104:107]\n\t"
  "v_exp_f32 v211, v211\n\t"
  "v_mul_f32 v212, v132, v212\n\t"
  "v_mfma_f32_16x16x32_bf16 v[108:111], v[168:171], a[4:7], 0\n\t"
  "global_load_dwordx4 a[112:115], v228, s[50:51] offset:0 sc1\n\t"
  "v_add_f32 v211, 1.0, v211\n\t"
  "v_mul_f32 v213, v133, v213\n\t"
  "v_mfma_f32_16x16x32_bf16 v[108:111], v[172:175], a[12:15], v[108:111]\n\t"
  "v_mul_f32 v214, v134, v214\n\t"
  "v_mul_f32 v215, v135, v215\n\t"
  "s_waitcnt lgkmcnt(4)\n\t"
  "v_mfma_f32_16x16x32_bf16 v[112:115], v[32:35], a[0:3], 0\n\t"
  "ds_read_b128 v[160:163], v216 offset:17408\n\t"
  "v_mfma_f32_16x16x32_bf16 v[112:115], v[36:39], a[8:11], v[112:115]\n\t"
  "PG5_%=:\n\t"
  "s_cbranch_execz PGBACK_%=\n\t"
  "ds_read_b128 v[164:167], v216 offset:24576\n\t"
  "v_rcp_f32 v208, v208\n\t"
  "v_mfma_f32_16x16x32_bf16 v[116:119], v[32:35], a[4:7], 0\n\t"
  "ds_read_b128 v[168:171], v216 offset:18432\n\t"
  "v_mul_f32 v144, v144, v208\n\t"
  "v_mul_f32 v148, v132, v148\n\t"
  "v_mfma_f32_16x16x32_bf16 v[116:119], v[36:39], a[12:15], v[116:119]\n\t"
  "ds_read_b128 v[172:175], v216 offset:25600\n\t"
  "v_mul_f32 v149, v133, v149\n\t"
  "v_mul_f32 v150, v134, v150\n\t"
  "v_mfma_f32_16x16x32_bf16 v[64:67], v[40:43], a[16:19], v[64:67]\n\t"
  "global_load_dwordx4 a[116:119], v228, s[52:53] offset:0 sc1\n\t"
  "v_mul_f32 v151, v135, v151\n\t"
  "v_mfma_f32_16x16x32_bf16 v[64:67], v[44:47], a[24:27], v[64:67]\n\t"
  "v_rcp_f32 v209, v209\n\t"
  "v_mfma_f32_16x16x32_bf16 v[68:71], v[40:43], a[20:23], v[68:71]\n\t"
  "v_rcp_f32 v210, v210\n\t"
  "v_mul_f32 v145, v145, v209\n\t"
  "v_mfma_f32_16x16x32_bf16 v[68:71], v[44:47], a[28:31], v[68:71]\n\t"
  "v_mul_f32 v146, v146, v210\n\t"
  "v_cvt_pk_bf16_f32 v184, v144, v145\n\t"
  "s_waitcnt lgkmcnt(4)\n\t"
  "v_mfma_f32_16x16x32_bf16 v[72:75], v[48:51], a[16:19], v[72:75]\n\t"
  "ds_read_b128 v[32:35], v216 offset:19456\n\t"
  "v_rcp_f32 v211, v211\n\t"
  "v_mfma_f32_16x16x32_bf16 v[72:75], v[52:55], a[24:27], v[72:75]\n\t"
  "ds_read_b128 v[36:39], v216 offset:26624\n\t"
  "v_mul_f32 v147, v147, v211\n\t"
  "v_cvt_pk_bf16_f32 v185, v146, v147\n\t"
  "v_mfma_f32_16x16x32_bf16 v[76:79], v[48:51], a[20:23], v[76:79]\n\t"
  "ds_read_b128 v[40:43], v216 offset:20480\n\t"
  "global_store_dwordx2 v222, v[184:185], s[34:35] offset:-2048 sc1\n\t"
  "v_mfma_f32_16x16x32_bf16 v[76:79], v[52:55], a[28:31], v[76:79]\n\t"
  "ds_read_b128 v[44:47], v216 offset:27648\n\t"
  "v_exp_f32 v212, v212\n\t"
  "v_mfma_f32_16x16x32_bf16 v[80:83], v[56:59], a[16:19], v[80:83]\n\t"
  "v_exp_f32 v213, v213\n\t"
  "v_add_f32 v212, 1.0, v212\n\t"
  "v_mfma_f32_16x16x32_bf16 v[80:83], v[60:63], a[24:27], v[80:83]\n\t"
  "s_mul_i32 s14, s16, 3\n\t"
  "s_add_u32 s26, s58, s14\n\t"
  "s_addc_u32 s27, s59, 0\n\t"
  "v_mfma_f32_16x16x32_bf16 v[84:87], v[56:59], a[20:23], v[84:87]\n\t"
  "v_exp_f32 v214, v214\n\t"
  "v_add_f32 v213, 1.0, v213\n\t"
  "v_mfma_f32_16x16x32_bf16 v[84:87], v[60:63], a[28:31], v[84:87]\n\t"
  "v_add_f32 v214, 1.0, v214\n\t"
  "s_waitcnt vmcnt(14) lgkmcnt(4)\n\t"
  "s_barrier\n\t"
  "v_mfma_f32_16x16x32_bf16 v[88:91], v[160:163], a[16:19], v[88:91]\n\t"
  "ds_read_b128 v[48:51], v216 offset:28672\n\t"
  "v_mul_f32 v208, v136, v136\n\t"
  "v_mul_f32 v209, v137, v137\n\t"
  "v_mfma_f32_16x16x32_bf16 v[88:91], v[164:167], a[24:27], v[88:91]\n\t"
  "ds_read_b128 v[52:55], v216 offset:35840\n\t"
  "v_mul_f32 v210, v138, v138\n\t"
  "v_mul_f32 v211, v139, v139\n\t"
  "v_mfma_f32_16x16x32_bf16 v[92:95], v[160:163], a[20:23], v[92:95]\n\t"
  "ds_read_b128 v[56:59], v216 offset:29696\n\t"
  "v_fma_f32 v208, v208, v206, v202\n\t"
  "v_fma_f32 v209, v209, v206, v202\n\t"
  "v_mfma_f32_16x16x32_bf16 v[92:95], v[164:167], a[28:31], v[92:95]\n\t"
  "ds_read_b128 v[60:63], v216 offset:36864\n\t"
  "v_fma_f32 v210, v210, v206, v202\n\t"
  "v_mfma_f32_16x16x32_bf16 v[96:99], v[168:171], a[16:19], v[96:99]\n\t"
  "global_load_dwordx4 a[120:123], v229, s[50:51] offset:-3072 sc1\n\t"
  "v_fma_f32 v211, v211, v206, v202\n\t"
  "v_mul_f32 v208, v136, v208\n\t"
  "v_mfma_f32_16x16x32_bf16 v[96:99], v[172:175], a[24:27], v[96:99]\n\t"
  "global_load_dwordx4 a[124:127], v229, s[52:53] offset:-3072 sc1\n\t"
  "global_store_dword v236, v244, s[26:27] offset:0 sc1\n\t"
  "v_mul_f32 v209, v137, v209\n\t"
  "v_mfma_f32_16x16x32_bf16 v[100:103], v[168:171], a[20:23], v[100:103]\n\t"
  "v_exp_f32 v215, v215\n\t"
  "v_mul_f32 v210, v138, v210\n\t"
  "v_mfma_f32_16x16x32_bf16 v[100:103], v[172:175], a[28:31], v[100:103]\n\t"
  "global_load_dwordx4 a[128:131], v216, s[50:51] offset:-2048 sc1\n\t"
  "v_add_f32 v215, 1.0, v215\n\t"
  "s_waitcnt lgkmcnt(4)\n\t"
  "v_mfma_f32_16x16x32_bf16 v[104:107], v[32:35], a[16:19], v[104:107]\n\t"
  "ds_read_b128 v[160:163], v216 offset:30720\n\t"
  "v_mul_f32 v211, v139, v211\n\t"
  "v_mfma_f32_16x16x32_bf16 v[104:107], v[36:39], a[24:27], v[104:107]\n\t"
  "ds_read_b128 v[164:167], v216 offset:37888\n\t"
  "v_rcp_f32 v212, v212\n\t"
  "v_mfma_f32_16x16x32_bf16 v[108:111], v[32:35], a[20:23], v[108:111]\n\t"
  "ds_read_b128 v[168:171], v216 offset:31744\n\t"
  "v_mul_f32 v148, v148, v212\n\t"
  "v_mul_f32 v152, v136, v152\n\t"
  "v_mfma_f32_16x16x32_bf16 v[108:111], v[36:39], a[28:31], v[108:111]\n\t"
  "ds_read_b128 v[172:175], v216 offset:38912\n\t"
  "v_mul_f32 v153, v137, v153\n\t"
  "v_mul_f32 v154, v138, v154\n\t"
  "v_mfma_f32_16x16x32_bf16 v[112:115], v[40:43], a[16:19], v[112:115]\n\t"
  "global_load_dwordx4 a[132:135], v216, s[52:53] offset:-2048 sc1\n\t"
  "v_mul_f32 v155, v139, v155\n\t"
  "v_mfma_f32_16x16x32_bf16 v[112:115], v[44:47], a[24:27], v[112:115]\n\t"
  "v_rcp_f32 v213, v213\n\t"
  "v_mfma_f32_16x16x32_bf16 v[116:119], v[40:43], a[20:23], v[116:119]\n\t"
  "global_load_dwordx4 a[136:139], v216, s[50:51] offset:3072 sc1\n\t"
  "v_mul_f32 v149, v149, v213\n\t"
  "v_cvt_pk_bf16_f32 v188, v148, v149\n\t"
  "v_mfma_f32_16x16x32_bf16 v[116:119], v[44:47], a[28:31], v[116:119]\n\t"
  "global_load_dwordx4 a[140:143], v216, s[52:53] offset:3072 sc1\n\t"
  "s_waitcnt lgkmcnt(4)\n\t"
  "v_mfma_f32_16x16x32_bf16 v[64:67], v[48:51], a[32:35], v[64:67]\n\t"
  "ds_read_b128 v[32:35], v216 offset:32768\n\t"
  "v_rcp_f32 v214, v214\n\t"
  "v_mfma_f32_16x16x32_bf16 v[64:67], v[52:55], a[40:43], v[64:67]\n\t"
  "ds_read_b128 v[36:39], v216 offset:39936\n\t"
  "v_mul_f32 v150, v150, v214\n\t"
  "v_mfma_f32_16x16x32_bf16 v[68:71], v[48:51], a[36:39], v[68:71]\n\t"
  "ds_read_b128 v[40:43], v216 offset:33792\n\t"
  "v_rcp_f32 v215, v215\n\t"
  "v_mfma_f32_16x16x32_bf16 v[68:71], v[52:55], a[44:47], v[68:71]\n\t"
  "ds_read_b128 v[44:47], v216 offset:40960\n\t"
  "v_mul_f32 v151, v151, v215\n\t"
  "v_cvt_pk_bf16_f32 v189, v150, v151\n\t"
  "v_mfma_f32_16x16x32_bf16 v[72:75], v[56:59], a[32:35], v[72:75]\n\t"
  "v_exp_f32 v208, v208\n\t"
  "v_mfma_f32_16x16x32_bf16 v[72:75], v[60:63], a[40:43], v[72:75]\n\t"
  "global_load_dwordx4 a[144:147], v217, s[50:51] offset:-1024 sc1\n\t"
  "global_store_dwordx2 v222, v[188:189], s[36:37] offset:-2048 sc1\n\t"
  "v_add_f32 v208, 1.0, v208\n\t"
  "v_mfma_f32_16x16x32_bf16 v[76:79], v[56:59], a[36:39], v[76:79]\n\t"
  "global_load_dwordx4 a[148:151], v217, s[52:53] offset:-1024 sc1\n\t"
  "v_exp_f32 v209, v209\n\t"
  "v_mfma_f32_16x16x32_bf16 v[76:79], v[60:63], a[44:47], v[76:79]\n\t"
  "v_add_f32 v209, 1.0, v209\n\t"
  "s_waitcnt lgkmcnt(4)\n\t"
  "v_mfma_f32_16x16x32_bf16 v[80:83], v[160:163], a[32:35], v[80:83]\n\t"
  "ds_read_b128 v[48:51], v216 offset:34816\n\t"
  "v_exp_f32 v210, v210\n\t"
  "v_mfma_f32_16x16x32_bf16 v[80:83], v[164:167], a[40:43], v[80:83]\n\t"
  "ds_read_b128 v[52:55], v216 offset:41984\n\t"
  "v_add_f32 v210, 1.0, v210\n\t"
  "v_mul_f32 v212, v140, v140\n\t"
  "v_mfma_f32_16x16x32_bf16 v[84:87], v[160:163], a[36:39], v[84:87]\n\t"
  "ds_read_b128 v[56:59], v216 offset:43008\n\t"
  "v_mul_f32 v213, v141, v141\n\t"
  "v_mul_f32 v214, v142, v142\n\t"
  "v_mfma_f32_16x16x32_bf16 v[84:87], v[164:167], a[44:47], v[84:87]\n\t"
  "ds_read_b128 v[60:63], v216 offset:50176\n\t"
  "v_mul_f32 v215, v143, v143\n\t"
  "v_fma_f32 v212, v212, v207, v203\n\t"
  "v_mfma_f32_16x16x32_bf16 v[88:91], v[168:171], a[32:35], v[88:91]\n\t"
  "s_add_u32 s44, s54, 114688\n\t"
  "s_addc_u32 s45, s55, 0\n\t"
  "v_fma_f32 v213, v213, v207, v203\n\t"
  "v_mfma_f32_16x16x32_bf16 v[88:91], v[172:175], a[40:43], v[88:91]\n\t"
  "s_add_u32 m0, s48, 114688\n\t"
  "v_fma_f32 v214, v214, v207, v203\n\t"
  "v_fma_f32 v215, v215, v207, v203\n\t"
  "v_mfma_f32_16x16x32_bf16 v[92:95], v[168:171], a[36:39], v[92:95]\n\t"
  "v_exp_f32 v211, v211\n\t"
  "v_mfma_f32_16x16x32_bf16 v[92:95], v[172:175], a[44:47], v[92:95]\n\t"
  "global_load_lds_dwordx4 v230, s[44:45] offset:0 sc1\n\t"
  "v_add_f32 v211, 1.0, v211\n\t"
  "s_waitcnt lgkmcnt(4)\n\t"
  "v_mfma_f32_16x16x32_bf16 v[96:99], v[32:35], a[32:35], v[96:99]\n\t"
  "ds_read_b128 v[160:163], v216 offset:44032\n\t"
  "v_mul_f32 v212, v140, v212\n\t"
  "v_mul_f32 v213, v141, v213\n\t"
  "v_mfma_f32_16x16x32_bf16 v[96:99], v[36:39], a[40:43], v[96:99]\n\t"
  "ds_read_b128 v[164:167], v216 offset:51200\n\t"
  "v_mul_f32 v214, v142, v214\n\t"
  "v_mul_f32 v215, v143, v215\n\t"
  "v_mfma_f32_16x16x32_bf16 v[100:103], v[32:35], a[36:39], v[100:103]\n\t"
  "ds_read_b128 v[168:171], v216 offset:45056\n\t"
  "v_rcp_f32 v208, v208\n\t"
  "v_mfma_f32_16x16x32_bf16 v[100:103], v[36:39], a[44:47], v[100:103]\n\t"
  "ds_read_b128 v[172:175], v216 offset:52224\n\t"
  "v_mul_f32 v152, v152, v208\n\t"
  "v_mfma_f32_16x16x32_bf16 v[104:107], v[40:43], a[32:35], v[104:107]\n\t"
  "global_load_lds_dwordx4 v230, s[44:45] offset:1024 sc1\n\t"
  "v_mul_f32 v156, v140, v156\n\t"
  "v_mul_f32 v157, v141, v157\n\t"
  "v_mfma_f32_16x16x32_bf16 v[104:107], v[44:47], a[40:43], v[104:107]\n\t"
  "v_rcp_f32 v209, v209\n\t"
  "v_mul_f32 v158, v142, v158\n\t"
  "v_mfma_f32_16x16x32_bf16 v[108:111], v[40:43], a[36:39], v[108:111]\n\t"
  "global_load_lds_dwordx4 v230, s[44:45] offset:2048 sc1\n\t"
  "v_mul_f32 v153, v153, v209\n\t"
  "v_cvt_pk_bf16_f32 v192, v152, v153\n\t"
  "v_mfma_f32_16x16x32_bf16 v[108:111], v[44:47], a[44:47], v[108:111]\n\t"
  "global_load_lds_dwordx4 v230, s[44:45] offset:3072 sc1\n\t"
  "s_waitcnt lgkmcnt(4)\n\t"
  "v_mfma_f32_16x16x32_bf16 v[112:115], v[48:51], a[32:35], v[112:115]\n\t"
  "ds_read_b128 v[32:35], v216 offset:46080\n\t"
  "v_mul_f32 v159, v143, v159\n\t"
  "v_mfma_f32_16x16x32_bf16 v[112:115], v[52:55], a[40:43], v[112:115]\n\t"
  "ds_read_b128 v[36:39], v216 offset:53248\n\t"
  "v_rcp_f32 v210, v210\n\t"
  "v_mfma_f32_16x16x32_bf16 v[116:119], v[48:51], a[36:39], v[116:119]\n\t"
  "ds_read_b128 v[40:43], v216 offset:47104\n\t"
  "v_mul_f32 v154, v154, v210\n\t"
  "v_mfma_f32_16x16x32_bf16 v[116:119], v[52:55], a[44:47], v[116:119]\n\t"
  "ds_read_b128 v[44:47], v216 offset:54272\n\t"
  "v_rcp_f32 v211, v211\n\t"
  "v_mfma_f32_16x16x32_bf16 v[64:67], v[56:59], a[48:51], v[64:67]\n\t"
  "v_exp_f32 v212, v212\n\t"
  "v_mul_f32 v155, v155, v211\n\t"
  "v_mfma_f32_16x16x32_bf16 v[64:67], v[60:63], a[56:59], v[64:67]\n\t"
  "s_add_u32 m0, s48, 118784\n\t"
  "v_cvt_pk_bf16_f32 v193, v154, v155\n\t"
  "v_add_f32 v212, 1.0, v212\n\t"
  "v_mfma_f32_16x16x32_bf16 v[68:71], v[56:59], a[52:55], v[68:71]\n\t"
  "global_load_lds_dwordx4 v231, s[44:45] offset:0 sc1\n\t"
  "global_store_dwordx2 v222, v[192:193], s[38:39] offset:-2048 sc1\n\t"
  "v_mfma_f32_16x16x32_bf16 v[68:71], v[60:63], a[60:63], v[68:71]\n\t"
  "v_exp_f32 v213, v213\n\t"
  "s_waitcnt lgkmcnt(4)\n\t"
  "v_mfma_f32_16x16x32_bf16 v[72:75], v[160:163], a[48:51], v[72:75]\n\t"
  "ds_read_b128 v[48:51], v216 offset:48128\n\t"
  "v_add_f32 v213, 1.0, v213\n\t"
  "v_mfma_f32_16x16x32_bf16 v[72:75], v[164:167], a[56:59], v[72:75]\n\t"
  "ds_read_b128 v[52:55], v216 offset:55296\n\t"
  "v_exp_f32 v214, v214\n\t"
  "v_mfma_f32_16x16x32_bf16 v[76:79], v[160:163], a[52:55], v[76:79]\n\t"
  "ds_read_b128 v[56:59], v216 offset:49152\n\t"
  "v_add_f32 v214, 1.0, v214\n\t"
  "v_mfma_f32_16x16x32_bf16 v[76:79], v[164:167], a[60:63], v[76:79]\n\t"
  "ds_read_b128 v[60:63], v216 offset:56320\n\t"
  "s_add_u32 s28, s58, 98304\n\t"
  "s_addc_u32 s29, s59, 0\n\t"
  "v_mfma_f32_16x16x32_bf16 v[80:83], v[168:171], a[48:51], v[80:83]\n\t"
  "global_load_lds_dwordx4 v231, s[44:45] offset:1024 sc1\n\t"
  "v_exp_f32 v215, v215\n\t"
  "v_mfma_f32_16x16x32_bf16 v[80:83], v[172:175], a[56:59], v[80:83]\n\t"
  "global_load_lds_dwordx4 v231, s[44:45] offset:2048 sc1\n\t"
  "v_add_f32 v215, 1.0, v215\n\t"
  "v_mfma_f32_16x16x32_bf16 v[84:87], v[168:171], a[52:55], v[84:87]\n\t"
  "v_rcp_f32 v212, v212\n\t"
  "v_mfma_f32_16x16x32_bf16 v[84:87], v[172:175], a[60:63], v[84:87]\n\t"
  "v_mul_f32 v156, v156, v212\n\t"
  "s_waitcnt vmcnt(25) lgkmcnt(4)\n\t"
  "s_barrier\n\t"
  "v_mfma_f32_16x16x32_bf16 v[88:91], v[32:35], a[48:51], v[88:91]\n\t"
  "ds_read_b128 v[160:163], v216 offset:57344\n\t"
  "v_rcp_f32 v213, v213\n\t"
  "v_mfma_f32_16x16x32_bf16 v[88:91], v[36:39], a[56:59], v[88:91]\n\t"
  "ds_read_b128 v[164:167], v216 offset:64512\n\t"
  "v_mul_f32 v157, v157, v213\n\t"
  "v_cvt_pk_bf16_f32 v196, v156, v157\n\t"
  "v_mfma_f32_16x16x32_bf16 v[92:95], v[32:35], a[52:55], v[92:95]\n\t"
  "ds_read_b128 v[168:171], v216 offset:58368\n\t"
  "v_rcp_f32 v214, v214\n\t"
  "v_mfma_f32_16x16x32_bf16 v[92:95], v[36:39], a[60:63], v[92:95]\n\t"
  "ds_read_b128 v[172:175], v219 offset:0\n\t"
  "v_mul_f32 v158, v158, v214\n\t"
  "v_mfma_f32_16x16x32_bf16 v[96:99], v[40:43], a[48:51], v[96:99]\n\t"
  "global_load_dword v243, v235, s[28:29] offset:0 sc1\n\t"
  "v_rcp_f32 v215, v215\n\t"
  "v_mfma_f32_16x16x32_bf16 v[96:99], v[44:47], a[56:59], v[96:99]\n\t"
  "global_load_dwordx4 a[152:155], v218, s[50:51] offset:-4096 sc1\n\t"
  "v_mul_f32 v159, v159, v215\n\t"
  "v_cvt_pk_bf16_f32 v197, v158, v159\n\t"
  "v_mfma_f32_16x16x32_bf16 v[100:103], v[40:43], a[52:55], v[100:103]\n\t"
  "global_load_dwordx4 a[156:159], v218, s[52:53] offset:-4096 sc1\n\t"
  "global_store_dwordx2 v222, v[196:197], s[40:41] offset:-2048 sc1\n\t"
  "v_mfma_f32_16x16x32_bf16 v[100:103], v[44:47], a[60:63], v[100:103]\n\t"
  "global_load_dwordx4 a[160:163], v218, s[50:51] offset:0 sc1\n\t"
  "s_waitcnt lgkmcnt(4)\n\t"
  "v_mfma_f32_16x16x32_bf16 v[104:107], v[48:51], a[48:51], v[104:107]\n\t"
  "ds_read_b128 v[32:35], v216 offset:59392\n\t"
  "v_mfma_f32_16x16x32_bf16 v[104:107], v[52:55], a[56:59], v[104:107]\n\t"
  "ds_read_b128 v[36:39], v219 offset:1024\n\t"
  "v_mfma_f32_16x16x32_bf16 v[108:111], v[48:51], a[52:55], v[108:111]\n\t"
  "ds_read_b128 v[40:43], v216 offset:60416\n\t"
  "v_mfma_f32_16x16x32_bf16 v[108:111], v[52:55], a[60:63], v[108:111]\n\t"
  "ds_read_b128 v[44:47], v219 offset:2048\n\t"
  "v_mfma_f32_16x16x32_bf16 v[112:115], v[56:59], a[48:51], v[112:115]\n\t"
  "global_load_dwordx4 a[164:167], v218, s[52:53] offset:0 sc1\n\t"
  "v_mfma_f32_16x16x32_bf16 v[112:115], v[60:63], a[56:59], v[112:115]\n\t"
  "v_mfma_f32_16x16x32_bf16 v[116:119], v[56:59], a[52:55], v[116:119]\n\t"
  "global_load_dwordx4 a[168:171], v228, s[50:51] offset:-3072 sc1\n\t"
  "v_mfma_f32_16x16x32_bf16 v[116:119], v[60:63], a[60:63], v[116:119]\n\t"
  "global_load_dwordx4 a[172:175], v228, s[52:53] offset:-3072 sc1\n\t"
  "s_waitcnt lgkmcnt(4)\n\t"
  "v_mfma_f32_16x16x32_bf16 v[64:67], v[160:163], a[64:67], v[64:67]\n\t"
  "ds_read_b128 v[48:51], v216 offset:61440\n\t"
  "v_mfma_f32_16x16x32_bf16 v[64:67], v[164:167], a[72:75], v[64:67]\n\t"
  "ds_read_b128 v[52:55], v219 offset:3072\n\t"
  "v_mfma_f32_16x16x32_bf16 v[68:71], v[160:163], a[68:71], v[68:71]\n\t"
  "ds_read_b128 v[56:59], v216 offset:62464\n\t"
  "v_mfma_f32_16x16x32_bf16 v[68:71], v[164:167], a[76:79], v[68:71]\n\t"
  "ds_read_b128 v[60:63], v219 offset:4096\n\t"
  "v_mfma_f32_16x16x32_bf16 v[72:75], v[168:171], a[64:67], v[72:75]\n\t"
  "global_load_dwordx4 a[176:179], v228, s[50:51] offset:1024 sc1\n\t"
  "v_mfma_f32_16x16x32_bf16 v[72:75], v[172:175], a[72:75], v[72:75]\n\t"
  "v_mfma_f32_16x16x32_bf16 v[76:79], v[168:171], a[68:71], v[76:79]\n\t"
  "global_load_dwordx4 a[180:183], v228, s[52:53] offset:1024 sc1\n\t"
  "v_mfma_f32_16x16x32_bf16 v[76:79], v[172:175], a[76:79], v[76:79]\n\t"
  "global_load_dwordx4 a[184:187], v229, s[50:51] offset:-2048 sc1\n\t"
  "s_waitcnt lgkmcnt(4)\n\t"
  "v_mfma_f32_16x16x32_bf16 v[80:83], v[32:35], a[64:67], v[80:83]\n\t"
  "ds_read_b128 v[160:163], v216 offset:63488\n\t"
  "v_mfma_f32_16x16x32_bf16 v[80:83], v[36:39], a[72:75], v[80:83]\n\t"
  "ds_read_b128 v[164:167], v219 offset:5120\n\t"
  "v_mfma_f32_16x16x32_bf16 v[84:87], v[32:35], a[68:71], v[84:87]\n\t"
  "ds_read_b128 v[168:171], v219 offset:6144\n\t"
  "v_mfma_f32_16x16x32_bf16 v[84:87], v[36:39], a[76:79], v[84:87]\n\t"
  "ds_read_b128 v[172:175], v219 offset:13312\n\t"
  "v_mfma_f32_16x16x32_bf16 v[88:91], v[40:43], a[64:67], v[88:91]\n\t"
  "global_load_dwordx4 a[188:191], v229, s[52:53] offset:-2048 sc1\n\t"
  "v_mfma_f32_16x16x32_bf16 v[88:91], v[44:47], a[72:75], v[88:91]\n\t"
  "v_mfma_f32_16x16x32_bf16 v[92:95], v[40:43], a[68:71], v[92:95]\n\t"
  "s_add_u32 s44, s54, 143360\n\t"
  "s_addc_u32 s45, s55, 0\n\t"
  "v_mfma_f32_16x16x32_bf16 v[92:95], v[44:47], a[76:79], v[92:95]\n\t"
  "s_add_u32 m0, s48, 0\n\t"
  "s_waitcnt lgkmcnt(4)\n\t"
  "v_mfma_f32_16x16x32_bf16 v[96:99], v[48:51], a[64:67], v[96:99]\n\t"
  "ds_read_b128 v[32:35], v219 offset:7168\n\t"
  "v_mfma_f32_16x16x32_bf16 v[96:99], v[52:55], a[72:75], v[96:99]\n\t"
  "ds_read_b128 v[36:39], v219 offset:14336\n\t"
  "v_mfma_f32_16x16x32_bf16 v[100:103], v[48:51], a[68:71], v[100:103]\n\t"
  "ds_read_b128 v[40:43], v219 offset:8192\n\t"
  "v_mfma_f32_16x16x32_bf16 v[100:103], v[52:55], a[76:79], v[100:103]\n\t"
  "ds_read_b128 v[44:47], v219 offset:15360\n\t"
  "v_mfma_f32_16x16x32_bf16 v[104:107], v[56:59], a[64:67], v[104:107]\n\t"
  "global_load_lds_dwordx4 v230, s[44:45] offset:0 sc1\n\t"
  "v_mfma_f32_16x16x32_bf16 v[104:107], v[60:63], a[72:75], v[104:107]\n\t"
  "global_load_lds_dwordx4 v230, s[44:45] offset:1024 sc1\n\t"
  "v_mfma_f32_16x16x32_bf16 v[108:111], v[56:59], a[68:71], v[108:111]\n\t"
  "v_mfma_f32_16x16x32_bf16 v[108:111], v[60:63], a[76:79], v[108:111]\n\t"
  "global_load_lds_dwordx4 v230, s[44:45] offset:2048 sc1\n\t"
  "s_waitcnt lgkmcnt(4)\n\t"
  "v_mfma_f32_16x16x32_bf16 v[112:115], v[160:163], a[64:67], v[112:115]\n\t"
  "ds_read_b128 v[48:51], v219 offset:9216\n\t"
  "v_mfma_f32_16x16x32_bf16 v[112:115], v[164:167], a[72:75], v[112:115]\n\t"
  "ds_read_b128 v[52:55], v219 offset:16384\n\t"
  "v_mfma_f32_16x16x32_bf16 v[116:119], v[160:163], a[68:71], v[116:119]\n\t"
  "ds_read_b128 v[56:59], v219 offset:10240\n\t"
  "v_mfma_f32_16x16x32_bf16 v[116:119], v[164:167], a[76:79], v[116:119]\n\t"
  "ds_read_b128 v[60:63], v219 offset:17408\n\t"
  "v_mfma_f32_16x16x32_bf16 v[64:67], v[168:171], a[80:83], v[64:67]\n\t"
  "global_load_lds_dwordx4 v230, s[44:45] offset:3072 sc1\n\t"
  "v_mfma_f32_16x16x32_bf16 v[64:67], v[172:175], a[88:91], v[64:67]\n\t"
  "s_add_u32 m0, s48, 4096\n\t"
  "v_mfma_f32_16x16x32_bf16 v[68:71], v[168:171], a[84:87], v[68:71]\n\t"
  "v_mfma_f32_16x16x32_bf16 v[68:71], v[172:175], a[92:95], v[68:71]\n\t"
  "global_load_lds_dwordx4 v231, s[44:45] offset:0 sc1\n\t"
  "s_waitcnt lgkmcnt(4)\n\t"
  "v_mfma_f32_16x16x32_bf16 v[72:75], v[32:35], a[80:83], v[72:75]\n\t"
  "ds_read_b128 v[160:163], v219 offset:11264\n\t"
  "v_mfma_f32_16x16x32_bf16 v[72:75], v[36:39], a[88:91], v[72:75]\n\t"
  "ds_read_b128 v[164:167], v219 offset:18432\n\t"
  "v_mfma_f32_16x16x32_bf16 v[76:79], v[32:35], a[84:87], v[76:79]\n\t"
  "ds_read_b128 v[168:171], v219 offset:12288\n\t"
  "v_mfma_f32_16x16x32_bf16 v[76:79], v[36:39], a[92:95], v[76:79]\n\t"
  "ds_read_b128 v[172:175], v219 offset:19456\n\t"
  "v_mfma_f32_16x16x32_bf16 v[80:83], v[40:43], a[80:83], v[80:83]\n\t"
  "global_load_lds_dwordx4 v231, s[44:45] offset:1024 sc1\n\t"
  "v_mfma_f32_16x16x32_bf16 v[80:83], v[44:47], a[88:91], v[80:83]\n\t"
  "global_load_lds_dwordx4 v231, s[44:45] offset:2048 sc1\n\t"
  "s_mul_i32 s14, s16, 4\n\t"
  "s_add_u32 s26, s58, s14\n\t"
  "s_addc_u32 s27, s59, 0\n\t"
  "v_mfma_f32_16x16x32_bf16 v[84:87], v[40:43], a[84:87], v[84:87]\n\t"
  "v_mfma_f32_16x16x32_bf16 v[84:87], v[44:47], a[92:95], v[84:87]\n\t"
  "s_waitcnt vmcnt(15) lgkmcnt(4)\n\t"
  "s_barrier\n\t"
  "v_mfma_f32_16x16x32_bf16 v[88:91], v[48:51], a[80:83], v[88:91]\n\t"
  "ds_read_b128 v[32:35], v219 offset:20480\n\t"
  "v_mfma_f32_16x16x32_bf16 v[88:91], v[52:55], a[88:91], v[88:91]\n\t"
  "ds_read_b128 v[36:39], v219 offset:27648\n\t"
  "v_mfma_f32_16x16x32_bf16 v[92:95], v[48:51], a[84:87], v[92:95]\n\t"
  "ds_read_b128 v[40:43], v219 offset:21504\n\t"
  "v_cmp_gt_u32 vcc, 1, v243\n\t"
  "v_mfma_f32_16x16x32_bf16 v[92:95], v[52:55], a[92:95], v[92:95]\n\t"
  "ds_read_b128 v[44:47], v219 offset:28672\n\t"
  "v_mfma_f32_16x16x32_bf16 v[96:99], v[56:59], a[80:83], v[96:99]\n\t"
  "v_mfma_f32_16x16x32_bf16 v[96:99], v[60:63], a[88:91], v[96:99]\n\t"
  "s_cbranch_vccnz RETRYC_%=\n\t"
  "BACKC_%=:\n\t"
  "global_store_dword v236, v244, s[26:27] offset:0 sc1\n\t"
  "v_mfma_f32_16x16x32_bf16 v[100:103], v[56:59], a[84:87], v[100:103]\n\t"
  "global_load_dwordx4 a[192:195], v216, s[50:51] offset:-1024 sc1\n\t"
  "v_mfma_f32_16x16x32_bf16 v[100:103], v[60:63], a[92:95], v[100:103]\n\t"
  "global_load_dwordx4 a[196:199], v216, s[52:53] offset:-1024 sc1\n\t"
  "s_waitcnt lgkmcnt(4)\n\t"
  "v_mfma_f32_16x16x32_bf16 v[104:107], v[160:163], a[80:83], v[104:107]\n\t"
  "ds_read_b128 v[48:51], v219 offset:22528\n\t"
  "v_mfma_f32_16x16x32_bf16 v[104:107], v[164:167], a[88:91], v[104:107]\n\t"
  "ds_read_b128 v[52:55], v219 offset:29696\n\t"
  "v_mfma_f32_16x16x32_bf16 v[108:111], v[160:163], a[84:87], v[108:111]\n\t"
  "ds_read_b128 v[56:59], v219 offset:23552\n\t"
  "v_mfma_f32_16x16x32_bf16 v[108:111], v[164:167], a[92:95], v[108:111]\n\t"
  "ds_read_b128 v[60:63], v219 offset:30720\n\t"
  "v_mfma_f32_16x16x32_bf16 v[112:115], v[168:171], a[80:83], v[112:115]\n\t"
  "global_load_dwordx4 a[200:203], v217, s[50:51] offset:-4096 sc1\n\t"
  "v_mfma_f32_16x16x32_bf16 v[112:115], v[172:175], a[88:91], v[112:115]\n\t"
  "v_mfma_f32_16x16x32_bf16 v[116:119], v[168:171], a[84:87], v[116:119]\n\t"
  "global_load_dwordx4 a[204:207], v217, s[52:53] offset:-4096 sc1\n\t"
  "v_mfma_f32_16x16x32_bf16 v[116:119], v[172:175], a[92:95], v[116:119]\n\t"
  "global_load_dwordx4 a[208:211], v217, s[50:51] offset:0 sc1\n\t"
  "s_waitcnt lgkmcnt(4)\n\t"
  "v_mfma_f32_16x16x32_bf16 v[64:67], v[32:35], a[96:99], v[64:67]\n\t"
  "ds_read_b128 v[160:163], v219 offset:24576\n\t"
  "v_mfma_f32_16x16x32_bf16 v[64:67], v[36:39], a[104:107], v[64:67]\n\t"
  "ds_read_b128 v[164:167], v219 offset:31744\n\t"
  "v_mfma_f32_16x16x32_bf16 v[68:71], v[32:35], a[100:103], v[68:71]\n\t"
  "ds_read_b128 v[168:171], v219 offset:25600\n\t"
  "v_mfma_f32_16x16x32_bf16 v[68:71], v[36:39], a[108:111], v[68:71]\n\t"
  "ds_read_b128 v[172:175], v219 offset:32768\n\t"
  "v_mfma_f32_16x16x32_bf16 v[72:75], v[40:43], a[96:99], v[72:75]\n\t"
  "global_load_dwordx4 a[212:215], v217, s[52:53] offset:0 sc1\n\t"
  "v_mfma_f32_16x16x32_bf16 v[72:75], v[44:47], a[104:107], v[72:75]\n\t"
  "v_mfma_f32_16x16x32_bf16 v[76:79], v[40:43], a[100:103], v[76:79]\n\t"
  "global_load_dwordx4 a[216:219], v218, s[50:51] offset:-3072 sc1\n\t"
  "v_mfma_f32_16x16x32_bf16 v[76:79], v[44:47], a[108:111], v[76:79]\n\t"
  "global_load_dwordx4 a[220:223], v218, s[52:53] offset:-3072 sc1\n\t"
  "s_waitcnt lgkmcnt(4)\n\t"
  "v_mfma_f32_16x16x32_bf16 v[80:83], v[48:51], a[96:99], v[80:83]\n\t"
  "ds_read_b128 v[32:35], v219 offset:26624\n\t"
  "v_mfma_f32_16x16x32_bf16 v[80:83], v[52:55], a[104:107], v[80:83]\n\t"
  "ds_read_b128 v[36:39], v219 offset:33792\n\t"
  "v_mfma_f32_16x16x32_bf16 v[84:87], v[48:51], a[100:103], v[84:87]\n\t"
  "ds_read_b128 v[40:43], v219 offset:34816\n\t"
  "v_mfma_f32_16x16x32_bf16 v[84:87], v[52:55], a[108:111], v[84:87]\n\t"
  "ds_read_b128 v[44:47], v219 offset:41984\n\t"
  "v_mfma_f32_16x16x32_bf16 v[88:91], v[56:59], a[96:99], v[88:91]\n\t"
  "v_mfma_f32_16x16x32_bf16 v[88:91], v[60:63], a[104:107], v[88:91]\n\t"
  "s_add_u32 s44, s54, 172032\n\t"
  "s_addc_u32 s45, s55, 0\n\t"
  "v_mfma_f32_16x16x32_bf16 v[92:95], v[56:59], a[100:103], v[92:95]\n\t"
  "s_add_u32 m0, s48, 28672\n\t"
  "v_mfma_f32_16x16x32_bf16 v[92:95], v[60:63], a[108:111], v[92:95]\n\t"
  "global_load_lds_dwordx4 v230, s[44:45] offset:0 sc1\n\t"
  "s_waitcnt lgkmcnt(4)\n\t"
  "v_mfma_f32_16x16x32_bf16 v[96:99], v[160:163], a[96:99], v[96:99]\n\t"
  "ds_read_b128 v[48:51], v219 offset:35840\n\t"
  "v_mfma_f32_16x16x32_bf16 v[96:99], v[164:167], a[104:107], v[96:99]\n\t"
  "ds_read_b128 v[52:55], v219 offset:43008\n\t"
  "v_mfma_f32_16x16x32_bf16 v[100:103], v[160:163], a[100:103], v[100:103]\n\t"
  "ds_read_b128 v[56:59], v219 offset:36864\n\t"
  "v_mfma_f32_16x16x32_bf16 v[100:103], v[164:167], a[108:111], v[100:103]\n\t"
  "ds_read_b128 v[60:63], v219 offset:44032\n\t"
  "v_mfma_f32_16x16x32_bf16 v[104:107], v[168:171], a[96:99], v[104:107]\n\t"
  "v_mfma_f32_16x16x32_bf16 v[104:107], v[172:175], a[104:107], v[104:107]\n\t"
  "global_load_lds_dwordx4 v230, s[44:45] offset:1024 sc1\n\t"
  "v_mfma_f32_16x16x32_bf16 v[108:111], v[168:171], a[100:103], v[108:111]\n\t"
  "global_load_lds_dwordx4 v230, s[44:45] offset:2048 sc1\n\t"
  "v_mfma_f32_16x16x32_bf16 v[108:111], v[172:175], a[108:111], v[108:111]\n\t"
  "s_waitcnt lgkmcnt(4)\n\t"
  "v_mfma_f32_16x16x32_bf16 v[112:115], v[32:35], a[96:99], v[112:115]\n\t"
  "ds_read_b128 v[160:163], v219 offset:37888\n\t"
  "v_mfma_f32_16x16x32_bf16 v[112:115], v[36:39], a[104:107], v[112:115]\n\t"
  "ds_read_b128 v[164:167], v219 offset:45056\n\t"
  "v_mfma_f32_16x16x32_bf16 v[116:119], v[32:35], a[100:103], v[116:119]\n\t"
  "ds_read_b128 v[168:171], v219 offset:38912\n\t"
  "v_mfma_f32_16x16x32_bf16 v[116:119], v[36:39], a[108:111], v[116:119]\n\t"
  "ds_read_b128 v[172:175], v219 offset:46080\n\t"
  "v_mfma_f32_16x16x32_bf16 v[64:67], v[40:43], a[112:115], v[64:67]\n\t"
  "global_load_lds_dwordx4 v230, s[44:45] offset:3072 sc1\n\t"
  "v_mfma_f32_16x16x32_bf16 v[64:67], v[44:47], a[120:123], v[64:67]\n\t"
  "s_add_u32 m0, s48, 32768\n\t"
  "v_mfma_f32_16x16x32_bf16 v[68:71], v[40:43], a[116:119], v[68:71]\n\t"
  "global_load_lds_dwordx4 v231, s[44:45] offset:0 sc1\n\t"
  "v_mfma_f32_16x16x32_bf16 v[68:71], v[44:47], a[124:127], v[68:71]\n\t"
  "s_waitcnt lgkmcnt(4)\n\t"
  "v_mfma_f32_16x16x32_bf16 v[72:75], v[48:51], a[112:115], v[72:75]\n\t"
  "ds_read_b128 v[32:35], v219 offset:39936\n\t"
  "v_mfma_f32_16x16x32_bf16 v[72:75], v[52:55], a[120:123], v[72:75]\n\t"
  "ds_read_b128 v[36:39], v219 offset:47104\n\t"
  "v_mfma_f32_16x16x32_bf16 v[76:79], v[48:51], a[116:119], v[76:79]\n\t"
  "ds_read_b128 v[40:43], v219 offset:40960\n\t"
  "v_mfma_f32_16x16x32_bf16 v[76:79], v[52:55], a[124:127], v[76:79]\n\t"
  "ds_read_b128 v[44:47], v219 offset:48128\n\t"
  "s_add_u32 s28, s58, 131072\n\t"
  "s_addc_u32 s29, s59, 0\n\t"
  "v_mfma_f32_16x16x32_bf16 v[80:83], v[56:59], a[112:115], v[80:83]\n\t"
  "global_load_lds_dwordx4 v231, s[44:45] offset:1024 sc1\n\t"
  "v_mfma_f32_16x16x32_bf16 v[80:83], v[60:63], a[120:123], v[80:83]\n\t"
  "global_load_lds_dwordx4 v231, s[44:45] offset:2048 sc1\n\t"
  "v_mfma_f32_16x16x32_bf16 v[84:87], v[56:59], a[116:119], v[84:87]\n\t"
  "v_mfma_f32_16x16x32_bf16 v[84:87], v[60:63], a[124:127], v[84:87]\n\t"
  "s_waitcnt lgkmcnt(4)\n\t"
  "s_barrier\n\t"
  "v_mfma_f32_16x16x32_bf16 v[88:91], v[160:163], a[112:115], v[88:91]\n\t"
  "ds_read_b128 v[48:51], v219 offset:49152\n\t"
  "v_mfma_f32_16x16x32_bf16 v[88:91], v[164:167], a[120:123], v[88:91]\n\t"
  "ds_read_b128 v[52:55], v219 offset:56320\n\t"
  "v_mfma_f32_16x16x32_bf16 v[92:95], v[160:163], a[116:119], v[92:95]\n\t"
  "ds_read_b128 v[56:59], v219 offset:50176\n\t"
  "v_mfma_f32_16x16x32_bf16 v[92:95], v[164:167], a[124:127], v[92:95]\n\t"
  "ds_read_b128 v[60:63], v219 offset:57344\n\t"
  "v_mfma_f32_16x16x32_bf16 v[96:99], v[168:171], a[112:115], v[96:99]\n\t"
  "global_load_dword v225, v235, s[28:29] offset:0 sc1\n\t"
  "v_mfma_f32_16x16x32_bf16 v[96:99], v[172:175], a[120:123], v[96:99]\n\t"
  "global_load_dwordx4 a[224:227], v218, s[50:51] offset:1024 sc1\n\t"
  "v_mfma_f32_16x16x32_bf16 v[100:103], v[168:171], a[116:119], v[100:103]\n\t"
  "v_mfma_f32_16x16x32_bf16 v[100:103], v[172:175], a[124:127], v[100:103]\n\t"
  "global_load_dwordx4 a[228:231], v218, s[52:53] offset:1024 sc1\n\t"
  "s_waitcnt lgkmcnt(4)\n\t"
  "v_mfma_f32_16x16x32_bf16 v[104:107], v[32:35], a[112:115], v[104:107]\n\t"
  "ds_read_b128 v[160:163], v219 offset:51200\n\t"
  "v_mfma_f32_16x16x32_bf16 v[104:107], v[36:39], a[120:123], v[104:107]\n\t"
  "ds_read_b128 v[164:167], v219 offset:58368\n\t"
  "v_mfma_f32_16x16x32_bf16 v[108:111], v[32:35], a[116:119], v[108:111]\n\t"
  "ds_read_b128 v[168:171], v219 offset:52224\n\t"
  "v_mfma_f32_16x16x32_bf16 v[108:111], v[36:39], a[124:127], v[108:111]\n\t"
  "ds_read_b128 v[172:175], v219 offset:59392\n\t"
  "v_mfma_f32_16x16x32_bf16 v[112:115], v[40:43], a[112:115], v[112:115]\n\t"
  "v_mfma_f32_16x16x32_bf16 v[112:115], v[44:47], a[120:123], v[112:115]\n\t"
  "global_load_dwordx4 a[232:235], v228, s[50:51] offset:-2048 sc1\n\t"
  "v_mfma_f32_16x16x32_bf16 v[116:119], v[40:43], a[116:119], v[116:119]\n\t"
  "v_mfma_f32_16x16x32_bf16 v[116:119], v[44:47], a[124:127], v[116:119]\n\t"
  "global_load_dwordx4 a[236:239], v228, s[52:53] offset:-2048 sc1\n\t"
  "s_waitcnt lgkmcnt(4)\n\t"
  "v_mfma_f32_16x16x32_bf16 v[64:67], v[48:51], a[128:131], v[64:67]\n\t"
  "ds_read_b128 v[32:35], v219 offset:53248\n\t"
  "v_mfma_f32_16x16x32_bf16 v[64:67], v[52:55], a[136:139], v[64:67]\n\t"
  "ds_read_b128 v[36:39], v219 offset:60416\n\t"
  "v_mfma_f32_16x16x32_bf16 v[68:71], v[48:51], a[132:135], v[68:71]\n\t"
  "ds_read_b128 v[40:43], v219 offset:54272\n\t"
  "v_mfma_f32_16x16x32_bf16 v[68:71], v[52:55], a[140:143], v[68:71]\n\t"
  "ds_read_b128 v[44:47], v219 offset:61440\n\t"
  "v_mfma_f32_16x16x32_bf16 v[72:75], v[56:59], a[128:131], v[72:75]\n\t"
  "v_mfma_f32_16x16x32_bf16 v[72:75], v[60:63], a[136:139], v[72:75]\n\t"
  "global_load_dwordx4 a[240:243], v228, s[50:51] offset:2048 sc1\n\t"
  "v_mfma_f32_16x16x32_bf16 v[76:79], v[56:59], a[132:135], v[76:79]\n\t"
  "v_mfma_f32_16x16x32_bf16 v[76:79], v[60:63], a[140:143], v[76:79]\n\t"
  "global_load_dwordx4 a[244:247], v228, s[52:53] offset:2048 sc1\n\t"
  "s_waitcnt lgkmcnt(4)\n\t"
  "v_mfma_f32_16x16x32_bf16 v[80:83], v[160:163], a[128:131], v[80:83]\n\t"
  "ds_read_b128 v[48:51], v219 offset:55296\n\t"
  "v_mfma_f32_16x16x32_bf16 v[80:83], v[164:167], a[136:139], v[80:83]\n\t"
  "ds_read_b128 v[52:55], v219 offset:62464\n\t"
  "v_mfma_f32_16x16x32_bf16 v[84:87], v[160:163], a[132:135], v[84:87]\n\t"
  "ds_read_b128 v[56:59], v219 offset:63488\n\t"
  "v_mfma_f32_16x16x32_bf16 v[84:87], v[164:167], a[140:143], v[84:87]\n\t"
  "ds_read_b128 v[60:63], v220 offset:5120\n\t"
  "v_mfma_f32_16x16x32_bf16 v[88:91], v[168:171], a[128:131], v[88:91]\n\t"
  "v_mfma_f32_16x16x32_bf16 v[88:91], v[172:175], a[136:139], v[88:91]\n\t"
  "global_load_dwordx4 a[248:251], v229, s[50:51] offset:-1024 sc1\n\t"
  "v_mfma_f32_16x16x32_bf16 v[92:95], v[168:171], a[132:135], v[92:95]\n\t"
  "s_add_u32 s24, s42, 8192\n\t"
  "s_addc_u32 s25, s43, 0\n\t"
  "v_mfma_f32_16x16x32_bf16 v[92:95], v[172:175], a[140:143], v[92:95]\n\t"
  "global_load_dwordx4 a[252:255], v229, s[52:53] offset:-1024 sc1\n\t"
  "s_waitcnt lgkmcnt(4)\n\t"
  "v_mfma_f32_16x16x32_bf16 v[96:99], v[32:35], a[128:131], v[96:99]\n\t"
  "ds_read_b128 v[160:163], v219 offset:64512\n\t"
  "v_mfma_f32_16x16x32_bf16 v[96:99], v[36:39], a[136:139], v[96:99]\n\t"
  "ds_read_b128 v[164:167], v220 offset:6144\n\t"
  "v_mfma_f32_16x16x32_bf16 v[100:103], v[32:35], a[132:135], v[100:103]\n\t"
  "ds_read_b128 v[168:171], v220 offset:0\n\t"
  "v_mfma_f32_16x16x32_bf16 v[100:103], v[36:39], a[140:143], v[100:103]\n\t"
  "ds_read_b128 v[172:175], v220 offset:7168\n\t"
  "v_mfma_f32_16x16x32_bf16 v[104:107], v[40:43], a[128:131], v[104:107]\n\t"
  "v_mfma_f32_16x16x32_bf16 v[104:107], v[44:47], a[136:139], v[104:107]\n\t"
  "v_mfma_f32_16x16x32_bf16 v[108:111], v[40:43], a[132:135], v[108:111]\n\t"
  "global_load_dwordx4 v[204:207], v237, s[60:61] offset:0 sc1\n\t"
  "v_mfma_f32_16x16x32_bf16 v[108:111], v[44:47], a[140:143], v[108:111]\n\t"
  "global_load_dwordx2 v[212:213], v238, s[60:61] offset:0 sc1\n\t"
  "s_waitcnt lgkmcnt(4)\n\t"
  "v_mfma_f32_16x16x32_bf16 v[112:115], v[48:51], a[128:131], v[112:115]\n\t"
  "ds_read_b128 v[32:35], v220 offset:1024\n\t"
  "v_mfma_f32_16x16x32_bf16 v[112:115], v[52:55], a[136:139], v[112:115]\n\t"
  "ds_read_b128 v[36:39], v220 offset:8192\n\t"
  "v_mfma_f32_16x16x32_bf16 v[116:119], v[48:51], a[132:135], v[116:119]\n\t"
  "ds_read_b128 v[40:43], v220 offset:2048\n\t"
  "v_mfma_f32_16x16x32_bf16 v[116:119], v[52:55], a[140:143], v[116:119]\n\t"
  "ds_read_b128 v[44:47], v220 offset:9216\n\t"
  "v_mfma_f32_16x16x32_bf16 v[64:67], v[56:59], a[144:147], v[64:67]\n\t"
  "global_load_dword v224, v241, s[24:25] offset:0\n\t"
  "v_mfma_f32_16x16x32_bf16 v[64:67], v[60:63], a[152:155], v[64:67]\n\t"
  "global_load_dword v226, v241, s[24:25] offset:64\n\t"
  "v_mfma_f32_16x16x32_bf16 v[68:71], v[56:59], a[148:151], v[68:71]\n\t"
  "global_load_dword v241, v241, s[42:43] offset:0 sc1\n\t"
  "v_mfma_f32_16x16x32_bf16 v[68:71], v[60:63], a[156:159], v[68:71]\n\t"
  "s_add_u32 s60, s60, 18432\n\t"
  "s_addc_u32 s61, s61, 0\n\t"
  "s_waitcnt lgkmcnt(4)\n\t"
  "v_mfma_f32_16x16x32_bf16 v[72:75], v[160:163], a[144:147], v[72:75]\n\t"
  "ds_read_b128 v[48:51], v220 offset:3072\n\t"
  "v_mfma_f32_16x16x32_bf16 v[72:75], v[164:167], a[152:155], v[72:75]\n\t"
  "PG6_%=:\n\t"
  "s_cbranch_execz PGBACK_%=\n\t"
  "ds_read_b128 v[52:55], v220 offset:10240\n\t"
  "v_mfma_f32_16x16x32_bf16 v[76:79], v[160:163], a[148:151], v[76:79]\n\t"
  "ds_read_b128 v[56:59], v220 offset:4096\n\t"
  "v_mfma_f32_16x16x32_bf16 v[76:79], v[164:167], a[156:159], v[76:79]\n\t"
  "ds_read_b128 v[60:63], v220 offset:11264\n\t"
  "v_mfma_f32_16x16x32_bf16 v[80:83], v[168:171], a[144:147], v[80:83]\n\t"
  "global_load_dwordx4 v[208:211], v237, s[60:61] offset:0 sc1\n\t"
  "v_mfma_f32_16x16x32_bf16 v[80:83], v[172:175], a[152:155], v[80:83]\n\t"
  "global_load_dwordx2 v[214:215], v238, s[60:61] offset:0 sc1\n\t"
  "v_mfma_f32_16x16x32_bf16 v[84:87], v[168:171], a[148:151], v[84:87]\n\t"
  "global_load_dword v242, v242, s[42:43] offset:64 sc1\n\t"
  "v_mfma_f32_16x16x32_bf16 v[84:87], v[172:175], a[156:159], v[84:87]\n\t"
  "s_waitcnt vmcnt(16) lgkmcnt(4)\n\t"
  "s_barrier\n\t"
  "v_mfma_f32_16x16x32_bf16 v[88:91], v[32:35], a[144:147], v[88:91]\n\t"
  "ds_read_b128 v[160:163], v216 offset:0\n\t"
  "v_mfma_f32_16x16x32_bf16 v[88:91], v[36:39], a[152:155], v[88:91]\n\t"
  "ds_read_b128 v[164:167], v216 offset:7168\n\t"
  "v_mfma_f32_16x16x32_bf16 v[92:95], v[32:35], a[148:151], v[92:95]\n\t"
  "ds_read_b128 v[168:171], v216 offset:1024\n\t"
  "v_cmp_gt_u32 vcc, 1, v225\n\t"
  "v_mfma_f32_16x16x32_bf16 v[92:95], v[36:39], a[156:159], v[92:95]\n\t"
  "ds_read_b128 v[172:175], v216 offset:8192\n\t"
  "v_mfma_f32_16x16x32_bf16 v[96:99], v[40:43], a[144:147], v[96:99]\n\t"
  "v_mfma_f32_16x16x32_bf16 v[96:99], v[44:47], a[152:155], v[96:99]\n\t"
  "s_cbranch_vccnz RETRYD_%=\n\t"
  "BACKD_%=:\n\t"
  "v_mfma_f32_16x16x32_bf16 v[100:103], v[40:43], a[148:151], v[100:103]\n\t"
  "global_load_dwordx4 v[0:3], v216, s[50:51] offset:0 sc1\n\t"
  "v_mfma_f32_16x16x32_bf16 v[100:103], v[44:47], a[156:159], v[100:103]\n\t"
  "global_load_dwordx4 v[4:7], v216, s[52:53] offset:0 sc1\n\t"
  "s_waitcnt lgkmcnt(4)\n\t"
  "v_mfma_f32_16x16x32_bf16 v[104:107], v[48:51], a[144:147], v[104:107]\n\t"
  "ds_read_b128 v[32:35], v216 offset:2048\n\t"
  "v_mfma_f32_16x16x32_bf16 v[104:107], v[52:55], a[152:155], v[104:107]\n\t"
  "ds_read_b128 v[36:39], v216 offset:9216\n\t"
  "v_mfma_f32_16x16x32_bf16 v[108:111], v[48:51], a[148:151], v[108:111]\n\t"
  "ds_read_b128 v[40:43], v216 offset:3072\n\t"
  "v_mfma_f32_16x16x32_bf16 v[108:111], v[52:55], a[156:159], v[108:111]\n\t"
  "ds_read_b128 v[44:47], v216 offset:10240\n\t"
  "v_mfma_f32_16x16x32_bf16 v[112:115], v[56:59], a[144:147], v[112:115]\n\t"
  "global_load_dwordx4 v[8:11], v217, s[50:51] offset:1024 sc1\n\t"
  "v_mfma_f32_16x16x32_bf16 v[112:115], v[60:63], a[152:155], v[112:115]\n\t"
  "v_mfma_f32_16x16x32_bf16 v[116:119], v[56:59], a[148:151], v[116:119]\n\t"
  "global_load_dwordx4 v[12:15], v217, s[52:53] offset:1024 sc1\n\t"
  "v_mfma_f32_16x16x32_bf16 v[116:119], v[60:63], a[156:159], v[116:119]\n\t"
  "global_load_dwordx4 v[16:19], v218, s[50:51] offset:2048 sc1\n\t"
  "s_waitcnt lgkmcnt(4)\n\t"
  "v_mfma_f32_16x16x32_bf16 v[64:67], v[160:163], a[160:163], v[64:67]\n\t"
  "ds_read_b128 v[48:51], v216 offset:4096\n\t"
  "v_mfma_f32_16x16x32_bf16 v[64:67], v[164:167], a[168:171], v[64:67]\n\t"
  "ds_read_b128 v[52:55], v216 offset:11264\n\t"
  "v_mfma_f32_16x16x32_bf16 v[68:71], v[160:163], a[164:167], v[68:71]\n\t"
  "ds_read_b128 v[56:59], v216 offset:5120\n\t"
  "v_mfma_f32_16x16x32_bf16 v[68:71], v[164:167], a[172:175], v[68:71]\n\t"
  "ds_read_b128 v[60:63], v216 offset:12288\n\t"
  "v_mfma_f32_16x16x32_bf16 v[72:75], v[168:171], a[160:163], v[72:75]\n\t"
  "global_load_dwordx4 v[20:23], v218, s[52:53] offset:2048 sc1\n\t"
  "v_mfma_f32_16x16x32_bf16 v[72:75], v[172:175], a[168:171], v[72:75]\n\t"
  "v_mfma_f32_16x16x32_bf16 v[76:79], v[168:171], a[164:167], v[76:79]\n\t"
  "global_load_dwordx4 v[24:27], v228, s[50:51] offset:3072 sc1\n\t"
  "v_mfma_f32_16x16x32_bf16 v[76:79], v[172:175], a[172:175], v[76:79]\n\t"
  "global_load_dwordx4 v[28:31], v228, s[52:53] offset:3072 sc1\n\t"
  "s_waitcnt lgkmcnt(4)\n\t"
  "v_mfma_f32_16x16x32_bf16 v[80:83], v[32:35], a[160:163], v[80:83]\n\t"
  "ds_read_b128 v[160:163], v216 offset:6144\n\t"
  "v_mfma_f32_16x16x32_bf16 v[80:83], v[36:39], a[168:171], v[80:83]\n\t"
  "ds_read_b128 v[164:167], v216 offset:13312\n\t"
  "v_mfma_f32_16x16x32_bf16 v[84:87], v[32:35], a[164:167], v[84:87]\n\t"
  "ds_read_b128 v[168:171], v216 offset:14336\n\t"
  "v_mfma_f32_16x16x32_bf16 v[84:87], v[36:39], a[172:175], v[84:87]\n\t"
  "ds_read_b128 v[172:175], v216 offset:21504\n\t"
  "v_mfma_f32_16x16x32_bf16 v[88:91], v[40:43], a[160:163], v[88:91]\n\t"
  "v_mfma_f32_16x16x32_bf16 v[88:91], v[44:47], a[168:171], v[88:91]\n\t"
  "s_add_u32 s44, s54, 200704\n\t"
  "s_addc_u32 s45, s55, 0\n\t"
  "v_mfma_f32_16x16x32_bf16 v[92:95], v[40:43], a[164:167], v[92:95]\n\t"
  "s_add_u32 m0, s48, 57344\n\t"
  "v_mfma_f32_16x16x32_bf16 v[92:95], v[44:47], a[172:175], v[92:95]\n\t"
  "global_load_lds_dwordx4 v230, s[44:45] offset:0 sc1\n\t"
  "s_waitcnt lgkmcnt(4)\n\t"
  "v_mfma_f32_16x16x32_bf16 v[96:99], v[48:51], a[160:163], v[96:99]\n\t"
  "ds_read_b128 v[32:35], v216 offset:15360\n\t"
  "v_mfma_f32_16x16x32_bf16 v[96:99], v[52:55], a[168:171], v[96:99]\n\t"
  "ds_read_b128 v[36:39], v216 offset:22528\n\t"
  "v_mfma_f32_16x16x32_bf16 v[100:103], v[48:51], a[164:167], v[100:103]\n\t"
  "ds_read_b128 v[40:43], v216 offset:16384\n\t"
  "v_mfma_f32_16x16x32_bf16 v[100:103], v[52:55], a[172:175], v[100:103]\n\t"
  "ds_read_b128 v[44:47], v216 offset:23552\n\t"
  "v_mfma_f32_16x16x32_bf16 v[104:107], v[56:59], a[160:163], v[104:107]\n\t"
  "v_mfma_f32_16x16x32_bf16 v[104:107], v[60:63], a[168:171], v[104:107]\n\t"
  "global_load_lds_dwordx4 v230, s[44:45] offset:1024 sc1\n\t"
  "v_mfma_f32_16x16x32_bf16 v[108:111], v[56:59], a[164:167], v[108:111]\n\t"
  "global_load_lds_dwordx4 v230, s[44:45] offset:2048 sc1\n\t"
  "v_mfma_f32_16x16x32_bf16 v[108:111], v[60:63], a[172:175], v[108:111]\n\t"
  "s_waitcnt lgkmcnt(4)\n\t"
  "v_mfma_f32_16x16x32_bf16 v[112:115], v[160:163], a[160:163], v[112:115]\n\t"
  "ds_read_b128 v[48:51], v216 offset:17408\n\t"
  "v_mfma_f32_16x16x32_bf16 v[112:115], v[164:167], a[168:171], v[112:115]\n\t"
  "ds_read_b128 v[52:55], v216 offset:24576\n\t"
  "v_mfma_f32_16x16x32_bf16 v[116:119], v[160:163], a[164:167], v[116:119]\n\t"
  "ds_read_b128 v[56:59], v216 offset:18432\n\t"
  "v_mfma_f32_16x16x32_bf16 v[116:119], v[164:167], a[172:175], v[116:119]\n\t"
  "ds_read_b128 v[60:63], v216 offset:25600\n\t"
  "v_mfma_f32_16x16x32_bf16 v[64:67], v[168:171], a[176:179], v[64:67]\n\t"
  "global_load_lds_dwordx4 v230, s[44:45] offset:3072 sc1\n\t"
  "v_mfma_f32_16x16x32_bf16 v[64:67], v[172:175], a[184:187], v[64:67]\n\t"
  "s_add_u32 m0, s48, 61440\n\t"
  "v_mfma_f32_16x16x32_bf16 v[68:71], v[168:171], a[180:183], v[68:71]\n\t"
  "global_load_lds_dwordx4 v231, s[44:45] offset:0 sc1\n\t"
  "v_mfma_f32_16x16x32_bf16 v[68:71], v[172:175], a[188:191], v[68:71]\n\t"
  "s_waitcnt lgkmcnt(4)\n\t"
  "v_mfma_f32_16x16x32_bf16 v[72:75], v[32:35], a[176:179], v[72:75]\n\t"
  "ds_read_b128 v[160:163], v216 offset:19456\n\t"
  "v_mfma_f32_16x16x32_bf16 v[72:75], v[36:39], a[184:187], v[72:75]\n\t"
  "ds_read_b128 v[164:167], v216 offset:26624\n\t"
  "v_mfma_f32_16x16x32_bf16 v[76:79], v[32:35], a[180:183], v[76:79]\n\t"
  "ds_read_b128 v[168:171], v216 offset:20480\n\t"
  "v_mfma_f32_16x16x32_bf16 v[76:79], v[36:39], a[188:191], v[76:79]\n\t"
  "ds_read_b128 v[172:175], v216 offset:27648\n\t"
  "v_mfma_f32_16x16x32_bf16 v[80:83], v[40:43], a[176:179], v[80:83]\n\t"
  "global_load_lds_dwordx4 v231, s[44:45] offset:1024 sc1\n\t"
  "v_mfma_f32_16x16x32_bf16 v[80:83], v[44:47], a[184:187], v[80:83]\n\t"
  "global_load_lds_dwordx4 v231, s[44:45] offset:2048 sc1\n\t"
  "v_mfma_f32_16x16x32_bf16 v[84:87], v[40:43], a[180:183], v[84:87]\n\t"
  "v_mfma_f32_16x16x32_bf16 v[84:87], v[44:47], a[188:191], v[84:87]\n\t"
  "s_waitcnt vmcnt(23) lgkmcnt(4)\n\t"
  "s_barrier\n\t"
  "v_mfma_f32_16x16x32_bf16 v[88:91], v[48:51], a[176:179], v[88:91]\n\t"
  "ds_read_b128 v[32:35], v216 offset:28672\n\t"
  "v_mfma_f32_16x16x32_bf16 v[88:91], v[52:55], a[184:187], v[88:91]\n\t"
  "ds_read_b128 v[36:39], v216 offset:29696\n\t"
  "v_mfma_f32_16x16x32_bf16 v[92:95], v[48:51], a[180:183], v[92:95]\n\t"
  "ds_read_b128 v[40:43], v216 offset:30720\n\t"
  "v_mfma_f32_16x16x32_bf16 v[92:95], v[52:55], a[188:191], v[92:95]\n\t"
  "ds_read_b128 v[44:47], v216 offset:31744\n\t"
  "v_mfma_f32_16x16x32_bf16 v[96:99], v[56:59], a[176:179], v[96:99]\n\t"
  "s_add_u32 s44, s54, 229376\n\t"
  "s_addc_u32 s45, s55, 0\n\t"
  "v_mfma_f32_16x16x32_bf16 v[96:99], v[60:63], a[184:187], v[96:99]\n\t"
  "s_add_u32 m0, s48, 86016\n\t"
  "v_mfma_f32_16x16x32_bf16 v[100:103], v[56:59], a[180:183], v[100:103]\n\t"
  "global_load_lds_dwordx4 v230, s[44:45] offset:0 sc1\n\t"
  "v_mfma_f32_16x16x32_bf16 v[100:103], v[60:63], a[188:191], v[100:103]\n\t"
  "s_waitcnt lgkmcnt(4)\n\t"
  "v_mfma_f32_16x16x32_bf16 v[104:107], v[160:163], a[176:179], v[104:107]\n\t"
  "ds_read_b128 v[48:51], v216 offset:32768\n\t"
  "v_mfma_f32_16x16x32_bf16 v[104:107], v[164:167], a[184:187], v[104:107]\n\t"
  "ds_read_b128 v[52:55], v216 offset:33792\n\t"
  "v_mfma_f32_16x16x32_bf16 v[108:111], v[160:163], a[180:183], v[108:111]\n\t"
  "ds_read_b128 v[56:59], v216 offset:34816\n\t"
  "v_mfma_f32_16x16x32_bf16 v[108:111], v[164:167], a[188:191], v[108:111]\n\t"
  "ds_read_b128 v[60:63], v216 offset:35840\n\t"
  "v_mfma_f32_16x16x32_bf16 v[112:115], v[168:171], a[176:179], v[112:115]\n\t"
  "global_load_lds_dwordx4 v230, s[44:45] offset:1024 sc1\n\t"
  "v_mfma_f32_16x16x32_bf16 v[112:115], v[172:175], a[184:187], v[112:115]\n\t"
  "global_load_lds_dwordx4 v230, s[44:45] offset:2048 sc1\n\t"
  "v_mfma_f32_16x16x32_bf16 v[116:119], v[168:171], a[180:183], v[116:119]\n\t"
  "v_mfma_f32_16x16x32_bf16 v[116:119], v[172:175], a[188:191], v[116:119]\n\t"
  "global_load_lds_dwordx4 v230, s[44:45] offset:3072 sc1\n\t"
  "s_waitcnt lgkmcnt(4)\n\t"
  "v_mfma_f32_16x16x32_bf16 v[64:67], v[32:35], a[192:195], v[64:67]\n\t"
  "ds_read_b128 v[160:163], v216 offset:36864\n\t"
  "v_mfma_f32_16x16x32_bf16 v[64:67], v[36:39], a[200:203], v[64:67]\n\t"
  "ds_read_b128 v[164:167], v216 offset:37888\n\t"
  "v_mfma_f32_16x16x32_bf16 v[68:71], v[32:35], a[196:199], v[68:71]\n\t"
  "ds_read_b128 v[168:171], v216 offset:38912\n\t"
  "v_mfma_f32_16x16x32_bf16 v[68:71], v[36:39], a[204:207], v[68:71]\n\t"
  "ds_read_b128 v[172:175], v216 offset:39936\n\t"
  "v_mfma_f32_16x16x32_bf16 v[72:75], v[40:43], a[192:195], v[72:75]\n\t"
  "s_add_u32 m0, s48, 90112\n\t"
  "v_mfma_f32_16x16x32_bf16 v[72:75], v[44:47], a[200:203], v[72:75]\n\t"
  "global_load_lds_dwordx4 v231, s[44:45] offset:0 sc1\n\t"
  "v_mfma_f32_16x16x32_bf16 v[76:79], v[40:43], a[196:199], v[76:79]\n\t"
  "v_mfma_f32_16x16x32_bf16 v[76:79], v[44:47], a[204:207], v[76:79]\n\t"
  "global_load_lds_dwordx4 v231, s[44:45] offset:1024 sc1\n\t"
  "s_waitcnt lgkmcnt(4)\n\t"
  "v_mfma_f32_16x16x32_bf16 v[64:67], v[48:51], a[208:211], v[64:67]\n\t"
  "ds_read_b128 v[32:35], v216 offset:40960\n\t"
  "v_mfma_f32_16x16x32_bf16 v[64:67], v[52:55], a[216:219], v[64:67]\n\t"
  "ds_read_b128 v[36:39], v216 offset:41984\n\t"
  "v_mfma_f32_16x16x32_bf16 v[68:71], v[48:51], a[212:215], v[68:71]\n\t"
  "ds_read_b128 v[40:43], v216 offset:43008\n\t"
  "v_mfma_f32_16x16x32_bf16 v[68:71], v[52:55], a[220:223], v[68:71]\n\t"
  "ds_read_b128 v[44:47], v216 offset:44032\n\t"
  "v_mfma_f32_16x16x32_bf16 v[72:75], v[56:59], a[208:211], v[72:75]\n\t"
  "global_load_lds_dwordx4 v231, s[44:45] offset:2048 sc1\n\t"
  "v_mfma_f32_16x16x32_bf16 v[72:75], v[60:63], a[216:219], v[72:75]\n\t"
  "v_mfma_f32_16x16x32_bf16 v[76:79], v[56:59], a[212:215], v[76:79]\n\t"
  "s_add_u32 s44, s54, 258048\n\t"
  "s_addc_u32 s45, s55, 0\n\t"
  "v_mfma_f32_16x16x32_bf16 v[76:79], v[60:63], a[220:223], v[76:79]\n\t"
  "s_add_u32 m0, s49, 114688\n\t"
  "s_waitcnt lgkmcnt(4)\n\t"
  "v_mfma_f32_16x16x32_bf16 v[64:67], v[160:163], a[224:227], v[64:67]\n\t"
  "ds_read_b128 v[48:51], v216 offset:45056\n\t"
  "v_mfma_f32_16x16x32_bf16 v[64:67], v[164:167], a[232:235], v[64:67]\n\t"
  "ds_read_b128 v[52:55], v216 offset:46080\n\t"
  "v_mfma_f32_16x16x32_bf16 v[68:71], v[160:163], a[228:231], v[68:71]\n\t"
  "ds_read_b128 v[56:59], v216 offset:47104\n\t"
  "v_mfma_f32_16x16x32_bf16 v[68:71], v[164:167], a[236:239], v[68:71]\n\t"
  "ds_read_b128 v[60:63], v216 offset:48128\n\t"
  "v_mfma_f32_16x16x32_bf16 v[72:75], v[168:171], a[224:227], v[72:75]\n\t"
  "v_mfma_f32_16x16x32_bf16 v[72:75], v[172:175], a[232:235], v[72:75]\n\t"
  "global_load_lds_dwordx4 v232, s[44:45] offset:0 sc1\n\t"
  "v_mfma_f32_16x16x32_bf16 v[76:79], v[168:171], a[228:231], v[76:79]\n\t"
  "global_load_lds_dwordx4 v232, s[44:45] offset:1024 sc1\n\t"
  "v_mfma_f32_16x16x32_bf16 v[76:79], v[172:175], a[236:239], v[76:79]\n\t"
  "global_load_lds_dwordx4 v232, s[44:45] offset:2048 sc1\n\t"
  "s_waitcnt lgkmcnt(4)\n\t"
  "v_mfma_f32_16x16x32_bf16 v[64:67], v[32:35], a[240:243], v[64:67]\n\t"
  "ds_read_b128 v[160:163], v216 offset:49152\n\t"
  "v_mfma_f32_16x16x32_bf16 v[64:67], v[36:39], a[248:251], v[64:67]\n\t"
  "ds_read_b128 v[164:167], v216 offset:50176\n\t"
  "v_mfma_f32_16x16x32_bf16 v[68:71], v[32:35], a[244:247], v[68:71]\n\t"
  "ds_read_b128 v[168:171], v216 offset:51200\n\t"
  "v_mfma_f32_16x16x32_bf16 v[68:71], v[36:39], a[252:255], v[68:71]\n\t"
  "ds_read_b128 v[172:175], v216 offset:52224\n\t"
  "v_mfma_f32_16x16x32_bf16 v[72:75], v[40:43], a[240:243], v[72:75]\n\t"
  "v_mfma_f32_16x16x32_bf16 v[72:75], v[44:47], a[248:251], v[72:75]\n\t"
  "global_load_lds_dwordx4 v232, s[44:45] offset:3072 sc1\n\t"
  "v_mfma_f32_16x16x32_bf16 v[76:79], v[40:43], a[244:247], v[76:79]\n\t"
  "s_add_u32 m0, s49, 118784\n\t"
  "v_mfma_f32_16x16x32_bf16 v[76:79], v[44:47], a[252:255], v[76:79]\n\t"
  "s_waitcnt vmcnt(18)\n\t"
  "s_waitcnt lgkmcnt(4)\n\t"
  "v_mfma_f32_16x16x32_bf16 v[64:67], v[48:51], v[0:3], v[64:67]\n\t"
  "ds_read_b128 v[32:35], v216 offset:53248\n\t"
  "v_mfma_f32_16x16x32_bf16 v[64:67], v[52:55], v[8:11], v[64:67]\n\t"
  "ds_read_b128 v[36:39], v216 offset:54272\n\t"
  "v_mfma_f32_16x16x32_bf16 v[68:71], v[48:51], v[4:7], v[68:71]\n\t"
  "ds_read_b128 v[40:43], v216 offset:55296\n\t"
  "v_mfma_f32_16x16x32_bf16 v[68:71], v[52:55], v[12:15], v[68:71]\n\t"
  "ds_read_b128 v[44:47], v216 offset:56320\n\t"
  "v_mfma_f32_16x16x32_bf16 v[72:75], v[56:59], v[0:3], v[72:75]\n\t"
  "global_load_lds_dwordx4 v233, s[44:45] offset:0 sc1\n\t"
  "v_mfma_f32_16x16x32_bf16 v[72:75], v[60:63], v[8:11], v[72:75]\n\t"
  "global_load_lds_dwordx4 v233, s[44:45] offset:1024 sc1\n\t"
  "v_mfma_f32_16x16x32_bf16 v[76:79], v[56:59], v[4:7], v[76:79]\n\t"
  "v_mfma_f32_16x16x32_bf16 v[76:79], v[60:63], v[12:15], v[76:79]\n\t"
  "s_waitcnt vmcnt(13) lgkmcnt(4)\n\t"
  "s_barrier\n\t"
  "v_mfma_f32_16x16x32_bf16 v[64:67], v[160:163], v[16:19], v[64:67]\n\t"
  "ds_read_b128 v[48:51], v216 offset:57344\n\t"
  "v_mfma_f32_16x16x32_bf16 v[64:67], v[164:167], v[24:27], v[64:67]\n\t"
  "ds_read_b128 v[52:55], v216 offset:58368\n\t"
  "v_mfma_f32_16x16x32_bf16 v[68:71], v[160:163], v[20:23], v[68:71]\n\t"
  "ds_read_b128 v[56:59], v216 offset:59392\n\t"
  "s_add_u32 s44, s54, 282624\n\t"
  "s_addc_u32 s45, s55, 0\n\t"
  "v_mfma_f32_16x16x32_bf16 v[68:71], v[164:167], v[28:31], v[68:71]\n\t"
  "ds_read_b128 v[60:63], v216 offset:60416\n\t"
  "s_add_u32 m0, s49, 0\n\t"
  "v_mfma_f32_16x16x32_bf16 v[72:75], v[168:171], v[16:19], v[72:75]\n\t"
  "global_load_lds_dwordx4 v232, s[44:45] offset:0 sc1\n\t"
  "v_mfma_f32_16x16x32_bf16 v[72:75], v[172:175], v[24:27], v[72:75]\n\t"
  "global_load_lds_dwordx4 v232, s[44:45] offset:1024 sc1\n\t"
  "v_mfma_f32_16x16x32_bf16 v[76:79], v[168:171], v[20:23], v[76:79]\n\t"
  "global_load_lds_dwordx4 v232, s[44:45] offset:2048 sc1\n\t"
  "v_mfma_f32_16x16x32_bf16 v[76:79], v[172:175], v[28:31], v[76:79]\n\t"
  "global_load_lds_dwordx4 v232, s[44:45] offset:3072 sc1\n\t"
  "s_waitcnt lgkmcnt(4)\n\t"
  "v_mfma_f32_16x16x32_bf16 v[112:115], v[32:35], a[192:195], v[112:115]\n\t"
  "ds_read_b128 v[160:163], v216 offset:61440\n\t"
  "s_add_u32 m0, s49, 4096\n\t"
  "v_mfma_f32_16x16x32_bf16 v[112:115], v[36:39], a[200:203], v[112:115]\n\t"
  "ds_read_b128 v[164:167], v216 offset:62464\n\t"
  "global_load_lds_dwordx4 v233, s[44:45] offset:0 sc1\n\t"
  "v_mfma_f32_16x16x32_bf16 v[116:119], v[32:35], a[196:199], v[116:119]\n\t"
  "ds_read_b128 v[168:171], v216 offset:63488\n\t"
  "global_load_lds_dwordx4 v233, s[44:45] offset:1024 sc1\n\t"
  "v_mfma_f32_16x16x32_bf16 v[116:119], v[36:39], a[204:207], v[116:119]\n\t"
  "ds_read_b128 v[172:175], v216 offset:64512\n\t"
  "v_mfma_f32_16x16x32_bf16 v[112:115], v[40:43], a[208:211], v[112:115]\n\t"
  "v_cvt_pk_bf16_f32 v184, v64, v65\n\t"
  "v_cvt_pk_bf16_f32 v185, v66, v67\n\t"
  "v_mfma_f32_16x16x32_bf16 v[112:115], v[44:47], a[216:219], v[112:115]\n\t"
  "v_cvt_pk_bf16_f32 v186, v72, v73\n\t"
  "v_cvt_pk_bf16_f32 v187, v74, v75\n\t"
  "v_mfma_f32_16x16x32_bf16 v[116:119], v[40:43], a[212:215], v[116:119]\n\t"
  "global_store_dwordx4 v245, v[184:187], s[56:57] offset:0 sc1\n\t"
  "v_cvt_pk_bf16_f32 v188, v68, v69\n\t"
  "v_mfma_f32_16x16x32_bf16 v[116:119], v[44:47], a[220:223], v[116:119]\n\t"
  "v_cvt_pk_bf16_f32 v189, v70, v71\n\t"
  "s_waitcnt lgkmcnt(4)\n\t"
  "v_mfma_f32_16x16x32_bf16 v[80:83], v[48:51], a[192:195], v[80:83]\n\t"
  "ds_read_b128 v[32:35], v219 offset:0\n\t"
  "v_cvt_pk_bf16_f32 v190, v76, v77\n\t"
  "v_mfma_f32_16x16x32_bf16 v[80:83], v[52:55], a[200:203], v[80:83]\n\t"
  "ds_read_b128 v[36:39], v219 offset:1024\n\t"
  "v_cvt_pk_bf16_f32 v191, v78, v79\n\t"
  "v_mfma_f32_16x16x32_bf16 v[84:87], v[48:51], a[196:199], v[84:87]\n\t"
  "ds_read_b128 v[40:43], v219 offset:2048\n\t"
  "global_store_dwordx4 v245, v[188:191], s[56:57] offset:1024 sc1\n\t"
  "v_mfma_f32_16x16x32_bf16 v[84:87], v[52:55], a[204:207], v[84:87]\n\t"
  "ds_read_b128 v[44:47], v219 offset:3072\n\t"
  "v_mfma_f32_16x16x32_bf16 v[88:91], v[56:59], a[192:195], v[88:91]\n\t"
  "v_mfma_f32_16x16x32_bf16 v[88:91], v[60:63], a[200:203], v[88:91]\n\t"
  "v_mfma_f32_16x16x32_bf16 v[92:95], v[56:59], a[196:199], v[92:95]\n\t"
  "v_mfma_f32_16x16x32_bf16 v[92:95], v[60:63], a[204:207], v[92:95]\n\t"
  "s_waitcnt lgkmcnt(4)\n\t"
  "v_mfma_f32_16x16x32_bf16 v[80:83], v[160:163], a[208:211], v[80:83]\n\t"
  "ds_read_b128 v[48:51], v219 offset:4096\n\t"
  "v_mfma_f32_16x16x32_bf16 v[80:83], v[164:167], a[216:219], v[80:83]\n\t"
  "ds_read_b128 v[52:55], v219 offset:5120\n\t"
  "v_mfma_f32_16x16x32_bf16 v[84:87], v[160:163], a[212:215], v[84:87]\n\t"
  "ds_read_b128 v[56:59], v219 offset:6144\n\t"
  "v_mfma_f32_16x16x32_bf16 v[84:87], v[164:167], a[220:223], v[84:87]\n\t"
  "ds_read_b128 v[60:63], v219 offset:7168\n\t"
  "v_mfma_f32_16x16x32_bf16 v[88:91], v[168:171], a[208:211], v[88:91]\n\t"
  "v_mfma_f32_16x16x32_bf16 v[88:91], v[172:175], a[216:219], v[88:91]\n\t"
  "v_mfma_f32_16x16x32_bf16 v[92:95], v[168:171], a[212:215], v[92:95]\n\t"
  "v_mfma_f32_16x16x32_bf16 v[92:95], v[172:175], a[220:223], v[92:95]\n\t"
  "s_waitcnt lgkmcnt(4)\n\t"
  "v_mfma_f32_16x16x32_bf16 v[80:83], v[32:35], a[224:227], v[80:83]\n\t"
  "ds_read_b128 v[160:163], v219 offset:8192\n\t"
  "v_mfma_f32_16x16x32_bf16 v[80:83], v[36:39], a[232:235], v[80:83]\n\t"
  "ds_read_b128 v[164:167], v219 offset:9216\n\t"
  "v_mfma_f32_16x16x32_bf16 v[84:87], v[32:35], a[228:231], v[84:87]\n\t"
  "ds_read_b128 v[168:171], v219 offset:10240\n\t"
  "v_mfma_f32_16x16x32_bf16 v[84:87], v[36:39], a[236:239], v[84:87]\n\t"
  "ds_read_b128 v[172:175], v219 offset:11264\n\t"
  "v_mfma_f32_16x16x32_bf16 v[88:91], v[40:43], a[224:227], v[88:91]\n\t"
  "v_mfma_f32_16x16x32_bf16 v[88:91], v[44:47], a[232:235], v[88:91]\n\t"
  "v_mfma_f32_16x16x32_bf16 v[92:95], v[40:43], a[228:231], v[92:95]\n\t"
  "v_mfma_f32_16x16x32_bf16 v[92:95], v[44:47], a[236:239], v[92:95]\n\t"
  "s_waitcnt lgkmcnt(4)\n\t"
  "v_mfma_f32_16x16x32_bf16 v[80:83], v[48:51], a[240:243], v[80:83]\n\t"
  "ds_read_b128 v[32:35], v219 offset:12288\n\t"
  "v_mfma_f32_16x16x32_bf16 v[80:83], v[52:55], a[248:251], v[80:83]\n\t"
  "ds_read_b128 v[36:39], v219 offset:13312\n\t"
  "v_mfma_f32_16x16x32_bf16 v[84:87], v[48:51], a[244:247], v[84:87]\n\t"
  "ds_read_b128 v[40:43], v219 offset:14336\n\t"
  "v_mfma_f32_16x16x32_bf16 v[84:87], v[52:55], a[252:255], v[84:87]\n\t"
  "ds_read_b128 v[44:47], v219 offset:15360\n\t"
  "v_mfma_f32_16x16x32_bf16 v[88:91], v[56:59], a[240:243], v[88:91]\n\t"
  "v_mfma_f32_16x16x32_bf16 v[88:91], v[60:63], a[248:251], v[88:91]\n\t"
  "v_mfma_f32_16x16x32_bf16 v[92:95], v[56:59], a[244:247], v[92:95]\n\t"
  "v_mfma_f32_16x16x32_bf16 v[92:95], v[60:63], a[252:255], v[92:95]\n\t"
  "s_waitcnt lgkmcnt(4)\n\t"
  "v_mfma_f32_16x16x32_bf16 v[80:83], v[160:163], v[0:3], v[80:83]\n\t"
  "ds_read_b128 v[48:51], v219 offset:16384\n\t"
  "v_mfma_f32_16x16x32_bf16 v[80:83], v[164:167], v[8:11], v[80:83]\n\t"
  "ds_read_b128 v[52:55], v219 offset:17408\n\t"
  "v_mfma_f32_16x16x32_bf16 v[84:87], v[160:163], v[4:7], v[84:87]\n\t"
  "ds_read_b128 v[56:59], v219 offset:18432\n\t"
  "v_mfma_f32_16x16x32_bf16 v[84:87], v[164:167], v[12:15], v[84:87]\n\t"
  "ds_read_b128 v[60:63], v219 offset:19456\n\t"
  "v_mfma_f32_16x16x32_bf16 v[88:91], v[168:171], v[0:3], v[88:91]\n\t"
  "v_mfma_f32_16x16x32_bf16 v[88:91], v[172:175], v[8:11], v[88:91]\n\t"
  "v_mfma_f32_16x16x32_bf16 v[92:95], v[168:171], v[4:7], v[92:95]\n\t"
  "v_mfma_f32_16x16x32_bf16 v[92:95], v[172:175], v[12:15], v[92:95]\n\t"
  "s_waitcnt vmcnt(14) lgkmcnt(4)\n\t"
  "s_barrier\n\t"
  "v_mfma_f32_16x16x32_bf16 v[80:83], v[32:35], v[16:19], v[80:83]\n\t"
  "ds_read_b128 v[160:163], v219 offset:20480\n\t"
  "v_mfma_f32_16x16x32_bf16 v[80:83], v[36:39], v[24:27], v[80:83]\n\t"
  "ds_read_b128 v[164:167], v219 offset:21504\n\t"
  "v_mfma_f32_16x16x32_bf16 v[84:87], v[32:35], v[20:23], v[84:87]\n\t"
  "ds_read_b128 v[168:171], v219 offset:22528\n\t"
  "v_mfma_f32_16x16x32_bf16 v[84:87], v[36:39], v[28:31], v[84:87]\n\t"
  "ds_read_b128 v[172:175], v219 offset:23552\n\t"
  "v_mfma_f32_16x16x32_bf16 v[88:91], v[40:43], v[16:19], v[88:91]\n\t"
  "s_add_u32 s44, s54, 307200\n\t"
  "s_addc_u32 s45, s55, 0\n\t"
  "v_mfma_f32_16x16x32_bf16 v[88:91], v[44:47], v[24:27], v[88:91]\n\t"
  "v_mfma_f32_16x16x32_bf16 v[92:95], v[40:43], v[20:23], v[92:95]\n\t"
  "s_add_u32 m0, s49, 28672\n\t"
  "v_mfma_f32_16x16x32_bf16 v[92:95], v[44:47], v[28:31], v[92:95]\n\t"
  "s_waitcnt lgkmcnt(4)\n\t"
  "v_mfma_f32_16x16x32_bf16 v[112:115], v[48:51], a[224:227], v[112:115]\n\t"
  "ds_read_b128 v[32:35], v219 offset:24576\n\t"
  "v_mfma_f32_16x16x32_bf16 v[112:115], v[52:55], a[232:235], v[112:115]\n\t"
  "ds_read_b128 v[36:39], v219 offset:25600\n\t"
  "v_mfma_f32_16x16x32_bf16 v[116:119], v[48:51], a[228:231], v[116:119]\n\t"
  "ds_read_b128 v[40:43], v219 offset:26624\n\t"
  "v_mfma_f32_16x16x32_bf16 v[116:119], v[52:55], a[236:239], v[116:119]\n\t"
  "ds_read_b128 v[44:47], v219 offset:27648\n\t"
  "v_mfma_f32_16x16x32_bf16 v[112:115], v[56:59], a[240:243], v[112:115]\n\t"
  "global_load_lds_dwordx4 v232, s[44:45] offset:0 sc1\n\t"
  "v_cvt_pk_bf16_f32 v192, v80, v81\n\t"
  "v_mfma_f32_16x16x32_bf16 v[112:115], v[60:63], a[248:251], v[112:115]\n\t"
  "v_cvt_pk_bf16_f32 v193, v82, v83\n\t"
  "v_cvt_pk_bf16_f32 v194, v88, v89\n\t"
  "v_mfma_f32_16x16x32_bf16 v[116:119], v[56:59], a[244:247], v[116:119]\n\t"
  "global_load_lds_dwordx4 v232, s[44:45] offset:1024 sc1\n\t"
  "v_cvt_pk_bf16_f32 v195, v90, v91\n\t"
  "v_mfma_f32_16x16x32_bf16 v[116:119], v[60:63], a[252:255], v[116:119]\n\t"
  "global_store_dwordx4 v246, v[192:195], s[56:57] offset:0 sc1\n\t"
  "s_waitcnt lgkmcnt(4)\n\t"
  "v_mfma_f32_16x16x32_bf16 v[96:99], v[160:163], a[192:195], v[96:99]\n\t"
  "ds_read_b128 v[48:51], v219 offset:28672\n\t"
  "v_cvt_pk_bf16_f32 v196, v84, v85\n\t"
  "v_mfma_f32_16x16x32_bf16 v[96:99], v[164:167], a[200:203], v[96:99]\n\t"
  "ds_read_b128 v[52:55], v219 offset:29696\n\t"
  "v_cvt_pk_bf16_f32 v197, v86, v87\n\t"
  "v_mfma_f32_16x16x32_bf16 v[100:103], v[160:163], a[196:199], v[100:103]\n\t"
  "ds_read_b128 v[56:59], v219 offset:30720\n\t"
  "v_cvt_pk_bf16_f32 v198, v92, v93\n\t"
  "v_mfma_f32_16x16x32_bf16 v[100:103], v[164:167], a[204:207], v[100:103]\n\t"
  "ds_read_b128 v[60:63], v219 offset:31744\n\t"
  "v_cvt_pk_bf16_f32 v199, v94, v95\n\t"
  "v_mfma_f32_16x16x32_bf16 v[104:107], v[168:171], a[192:195], v[104:107]\n\t"
  "global_load_lds_dwordx4 v232, s[44:45] offset:2048 sc1\n\t"
  "global_store_dwordx4 v246, v[196:199], s[56:57] offset:1024 sc1\n\t"
  "v_mfma_f32_16x16x32_bf16 v[104:107], v[172:175], a[200:203], v[104:107]\n\t"
  "v_mfma_f32_16x16x32_bf16 v[108:111], v[168:171], a[196:199], v[108:111]\n\t"
  "global_load_lds_dwordx4 v232, s[44:45] offset:3072 sc1\n\t"
  "v_mfma_f32_16x16x32_bf16 v[108:111], v[172:175], a[204:207], v[108:111]\n\t"
  "s_waitcnt lgkmcnt(4)\n\t"
  "v_mfma_f32_16x16x32_bf16 v[96:99], v[32:35], a[208:211], v[96:99]\n\t"
  "ds_read_b128 v[160:163], v219 offset:32768\n\t"
  "v_mfma_f32_16x16x32_bf16 v[96:99], v[36:39], a[216:219], v[96:99]\n\t"
  "ds_read_b128 v[164:167], v219 offset:33792\n\t"
  "v_mfma_f32_16x16x32_bf16 v[100:103], v[32:35], a[212:215], v[100:103]\n\t"
  "ds_read_b128 v[168:171], v219 offset:34816\n\t"
  "v_mfma_f32_16x16x32_bf16 v[100:103], v[36:39], a[220:223], v[100:103]\n\t"
  "ds_read_b128 v[172:175], v219 offset:35840\n\t"
  "v_mfma_f32_16x16x32_bf16 v[104:107], v[40:43], a[208:211], v[104:107]\n\t"
  "s_add_u32 m0, s49, 32768\n\t"
  "v_mfma_f32_16x16x32_bf16 v[104:107], v[44:47], a[216:219], v[104:107]\n\t"
  "v_mfma_f32_16x16x32_bf16 v[108:111], v[40:43], a[212:215], v[108:111]\n\t"
  "global_load_lds_dwordx4 v233, s[44:45] offset:0 sc1\n\t"
  "v_mfma_f32_16x16x32_bf16 v[108:111], v[44:47], a[220:223], v[108:111]\n\t"
  "s_waitcnt lgkmcnt(4)\n\t"
  "v_mfma_f32_16x16x32_bf16 v[96:99], v[48:51], a[224:227], v[96:99]\n\t"
  "ds_read_b128 v[32:35], v219 offset:36864\n\t"
  "v_mfma_f32_16x16x32_bf16 v[96:99], v[52:55], a[232:235], v[96:99]\n\t"
  "ds_read_b128 v[36:39], v219 offset:37888\n\t"
  "v_mfma_f32_16x16x32_bf16 v[100:103], v[48:51], a[228:231], v[100:103]\n\t"
  "ds_read_b128 v[40:43], v219 offset:38912\n\t"
  "v_mfma_f32_16x16x32_bf16 v[100:103], v[52:55], a[236:239], v[100:103]\n\t"
  "ds_read_b128 v[44:47], v219 offset:39936\n\t"
  "v_mfma_f32_16x16x32_bf16 v[104:107], v[56:59], a[224:227], v[104:107]\n\t"
  "global_load_lds_dwordx4 v233, s[44:45] offset:1024 sc1\n\t"
  "v_mfma_f32_16x16x32_bf16 v[104:107], v[60:63], a[232:235], v[104:107]\n\t"
  "v_mfma_f32_16x16x32_bf16 v[108:111], v[56:59], a[228:231], v[108:111]\n\t"
  "v_mfma_f32_16x16x32_bf16 v[108:111], v[60:63], a[236:239], v[108:111]\n\t"
  "s_waitcnt lgkmcnt(4)\n\t"
  "v_mfma_f32_16x16x32_bf16 v[96:99], v[160:163], a[240:243], v[96:99]\n\t"
  "ds_read_b128 v[48:51], v219 offset:40960\n\t"
  "v_mfma_f32_16x16x32_bf16 v[96:99], v[164:167], a[248:251], v[96:99]\n\t"
  "ds_read_b128 v[52:55], v219 offset:41984\n\t"
  "v_mfma_f32_16x16x32_bf16 v[100:103], v[160:163], a[244:247], v[100:103]\n\t"
  "ds_read_b128 v[56:59], v219 offset:43008\n\t"
  "v_mfma_f32_16x16x32_bf16 v[100:103], v[164:167], a[252:255], v[100:103]\n\t"
  "ds_read_b128 v[60:63], v219 offset:44032\n\t"
  "v_mfma_f32_16x16x32_bf16 v[104:107], v[168:171], a[240:243], v[104:107]\n\t"
  "v_mfma_f32_16x16x32_bf16 v[104:107], v[172:175], a[248:251], v[104:107]\n\t"
  "v_mfma_f32_16x16x32_bf16 v[108:111], v[168:171], a[244:247], v[108:111]\n\t"
  "v_mfma_f32_16x16x32_bf16 v[108:111], v[172:175], a[252:255], v[108:111]\n\t"
  "s_waitcnt lgkmcnt(4)\n\t"
  "v_mfma_f32_16x16x32_bf16 v[96:99], v[32:35], v[0:3], v[96:99]\n\t"
  "ds_read_b128 v[160:163], v219 offset:45056\n\t"
  "v_mfma_f32_16x16x32_bf16 v[96:99], v[36:39], v[8:11], v[96:99]\n\t"
  "ds_read_b128 v[164:167], v219 offset:46080\n\t"
  "v_mfma_f32_16x16x32_bf16 v[100:103], v[32:35], v[4:7], v[100:103]\n\t"
  "ds_read_b128 v[168:171], v219 offset:47104\n\t"
  "v_mfma_f32_16x16x32_bf16 v[100:103], v[36:39], v[12:15], v[100:103]\n\t"
  "ds_read_b128 v[172:175], v219 offset:48128\n\t"
  "v_mfma_f32_16x16x32_bf16 v[104:107], v[40:43], v[0:3], v[104:107]\n\t"
  "v_mfma_f32_16x16x32_bf16 v[104:107], v[44:47], v[8:11], v[104:107]\n\t"
  "v_mfma_f32_16x16x32_bf16 v[108:111], v[40:43], v[4:7], v[108:111]\n\t"
  "v_mfma_f32_16x16x32_bf16 v[108:111], v[44:47], v[12:15], v[108:111]\n\t"
  "s_waitcnt vmcnt(16) lgkmcnt(4)\n\t"
  "s_barrier\n\t"
  "v_mfma_f32_16x16x32_bf16 v[96:99], v[48:51], v[16:19], v[96:99]\n\t"
  "ds_read_b128 v[32:35], v219 offset:49152\n\t"
  "v_mfma_f32_16x16x32_bf16 v[96:99], v[52:55], v[24:27], v[96:99]\n\t"
  "ds_read_b128 v[36:39], v219 offset:51200\n\t"
  "v_mfma_f32_16x16x32_bf16 v[100:103], v[48:51], v[20:23], v[100:103]\n\t"
  "ds_read_b128 v[40:43], v219 offset:50176\n\t"
  "v_mfma_f32_16x16x32_bf16 v[100:103], v[52:55], v[28:31], v[100:103]\n\t"
  "ds_read_b128 v[44:47], v219 offset:52224\n\t"
  "v_mfma_f32_16x16x32_bf16 v[104:107], v[56:59], v[16:19], v[104:107]\n\t"
  "v_mfma_f32_16x16x32_bf16 v[104:107], v[60:63], v[24:27], v[104:107]\n\t"
  "v_mfma_f32_16x16x32_bf16 v[108:111], v[56:59], v[20:23], v[108:111]\n\t"
  "v_mfma_f32_16x16x32_bf16 v[108:111], v[60:63], v[28:31], v[108:111]\n\t"
  "s_waitcnt lgkmcnt(4)\n\t"
  "v_mfma_f32_16x16x32_bf16 v[112:115], v[160:163], v[0:3], v[112:115]\n\t"
  "ds_read_b128 v[48:51], v219 offset:53248\n\t"
  "v_mfma_f32_16x16x32_bf16 v[112:115], v[164:167], v[8:11], v[112:115]\n\t"
  "ds_read_b128 v[52:55], v219 offset:55296\n\t"
  "v_mfma_f32_16x16x32_bf16 v[116:119], v[160:163], v[4:7], v[116:119]\n\t"
  "ds_read_b128 v[56:59], v219 offset:54272\n\t"
  "v_mfma_f32_16x16x32_bf16 v[116:119], v[164:167], v[12:15], v[116:119]\n\t"
  "ds_read_b128 v[60:63], v219 offset:56320\n\t"
  "v_mfma_f32_16x16x32_bf16 v[112:115], v[168:171], v[16:19], v[112:115]\n\t"
  "v_cvt_pk_bf16_f32 v184, v96, v97\n\t"
  "v_cvt_pk_bf16_f32 v185, v98, v99\n\t"
  "v_mfma_f32_16x16x32_bf16 v[112:115], v[172:175], v[24:27], v[112:115]\n\t"
  "v_cvt_pk_bf16_f32 v186, v104, v105\n\t"
  "v_cvt_pk_bf16_f32 v187, v106, v107\n\t"
  "v_mfma_f32_16x16x32_bf16 v[116:119], v[168:171], v[20:23], v[116:119]\n\t"
  "global_store_dwordx4 v247, v[184:187], s[56:57] offset:0 sc1\n\t"
  "v_cvt_pk_bf16_f32 v188, v100, v101\n\t"
  "v_mfma_f32_16x16x32_bf16 v[116:119], v[172:175], v[28:31], v[116:119]\n\t"
  "v_cvt_pk_bf16_f32 v189, v102, v103\n\t"
  "s_waitcnt lgkmcnt(4)\n\t"
  "v_mfma_f32_16x16x32_bf16 v[120:123], v[32:35], a[0:3], 0\n\t"
  "ds_read_b128 v[160:163], v219 offset:57344\n\t"
  "v_cvt_pk_bf16_f32 v190, v108, v109\n\t"
  "v_mfma_f32_16x16x32_bf16 v[120:123], v[36:39], a[8:11], v[120:123]\n\t"
  "ds_read_b128 v[164:167], v219 offset:59392\n\t"
  "v_cvt_pk_bf16_f32 v191, v110, v111\n\t"
  "v_mfma_f32_16x16x32_bf16 v[124:127], v[32:35], a[4:7], 0\n\t"
  "ds_read_b128 v[168:171], v219 offset:58368\n\t"
  "global_store_dwordx4 v247, v[188:191], s[56:57] offset:1024 sc1\n\t"
  "v_mfma_f32_16x16x32_bf16 v[124:127], v[36:39], a[12:15], v[124:127]\n\t"
  "ds_read_b128 v[172:175], v219 offset:60416\n\t"
  "v_mfma_f32_16x16x32_bf16 v[176:179], v[40:43], a[0:3], 0\n\t"
  "v_cvt_pk_bf16_f32 v192, v112, v113\n\t"
  "v_cvt_pk_bf16_f32 v193, v114, v115\n\t"
  "v_mfma_f32_16x16x32_bf16 v[176:179], v[44:47], a[8:11], v[176:179]\n\t"
  "v_cvt_pk_bf16_f32 v194, v116, v117\n\t"
  "v_cvt_pk_bf16_f32 v195, v118, v119\n\t"
  "v_mfma_f32_16x16x32_bf16 v[180:183], v[40:43], a[4:7], 0\n\t"
  "global_store_dwordx4 v248, v[192:195], s[56:57] offset:0 sc1\n\t"
  "v_mfma_f32_16x16x32_bf16 v[180:183], v[44:47], a[12:15], v[180:183]\n\t"
  "s_waitcnt lgkmcnt(4)\n\t"
  "v_mfma_f32_16x16x32_bf16 v[120:123], v[48:51], a[16:19], v[120:123]\n\t"
  "ds_read_b128 v[32:35], v219 offset:61440\n\t"
  "v_mfma_f32_16x16x32_bf16 v[120:123], v[52:55], a[24:27], v[120:123]\n\t"
  "ds_read_b128 v[36:39], v219 offset:63488\n\t"
  "v_mfma_f32_16x16x32_bf16 v[124:127], v[48:51], a[20:23], v[124:127]\n\t"
  "ds_read_b128 v[40:43], v219 offset:62464\n\t"
  "v_mfma_f32_16x16x32_bf16 v[124:127], v[52:55], a[28:31], v[124:127]\n\t"
  "ds_read_b128 v[44:47], v219 offset:64512\n\t"
  "v_mfma_f32_16x16x32_bf16 v[176:179], v[56:59], a[16:19], v[176:179]\n\t"
  "v_mfma_f32_16x16x32_bf16 v[176:179], v[60:63], a[24:27], v[176:179]\n\t"
  "v_mfma_f32_16x16x32_bf16 v[180:183], v[56:59], a[20:23], v[180:183]\n\t"
  "v_mfma_f32_16x16x32_bf16 v[180:183], v[60:63], a[28:31], v[180:183]\n\t"
  "s_waitcnt lgkmcnt(4)\n\t"
  "v_mfma_f32_16x16x32_bf16 v[120:123], v[160:163], a[32:35], v[120:123]\n\t"
  "ds_read_b128 v[48:51], v220 offset:0\n\t"
  "v_mfma_f32_16x16x32_bf16 v[120:123], v[164:167], a[40:43], v[120:123]\n\t"
  "PG7_%=:\n\t"
  "s_cbranch_execz PGBACK_%=\n\t"
  "ds_read_b128 v[52:55], v220 offset:2048\n\t"
  "v_mfma_f32_16x16x32_bf16 v[124:127], v[160:163], a[36:39], v[124:127]\n\t"
  "ds_read_b128 v[56:59], v220 offset:1024\n\t"
  "v_mfma_f32_16x16x32_bf16 v[124:127], v[164:167], a[44:47], v[124:127]\n\t"
  "ds_read_b128 v[60:63], v220 offset:3072\n\t"
  "v_mfma_f32_16x16x32_bf16 v[176:179], v[168:171], a[32:35], v[176:179]\n\t"
  "v_mfma_f32_16x16x32_bf16 v[176:179], v[172:175], a[40:43], v[176:179]\n\t"
  "v_mfma_f32_16x16x32_bf16 v[180:183], v[168:171], a[36:39], v[180:183]\n\t"
  "v_mfma_f32_16x16x32_bf16 v[180:183], v[172:175], a[44:47], v[180:183]\n\t"
  "s_waitcnt lgkmcnt(4)\n\t"
  "v_mfma_f32_16x16x32_bf16 v[120:123], v[32:35], a[48:51], v[120:123]\n\t"
  "ds_read_b128 v[160:163], v220 offset:4096\n\t"
  "v_mfma_f32_16x16x32_bf16 v[120:123], v[36:39], a[56:59], v[120:123]\n\t"
  "ds_read_b128 v[164:167], v220 offset:6144\n\t"
  "v_mfma_f32_16x16x32_bf16 v[124:127], v[32:35], a[52:55], v[124:127]\n\t"
  "ds_read_b128 v[168:171], v220 offset:5120\n\t"
  "v_mfma_f32_16x16x32_bf16 v[124:127], v[36:39], a[60:63], v[124:127]\n\t"
  "ds_read_b128 v[172:175], v220 offset:7168\n\t"
  "v_mfma_f32_16x16x32_bf16 v[176:179], v[40:43], a[48:51], v[176:179]\n\t"
  "v_mfma_f32_16x16x32_bf16 v[176:179], v[44:47], a[56:59], v[176:179]\n\t"
  "v_mfma_f32_16x16x32_bf16 v[180:183], v[40:43], a[52:55], v[180:183]\n\t"
  "v_lshrrev_b32 v254, 8, v216\n\t"
  "v_lshrrev_b32 v255, 6, v216\n\t"
  "v_mfma_f32_16x16x32_bf16 v[180:183], v[44:47], a[60:63], v[180:183]\n\t"
  "s_waitcnt vmcnt(13) lgkmcnt(4)\n\t"
  "s_barrier\n\t"
  "v_mfma_f32_16x16x32_bf16 v[120:123], v[48:51], a[64:67], v[120:123]\n\t"
  "ds_read_b128 v[32:35], v216 offset:0\n\t"
  "v_xor_b32 v254, v254, v255\n\t"
  "v_mfma_f32_16x16x32_bf16 v[120:123], v[52:55], a[72:75], v[120:123]\n\t"
  "ds_read_b128 v[36:39], v216 offset:2048\n\t"
  "v_and_b32 v254, 3, v254\n\t"
  "v_mfma_f32_16x16x32_bf16 v[124:127], v[48:51], a[68:71], v[124:127]\n\t"
  "ds_read_b128 v[40:43], v216 offset:1024\n\t"
  "v_bfe_u32 v255, v216, 5, 1\n\t"
  "v_mfma_f32_16x16x32_bf16 v[124:127], v[52:55], a[76:79], v[124:127]\n\t"
  "ds_read_b128 v[44:47], v216 offset:3072\n\t"
  "v_lshl_or_b32 v254, v255, 2, v254\n\t"
  "v_mfma_f32_16x16x32_bf16 v[176:179], v[56:59], a[64:67], v[176:179]\n\t"
  "global_load_dwordx4 v[152:155], v251, s[56:57] offset:0 sc1\n\t"
  "v_and_b32 v255, 16, v216\n\t"
  "v_mfma_f32_16x16x32_bf16 v[176:179], v[60:63], a[72:75], v[176:179]\n\t"
  "v_mov_b32 v252, 0x3f80\n\t"
  "v_lshlrev_b32 v252, v255, v252\n\t"
  "v_mfma_f32_16x16x32_bf16 v[180:183], v[56:59], a[68:71], v[180:183]\n\t"
  "global_load_dwordx4 v[156:159], v251, s[56:57] offset:1024 sc1\n\t"
  "v_mov_b32 v64, 0\n\t"
  "v_mfma_f32_16x16x32_bf16 v[180:183], v[60:63], a[76:79], v[180:183]\n\t"
  "v_mov_b32 v65, 0\n\t"
  "s_waitcnt lgkmcnt(4)\n\t"
  "v_mfma_f32_16x16x32_bf16 v[120:123], v[160:163], a[80:83], v[120:123]\n\t"
  "ds_read_b128 v[48:51], v216 offset:4096\n\t"
  "v_mov_b32 v68, 0\n\t"
  "v_mfma_f32_16x16x32_bf16 v[120:123], v[164:167], a[88:91], v[120:123]\n\t"
  "ds_read_b128 v[52:55], v216 offset:6144\n\t"
  "v_mov_b32 v69, 0\n\t"
  "v_mfma_f32_16x16x32_bf16 v[124:127], v[160:163], a[84:87], v[124:127]\n\t"
  "ds_read_b128 v[56:59], v216 offset:5120\n\t"
  "v_cmp_eq_u32 vcc, 0, v254\n\t"
  "s_nop 1\n\t"
  "v_cndmask_b32 v66, v64, v252, vcc\n\t"
  "v_mfma_f32_16x16x32_bf16 v[124:127], v[164:167], a[92:95], v[124:127]\n\t"
  "ds_read_b128 v[60:63], v216 offset:7168\n\t"
  "v_cmp_eq_u32 vcc, 4, v254\n\t"
  "s_nop 1\n\t"
  "v_cndmask_b32 v67, v64, v252, vcc\n\t"
  "v_mfma_f32_16x16x32_bf16 v[176:179], v[168:171], a[80:83], v[176:179]\n\t"
  "v_mul_f32 v241, v241, v241\n\t"
  "v_mfma_f32_16x16x32_bf16 v[176:179], v[172:175], a[88:91], v[176:179]\n\t"
  "v_mul_f32 v242, v242, v242\n\t"
  "v_lshlrev_b32 v72, 16, v204\n\t"
  "v_mfma_f32_16x16x32_bf16 v[180:183], v[168:171], a[84:87], v[180:183]\n\t"
  "v_and_b32 v73, 0xffff0000, v204\n\t"
  "v_lshlrev_b32 v74, 16, v205\n\t"
  "v_mfma_f32_16x16x32_bf16 v[180:183], v[172:175], a[92:95], v[180:183]\n\t"
  "v_and_b32 v75, 0xffff0000, v205\n\t"
  "s_waitcnt lgkmcnt(4)\n\t"
  "v_mfma_f32_16x16x32_bf16 v[120:123], v[32:35], a[96:99], v[120:123]\n\t"
  "ds_read_b128 v[160:163], v216 offset:8192\n\t"
  "v_lshlrev_b32 v76, 16, v206\n\t"
  "v_mfma_f32_16x16x32_bf16 v[120:123], v[36:39], a[104:107], v[120:123]\n\t"
  "ds_read_b128 v[164:167], v216 offset:10240\n\t"
  "v_and_b32 v77, 0xffff0000, v206\n\t"
  "v_mfma_f32_16x16x32_bf16 v[124:127], v[32:35], a[100:103], v[124:127]\n\t"
  "ds_read_b128 v[168:171], v216 offset:9216\n\t"
  "v_lshlrev_b32 v78, 16, v207\n\t"
  "v_mfma_f32_16x16x32_bf16 v[124:127], v[36:39], a[108:111], v[124:127]\n\t"
  "ds_read_b128 v[172:175], v216 offset:11264\n\t"
  "v_and_b32 v79, 0xffff0000, v207\n\t"
  "v_mfma_f32_16x16x32_bf16 v[176:179], v[40:43], a[96:99], v[176:179]\n\t"
  "global_load_dwordx4 v[140:143], v250, s[56:57] offset:0 sc1\n\t"
  "v_lshlrev_b32 v80, 16, v208\n\t"
  "v_mfma_f32_16x16x32_bf16 v[176:179], v[44:47], a[104:107], v[176:179]\n\t"
  "v_and_b32 v81, 0xffff0000, v208\n\t"
  "v_lshlrev_b32 v82, 16, v209\n\t"
  "v_mfma_f32_16x16x32_bf16 v[180:183], v[40:43], a[100:103], v[180:183]\n\t"
  "global_load_dwordx4 v[144:147], v250, s[56:57] offset:1024 sc1\n\t"
  "v_and_b32 v83, 0xffff0000, v209\n\t"
  "v_mfma_f32_16x16x32_bf16 v[180:183], v[44:47], a[108:111], v[180:183]\n\t"
  "v_lshlrev_b32 v84, 16, v210\n\t"
  "s_waitcnt lgkmcnt(4)\n\t"
  "v_mfma_f32_16x16x32_bf16 v[120:123], v[48:51], a[112:115], v[120:123]\n\t"
  "ds_read_b128 v[32:35], v216 offset:12288\n\t"
  "v_and_b32 v85, 0xffff0000, v210\n\t"
  "v_mfma_f32_16x16x32_bf16 v[120:123], v[52:55], a[120:123], v[120:123]\n\t"
  "ds_read_b128 v[36:39], v216 offset:14336\n\t"
  "v_lshlrev_b32 v86, 16, v211\n\t"
  "v_mfma_f32_16x16x32_bf16 v[124:127], v[48:51], a[116:119], v[124:127]\n\t"
  "ds_read_b128 v[40:43], v216 offset:13312\n\t"
  "v_and_b32 v87, 0xffff0000, v211\n\t"
  "v_mfma_f32_16x16x32_bf16 v[124:127], v[52:55], a[124:127], v[124:127]\n\t"
  "ds_read_b128 v[44:47], v216 offset:15360\n\t"
  "v_lshlrev_b32 v88, 16, v212\n\t"
  "v_mfma_f32_16x16x32_bf16 v[176:179], v[56:59], a[112:115], v[176:179]\n\t"
  "v_and_b32 v89, 0xffff0000, v212\n\t"
  "v_lshlrev_b32 v90, 16, v213\n\t"
  "v_mfma_f32_16x16x32_bf16 v[176:179], v[60:63], a[120:123], v[176:179]\n\t"
  "v_and_b32 v91, 0xffff0000, v213\n\t"
  "v_lshlrev_b32 v92, 16, v214\n\t"
  "v_mfma_f32_16x16x32_bf16 v[180:183], v[56:59], a[116:119], v[180:183]\n\t"
  "v_and_b32 v93, 0xffff0000, v214\n\t"
  "v_lshlrev_b32 v94, 16, v215\n\t"
  "v_mfma_f32_16x16x32_bf16 v[180:183], v[60:63], a[124:127], v[180:183]\n\t"
  "v_and_b32 v95, 0xffff0000, v215\n\t"
  "s_waitcnt lgkmcnt(4)\n\t"
  "v_mfma_f32_16x16x32_bf16 v[120:123], v[160:163], a[128:131], v[120:123]\n\t"
  "ds_read_b128 v[48:51], v216 offset:16384\n\t"
  "v_add_f32 v72, v72, v224\n\t"
  "v_mfma_f32_16x16x32_bf16 v[120:123], v[164:167], a[136:139], v[120:123]\n\t"
  "ds_read_b128 v[52:55], v216 offset:18432\n\t"
  "v_add_f32 v73, v73, v224\n\t"
  "v_mfma_f32_16x16x32_bf16 v[124:127], v[160:163], a[132:135], v[124:127]\n\t"
  "ds_read_b128 v[56:59], v216 offset:17408\n\t"
  "v_add_f32 v74, v74, v224\n\t"
  "v_mfma_f32_16x16x32_bf16 v[124:127], v[164:167], a[140:143], v[124:127]\n\t"
  "ds_read_b128 v[60:63], v216 offset:19456\n\t"
  "v_add_f32 v75, v75, v224\n\t"
  "v_mfma_f32_16x16x32_bf16 v[176:179], v[168:171], a[128:131], v[176:179]\n\t"
  "v_add_f32 v76, v76, v224\n\t"
  "v_add_f32 v77, v77, v224\n\t"
  "v_mfma_f32_16x16x32_bf16 v[176:179], v[172:175], a[136:139], v[176:179]\n\t"
  "v_add_f32 v78, v78, v224\n\t"
  "v_add_f32 v79, v79, v224\n\t"
  "v_mfma_f32_16x16x32_bf16 v[180:183], v[168:171], a[132:135], v[180:183]\n\t"
  "v_add_f32 v80, v80, v226\n\t"
  "v_add_f32 v81, v81, v226\n\t"
  "v_mfma_f32_16x16x32_bf16 v[180:183], v[172:175], a[140:143], v[180:183]\n\t"
  "v_add_f32 v82, v82, v226\n\t"
  "s_waitcnt lgkmcnt(4)\n\t"
  "v_mfma_f32_16x16x32_bf16 v[120:123], v[32:35], a[144:147], v[120:123]\n\t"
  "ds_read_b128 v[160:163], v216 offset:20480\n\t"
  "v_add_f32 v83, v83, v226\n\t"
  "v_mfma_f32_16x16x32_bf16 v[120:123], v[36:39], a[152:155], v[120:123]\n\t"
  "ds_read_b128 v[164:167], v216 offset:22528\n\t"
  "v_add_f32 v84, v84, v226\n\t"
  "v_mfma_f32_16x16x32_bf16 v[124:127], v[32:35], a[148:151], v[124:127]\n\t"
  "ds_read_b128 v[168:171], v216 offset:21504\n\t"
  "v_add_f32 v85, v85, v226\n\t"
  "v_mfma_f32_16x16x32_bf16 v[124:127], v[36:39], a[156:159], v[124:127]\n\t"
  "ds_read_b128 v[172:175], v216 offset:23552\n\t"
  "v_add_f32 v86, v86, v226\n\t"
  "v_mfma_f32_16x16x32_bf16 v[176:179], v[40:43], a[144:147], v[176:179]\n\t"
  "v_add_f32 v87, v87, v226\n\t"
  "v_add_f32 v88, v88, v224\n\t"
  "v_mfma_f32_16x16x32_bf16 v[176:179], v[44:47], a[152:155], v[176:179]\n\t"
  "v_add_f32 v89, v89, v224\n\t"
  "v_add_f32 v90, v90, v224\n\t"
  "v_mfma_f32_16x16x32_bf16 v[180:183], v[40:43], a[148:151], v[180:183]\n\t"
  "v_add_f32 v91, v91, v224\n\t"
  "v_add_f32 v92, v92, v226\n\t"
  "v_mfma_f32_16x16x32_bf16 v[180:183], v[44:47], a[156:159], v[180:183]\n\t"
  "s_waitcnt vmcnt(7) lgkmcnt(4)\n\t"
  "s_barrier\n\t"
  "v_mfma_f32_16x16x32_bf16 v[120:123], v[48:51], a[160:163], v[120:123]\n\t"
  "ds_read_b128 v[32:35], v216 offset:28672\n\t"
  "v_add_f32 v93, v93, v226\n\t"
  "v_mfma_f32_16x16x32_bf16 v[120:123], v[52:55], a[168:171], v[120:123]\n\t"
  "ds_read_b128 v[36:39], v216 offset:30720\n\t"
  "v_add_f32 v94, v94, v226\n\t"
  "v_mfma_f32_16x16x32_bf16 v[124:127], v[48:51], a[164:167], v[124:127]\n\t"
  "ds_read_b128 v[40:43], v216 offset:29696\n\t"
  "v_add_f32 v95, v95, v226\n\t"
  "v_mfma_f32_16x16x32_bf16 v[124:127], v[52:55], a[172:175], v[124:127]\n\t"
  "ds_read_b128 v[44:47], v216 offset:31744\n\t"
  "v_mfma_f32_16x16x32_bf16 v[176:179], v[56:59], a[160:163], v[176:179]\n\t"
  "global_load_dwordx4 v[128:131], v249, s[56:57] offset:0 sc1\n\t"
  "v_mfma_f32_16x16x32_bf16 v[176:179], v[60:63], a[168:171], v[176:179]\n\t"
  "global_load_dwordx4 v[132:135], v249, s[56:57] offset:1024 sc1\n\t"
  "v_mfma_f32_16x16x32_bf16 v[180:183], v[56:59], a[164:167], v[180:183]\n\t"
  "v_mfma_f32_16x16x32_bf16 v[180:183], v[60:63], a[172:175], v[180:183]\n\t"
  "global_load_dwordx4 v[200:203], v251, s[56:57] offset:2048 sc1\n\t"
  "s_waitcnt lgkmcnt(4)\n\t"
  "v_mfma_f32_16x16x32_bf16 v[120:123], v[160:163], a[176:179], v[120:123]\n\t"
  "ds_read_b128 v[48:51], v216 offset:32768\n\t"
  "v_mfma_f32_16x16x32_bf16 v[120:123], v[164:167], a[184:187], v[120:123]\n\t"
  "ds_read_b128 v[52:55], v216 offset:34816\n\t"
  "v_mfma_f32_16x16x32_bf16 v[124:127], v[160:163], a[180:183], v[124:127]\n\t"
  "ds_read_b128 v[56:59], v216 offset:33792\n\t"
  "v_mfma_f32_16x16x32_bf16 v[124:127], v[164:167], a[188:191], v[124:127]\n\t"
  "ds_read_b128 v[60:63], v216 offset:35840\n\t"
  "v_mfma_f32_16x16x32_bf16 v[176:179], v[168:171], a[176:179], v[176:179]\n\t"
  "global_load_dwordx4 v[148:151], v250, s[56:57] offset:2048 sc1\n\t"
  "v_mfma_f32_16x16x32_bf16 v[176:179], v[172:175], a[184:187], v[176:179]\n\t"
  "v_mfma_f32_16x16x32_bf16 v[180:183], v[168:171], a[180:183], v[180:183]\n\t"
  "global_load_dwordx4 v[136:139], v249, s[56:57] offset:2048 sc1\n\t"
  "v_mfma_f32_16x16x32_bf16 v[180:183], v[172:175], a[188:191], v[180:183]\n\t"
  "s_waitcnt lgkmcnt(4)\n\t"
  "v_mfma_f32_16x16x32_bf16 v[120:123], v[32:35], a[192:195], v[120:123]\n\t"
  "ds_read_b128 v[160:163], v216 offset:36864\n\t"
  "v_mfma_f32_16x16x32_bf16 v[120:123], v[36:39], a[200:203], v[120:123]\n\t"
  "ds_read_b128 v[164:167], v216 offset:38912\n\t"
  "v_mfma_f32_16x16x32_bf16 v[124:127], v[32:35], a[196:199], v[124:127]\n\t"
  "ds_read_b128 v[168:171], v216 offset:37888\n\t"
  "v_mfma_f32_16x16x32_bf16 v[124:127], v[36:39], a[204:207], v[124:127]\n\t"
  "ds_read_b128 v[172:175], v216 offset:39936\n\t"
  "v_mfma_f32_16x16x32_bf16 v[176:179], v[40:43], a[192:195], v[176:179]\n\t"
  "v_mfma_f32_16x16x32_bf16 v[176:179], v[44:47], a[200:203], v[176:179]\n\t"
  "v_mfma_f32_16x16x32_bf16 v[180:183], v[40:43], a[196:199], v[180:183]\n\t"
  "v_mfma_f32_16x16x32_bf16 v[180:183], v[44:47], a[204:207], v[180:183]\n\t"
  "s_waitcnt lgkmcnt(4)\n\t"
  "v_mfma_f32_16x16x32_bf16 v[120:123], v[48:51], a[208:211], v[120:123]\n\t"
  "ds_read_b128 v[32:35], v216 offset:40960\n\t"
  "v_mfma_f32_16x16x32_bf16 v[120:123], v[52:55], a[216:219], v[120:123]\n\t"
  "ds_read_b128 v[36:39], v216 offset:43008\n\t"
  "v_mfma_f32_16x16x32_bf16 v[124:127], v[48:51], a[212:215], v[124:127]\n\t"
  "ds_read_b128 v[40:43], v216 offset:41984\n\t"
  "v_mfma_f32_16x16x32_bf16 v[124:127], v[52:55], a[220:223], v[124:127]\n\t"
  "ds_read_b128 v[44:47], v216 offset:44032\n\t"
  "v_mfma_f32_16x16x32_bf16 v[176:179], v[56:59], a[208:211], v[176:179]\n\t"
  "v_mfma_f32_16x16x32_bf16 v[176:179], v[60:63], a[216:219], v[176:179]\n\t"
  "v_mfma_f32_16x16x32_bf16 v[180:183], v[56:59], a[212:215], v[180:183]\n\t"
  "v_mfma_f32_16x16x32_bf16 v[180:183], v[60:63], a[220:223], v[180:183]\n\t"
  "s_waitcnt lgkmcnt(4)\n\t"
  "v_mfma_f32_16x16x32_bf16 v[120:123], v[160:163], a[224:227], v[120:123]\n\t"
  "ds_read_b128 v[48:51], v216 offset:45056\n\t"
  "v_mfma_f32_16x16x32_bf16 v[120:123], v[164:167], a[232:235], v[120:123]\n\t"
  "ds_read_b128 v[52:55], v216 offset:47104\n\t"
  "v_mfma_f32_16x16x32_bf16 v[124:127], v[160:163], a[228:231], v[124:127]\n\t"
  "ds_read_b128 v[56:59], v216 offset:46080\n\t"
  "v_mfma_f32_16x16x32_bf16 v[124:127], v[164:167], a[236:239], v[124:127]\n\t"
  "ds_read_b128 v[60:63], v216 offset:48128\n\t"
  "v_mfma_f32_16x16x32_bf16 v[176:179], v[168:171], a[224:227], v[176:179]\n\t"
  "v_mfma_f32_16x16x32_bf16 v[176:179], v[172:175], a[232:235], v[176:179]\n\t"
  "v_mfma_f32_16x16x32_bf16 v[180:183], v[168:171], a[228:231], v[180:183]\n\t"
  "v_mfma_f32_16x16x32_bf16 v[180:183], v[172:175], a[236:239], v[180:183]\n\t"
  "s_waitcnt lgkmcnt(4)\n\t"
  "v_mfma_f32_16x16x32_bf16 v[120:123], v[32:35], a[240:243], v[120:123]\n\t"
  "ds_read_b128 v[160:163], v216 offset:49152\n\t"
  "v_mfma_f32_16x16x32_bf16 v[120:123], v[36:39], a[248:251], v[120:123]\n\t"
  "ds_read_b128 v[164:167], v216 offset:51200\n\t"
  "v_mfma_f32_16x16x32_bf16 v[124:127], v[32:35], a[244:247], v[124:127]\n\t"
  "ds_read_b128 v[168:171], v216 offset:50176\n\t"
  "v_mfma_f32_16x16x32_bf16 v[124:127], v[36:39], a[252:255], v[124:127]\n\t"
  "ds_read_b128 v[172:175], v216 offset:52224\n\t"
  "v_mfma_f32_16x16x32_bf16 v[176:179], v[40:43], a[240:243], v[176:179]\n\t"
  "v_mfma_f32_16x16x32_bf16 v[176:179], v[44:47], a[248:251], v[176:179]\n\t"
  "v_mfma_f32_16x16x32_bf16 v[180:183], v[40:43], a[244:247], v[180:183]\n\t"
  "v_mfma_f32_16x16x32_bf16 v[180:183], v[44:47], a[252:255], v[180:183]\n\t"
  "s_waitcnt lgkmcnt(4)\n\t"
  "v_mfma_f32_16x16x32_bf16 v[120:123], v[48:51], v[0:3], v[120:123]\n\t"
  "s_waitcnt vmcnt(0)\n\t"
  "v_max_u32 v252, v128, v129\n\t"
  "v_mfma_f32_16x16x32_bf16 v[120:123], v[52:55], v[8:11], v[120:123]\n\t"
  "v_max3_u32 v252, v252, v130, v131\n\t"
  "v_max3_u32 v252, v252, v132, v133\n\t"
  "v_mfma_f32_16x16x32_bf16 v[124:127], v[48:51], v[4:7], v[124:127]\n\t"
  "v_max3_u32 v252, v252, v134, v135\n\t"
  "v_max3_u32 v252, v252, v140, v141\n\t"
  "v_mfma_f32_16x16x32_bf16 v[124:127], v[52:55], v[12:15], v[124:127]\n\t"
  "v_max3_u32 v252, v252, v142, v143\n\t"
  "v_max3_u32 v252, v252, v144, v145\n\t"
  "v_mfma_f32_16x16x32_bf16 v[176:179], v[56:59], v[0:3], v[176:179]\n\t"
  "v_max3_u32 v252, v252, v146, v147\n\t"
  "v_max3_u32 v252, v252, v152, v153\n\t"
  "v_mfma_f32_16x16x32_bf16 v[176:179], v[60:63], v[8:11], v[176:179]\n\t"
  "v_max3_u32 v252, v252, v154, v155\n\t"
  "v_max3_u32 v252, v252, v156, v157\n\t"
  "v_mfma_f32_16x16x32_bf16 v[180:183], v[56:59], v[4:7], v[180:183]\n\t"
  "v_max3_u32 v252, v252, v158, v159\n\t"
  "v_max_u32 v253, v136, v137\n\t"
  "v_mfma_f32_16x16x32_bf16 v[180:183], v[60:63], v[12:15], v[180:183]\n\t"
  "v_max3_u32 v253, v253, v138, v139\n\t"
  "s_waitcnt lgkmcnt(0)\n\t"
  "v_mfma_f32_16x16x32_bf16 v[120:123], v[160:163], v[16:19], v[120:123]\n\t"
  "v_max3_u32 v253, v253, v148, v149\n\t"
  "v_max3_u32 v253, v253, v150, v151\n\t"
  "v_mfma_f32_16x16x32_bf16 v[120:123], v[164:167], v[24:27], v[120:123]\n\t"
  "v_max3_u32 v253, v253, v200, v201\n\t"
  "v_max3_u32 v253, v253, v202, v203\n\t"
  "v_mfma_f32_16x16x32_bf16 v[124:127], v[160:163], v[20:23], v[124:127]\n\t"
  "v_and_b32 v253, s99, v253\n\t"
  "v_max_u32 v252, v252, v253\n\t"
  "v_mfma_f32_16x16x32_bf16 v[124:127], v[164:167], v[28:31], v[124:127]\n\t"
  "v_cmp_eq_u32 vcc, -1, v252\n\t"
  "v_mfma_f32_16x16x32_bf16 v[176:179], v[168:171], v[16:19], v[176:179]\n\t"
  "v_mfma_f32_16x16x32_bf16 v[176:179], v[172:175], v[24:27], v[176:179]\n\t"
  "v_mfma_f32_16x16x32_bf16 v[180:183], v[168:171], v[20:23], v[180:183]\n\t"
  "v_mfma_f32_16x16x32_bf16 v[180:183], v[172:175], v[28:31], v[180:183]\n\t"
  "s_nop 2\n\t"
  "s_cbranch_vccnz RETRYP_%=\n\t"
  "BACKP_%=:\n\t"
  "v_mfma_f32_16x16x32_bf16 v[120:123], v[66:69], v[128:131], v[120:123]\n\t"
  "v_mfma_f32_16x16x32_bf16 v[120:123], v[66:69], v[140:143], v[120:123]\n\t"
  "v_mfma_f32_16x16x32_bf16 v[120:123], v[66:69], v[152:155], v[120:123]\n\t"
  "v_mfma_f32_16x16x32_bf16 v[176:179], v[64:67], v[128:131], v[176:179]\n\t"
  "v_mfma_f32_16x16x32_bf16 v[176:179], v[64:67], v[140:143], v[176:179]\n\t"
  "v_mfma_f32_16x16x32_bf16 v[176:179], v[64:67], v[152:155], v[176:179]\n\t"
  "v_mfma_f32_16x16x32_bf16 v[124:127], v[66:69], v[132:135], v[124:127]\n\t"
  "v_mfma_f32_16x16x32_bf16 v[124:127], v[66:69], v[144:147], v[124:127]\n\t"
  "v_mfma_f32_16x16x32_bf16 v[124:127], v[66:69], v[156:159], v[124:127]\n\t"
  "s_add_u32 s44, s62, 18432\n\t"
  "s_addc_u32 s45, s63, 0\n\t"
  "v_mfma_f32_16x16x32_bf16 v[180:183], v[64:67], v[132:135], v[180:183]\n\t"
  "v_mfma_f32_16x16x32_bf16 v[180:183], v[64:67], v[144:147], v[180:183]\n\t"
  "v_mfma_f32_16x16x32_bf16 v[180:183], v[64:67], v[156:159], v[180:183]\n\t"
  "s_cmp_eq_u32 s99, 0\n\t"
  "v_fma_f32 v72, v120, v241, v72\n\t"
  "v_fma_f32 v73, v121, v241, v73\n\t"
  "v_fma_f32 v74, v122, v241, v74\n\t"
  "v_fma_f32 v75, v123, v241, v75\n\t"
  "v_fma_f32 v76, v176, v241, v76\n\t"
  "v_fma_f32 v77, v177, v241, v77\n\t"
  "v_fma_f32 v78, v178, v241, v78\n\t"
  "v_fma_f32 v79, v179, v241, v79\n\t"
  "v_cvt_pk_bf16_f32 v184, v72, v73\n\t"
  "v_cvt_pk_bf16_f32 v185, v74, v75\n\t"
  "v_cvt_pk_bf16_f32 v186, v76, v77\n\t"
  "v_cvt_pk_bf16_f32 v187, v78, v79\n\t"
  "global_store_dwordx4 v239, v[184:187], s[62:63] offset:0\n\t"
  "v_fma_f32 v80, v124, v242, v80\n\t"
  "v_fma_f32 v81, v125, v242, v81\n\t"
  "v_fma_f32 v82, v126, v242, v82\n\t"
  "v_fma_f32 v83, v127, v242, v83\n\t"
  "v_fma_f32 v84, v180, v242, v84\n\t"
  "v_fma_f32 v85, v181, v242, v85\n\t"
  "v_fma_f32 v86, v182, v242, v86\n\t"
  "v_fma_f32 v87, v183, v242, v87\n\t"
  "v_cvt_pk_bf16_f32 v188, v80, v81\n\t"
  "v_cvt_pk_bf16_f32 v189, v82, v83\n\t"
  "v_cvt_pk_bf16_f32 v190, v84, v85\n\t"
  "v_cvt_pk_bf16_f32 v191, v86, v87\n\t"
  "global_store_dwordx4 v239, v[188:191], s[44:45] offset:0\n\t"
  "s_cbranch_scc1 FIN_%=\n\t"
  "v_mfma_f32_16x16x32_bf16 v[112:115], v[66:69], v[136:139], v[112:115]\n\t"
  "v_mfma_f32_16x16x32_bf16 v[112:115], v[66:69], v[148:151], v[112:115]\n\t"
  "v_mfma_f32_16x16x32_bf16 v[112:115], v[66:69], v[200:203], v[112:115]\n\t"
  "v_mfma_f32_16x16x32_bf16 v[116:119], v[64:67], v[136:139], v[116:119]\n\t"
  "v_mfma_f32_16x16x32_bf16 v[116:119], v[64:67], v[148:151], v[116:119]\n\t"
  "v_mfma_f32_16x16x32_bf16 v[116:119], v[64:67], v[200:203], v[116:119]\n\t"
  "s_nop 4\n\t"
  "v_fma_f32 v88, v112, v241, v88\n\t"
  "v_fma_f32 v89, v113, v241, v89\n\t"
  "v_fma_f32 v90, v114, v241, v90\n\t"
  "v_fma_f32 v91, v115, v241, v91\n\t"
  "v_cvt_pk_bf16_f32 v192, v88, v89\n\t"
  "v_cvt_pk_bf16_f32 v193, v90, v91\n\t"
  "global_store_dwordx2 v240, v[192:193], s[62:63] offset:256\n\t"
  "v_fma_f32 v92, v116, v242, v92\n\t"
  "v_fma_f32 v93, v117, v242, v93\n\t"
  "v_fma_f32 v94, v118, v242, v94\n\t"
  "v_fma_f32 v95, v119, v242, v95\n\t"
  "v_cvt_pk_bf16_f32 v194, v92, v93\n\t"
  "v_cvt_pk_bf16_f32 v195, v94, v95\n\t"
  "global_store_dwordx2 v240, v[194:195], s[44:45] offset:256\n\t"
  "FIN_%=:\n\t"
  "s_branch END_%=\n\t"
  "RETRYW_%=:\n\t"
  "s_mov_b32 s47, 0\n\t"
  "LOOPW_%=:\n\t"
  "global_load_dword v243, v241, s[58:59] sc1\n\t"
  "s_waitcnt vmcnt(0)\n\t"
  "v_cmp_gt_u32 vcc, 1, v243\n\t"
  "s_nop 4\n\t"
  "s_cbranch_vccz BACKW_%=\n\t"
  "s_add_u32 s47, s47, 1\n\t"
  "s_cmp_gt_u32 s47, 0x40000\n\t"
  "s_cbranch_scc1 BACKW_%=\n\t"
  "s_sleep 1\n\t"
  "s_branch LOOPW_%=\n\t"
  "RETRYA_%=:\n\t"
  "s_mov_b32 s47, 0\n\t"
  "s_add_u32 s28, s58, 32768\n\t"
  "s_addc_u32 s29, s59, 0\n\t"
  "LOOPA_%=:\n\t"
  "global_load_dword v243, v234, s[28:29] sc1\n\t"
  "s_waitcnt vmcnt(0)\n\t"
  "v_cmp_gt_u32 vcc, 1, v243\n\t"
  "s_nop 4\n\t"
  "s_cbranch_vccz BACKA_%=\n\t"
  "s_add_u32 s47, s47, 1\n\t"
  "s_cmp_gt_u32 s47, 0x40000\n\t"
  "s_cbranch_scc1 BACKA_%=\n\t"
  "s_sleep 1\n\t"
  "s_branch LOOPA_%=\n\t"
  "RETRYB_%=:\n\t"
  "s_mov_b32 s47, 0\n\t"
  "s_add_u32 s28, s58, 65536\n\t"
  "s_addc_u32 s29, s59, 0\n\t"
  "LOOPB_%=:\n\t"
  "global_load_dword v243, v235, s[28:29] sc1\n\t"
  "s_waitcnt vmcnt(0)\n\t"
  "v_cmp_gt_u32 vcc, 1, v243\n\t"
  "s_nop 4\n\t"
  "s_cbranch_vccz BACKB_%=\n\t"
  "s_add_u32 s47, s47, 1\n\t"
  "s_cmp_gt_u32 s47, 0x40000\n\t"
  "s_cbranch_scc1 BACKB_%=\n\t"
  "s_sleep 1\n\t"
  "s_branch LOOPB_%=\n\t"
  "RETRYC_%=:\n\t"
  "s_mov_b32 s47, 0\n\t"
  "s_add_u32 s28, s58, 98304\n\t"
  "s_addc_u32 s29, s59, 0\n\t"
  "LOOPC_%=:\n\t"
  "global_load_dword v243, v235, s[28:29] sc1\n\t"
  "s_waitcnt vmcnt(0)\n\t"
  "v_cmp_gt_u32 vcc, 1, v243\n\t"
  "s_nop 4\n\t"
  "s_cbranch_vccz BACKC_%=\n\t"
  "s_add_u32 s47, s47, 1\n\t"
  "s_cmp_gt_u32 s47, 0x40000\n\t"
  "s_cbranch_scc1 BACKC_%=\n\t"
  "s_sleep 1\n\t"
  "s_branch LOOPC_%=\n\t"
  "RETRYD_%=:\n\t"
  "s_mov_b32 s47, 0\n\t"
  "s_add_u32 s28, s58, 131072\n\t"
  "s_addc_u32 s29, s59, 0\n\t"
  "LOOPD_%=:\n\t"
  "global_load_dword v225, v235, s[28:29] sc1\n\t"
  "s_waitcnt vmcnt(0)\n\t"
  "v_cmp_gt_u32 vcc, 1, v225\n\t"
  "s_nop 4\n\t"
  "s_cbranch_vccz BACKD_%=\n\t"
  "s_add_u32 s47, s47, 1\n\t"
  "s_cmp_gt_u32 s47, 0x40000\n\t"
  "s_cbranch_scc1 BACKD_%=\n\t"
  "s_sleep 1\n\t"
  "s_branch LOOPD_%=\n\t"
  "RETRYG_%=:\n\t"
  "s_mov_b32 s47, 0\n\t"
  "LOOPG_%=:\n\t"
  "global_load_dword v200, v223, s[42:43] offset:0 sc1\n\t"
  "global_load_dword v201, v223, s[42:43] offset:64 sc1\n\t"
  "global_load_dword v202, v223, s[42:43] offset:128 sc1\n\t"
  "global_load_dword v203, v223, s[42:43] offset:192 sc1\n\t"
  "s_waitcnt vmcnt(0)\n\t"
  "v_min_u32 v208, v200, v201\n\t"
  "v_min3_u32 v208, v208, v202, v203\n\t"
  "v_cmp_eq_u32 vcc, 0, v208\n\t"
  "s_nop 4\n\t"
  "s_cbranch_vccz BACKG_%=\n\t"
  "s_add_u32 s47, s47, 1\n\t"
  "s_cmp_gt_u32 s47, 0x40000\n\t"
  "s_cbranch_scc1 BACKG_%=\n\t"
  "s_sleep 1\n\t"
  "s_branch LOOPG_%=\n\t"
  "RETRYP_%=:\n\t"
  "s_mov_b32 s47, 0\n\t"
  "LOOPP_%=:\n\t"
  "global_load_dwordx4 v[128:131], v249, s[56:57] offset:0 sc1\n\t"
  "global_load_dwordx4 v[132:135], v249, s[56:57] offset:1024 sc1\n\t"
  "global_load_dwordx4 v[136:139], v249, s[56:57] offset:2048 sc1\n\t"
  "global_load_dwordx4 v[140:143], v250, s[56:57] offset:0 sc1\n\t"
  "global_load_dwordx4 v[144:147], v250, s[56:57] offset:1024 sc1\n\t"
  "global_load_dwordx4 v[148:151], v250, s[56:57] offset:2048 sc1\n\t"
  "global_load_dwordx4 v[152:155], v251, s[56:57] offset:0 sc1\n\t"
  "global_load_dwordx4 v[156:159], v251, s[56:57] offset:1024 sc1\n\t"
  "global_load_dwordx4 v[200:203], v251, s[56:57] offset:2048 sc1\n\t"
  "s_waitcnt vmcnt(0)\n\t"
  "v_max_u32 v252, v128, v129\n\t"
  "v_max3_u32 v252, v252, v130, v131\n\t"
  "v_max3_u32 v252, v252, v132, v133\n\t"
  "v_max3_u32 v252, v252, v134, v135\n\t"
  "v_max3_u32 v252, v252, v140, v141\n\t"
  "v_max3_u32 v252, v252, v142, v143\n\t"
  "v_max3_u32 v252, v252, v144, v145\n\t"
  "v_max3_u32 v252, v252, v146, v147\n\t"
  "v_max3_u32 v252, v252, v152, v153\n\t"
  "v_max3_u32 v252, v252, v154, v155\n\t"
  "v_max3_u32 v252, v252, v156, v157\n\t"
  "v_max3_u32 v252, v252, v158, v159\n\t"
  "v_max_u32 v253, v136, v137\n\t"
  "v_max3_u32 v253, v253, v138, v139\n\t"
  "v_max3_u32 v253, v253, v148, v149\n\t"
  "v_max3_u32 v253, v253, v150, v151\n\t"
  "v_max3_u32 v253, v253, v200, v201\n\t"
  "v_max3_u32 v253, v253, v202, v203\n\t"
  "v_and_b32 v253, s99, v253\n\t"
  "v_max_u32 v252, v252, v253\n\t"
  "v_cmp_eq_u32 vcc, -1, v252\n\t"
  "s_nop 4\n\t"
  "s_cbranch_vccz BACKP_%=\n\t"
  "s_add_u32 s47, s47, 1\n\t"
  "s_cmp_gt_u32 s47, 0x10000\n\t"
  "s_cbranch_scc1 BACKP_%=\n\t"
  "s_branch LOOPP_%=\n\t"
  "END_%=:\n\t"
  "CODE_END_LABEL:\n\t"
  :: "s"(ws), "s"(b), "s"(w), "s"(out), "s"(t0) : "v0", "v1", "v2", "v3", "v4", "v5", "v6", "v7", "v8", "v9", "v10", "v11", "v12", "v13", "v14", "v15", "v16", "v17", "v18", "v19", "v20", "v21", "v22", "v23", "v24", "v25", "v26", "v27", "v28", "v29", "v30", "v31", "v32", "v33", "v34", "v35", "v36", "v37", "v38", "v39", "v40", "v41", "v42", "v43", "v44", "v45", "v46", "v47", "v48", "v49", "v50", "v51", "v52", "v53", "v54", "v55", "v56", "v57", "v58", "v59", "v60", "v61", "v62", "v63", "v64", "v65", "v66", "v67", "v68", "v69", "v70", "v71", "v72", "v73", "v74", "v75", "v76", "v77", "v78", "v79", "v80", "v81", "v82", "v83", "v84", "v85", "v86", "v87", "v88", "v89", "v90", "v91", "v92", "v93", "v94", "v95", "v96", "v97", "v98", "v99", "v100", "v101", "v102", "v103", "v104", "v105", "v106", "v107", "v108", "v109", "v110", "v111", "v112", "v113", "v114", "v115", "v116", "v117", "v118", "v119", "v120", "v121", "v122", "v123", "v124", "v125", "v126", "v127", "v128", "v129", "v130", "v131", "v132", "v133", "v134", "v135", "v136", "v137", "v138", "v139", "v140", "v141", "v142", "v143", "v144", "v145", "v146", "v147", "v148", "v149", "v150", "v151", "v152", "v153", "v154", "v155", "v156", "v157", "v158", "v159", "v160", "v161", "v162", "v163", "v164", "v165", "v166", "v167", "v168", "v169", "v170", "v171", "v172", "v173", "v174", "v175", "v176", "v177", "v178", "v179", "v180", "v181", "v182", "v183", "v184", "v185", "v186", "v187", "v188", "v189", "v190", "v191", "v192", "v193", "v194", "v195", "v196", "v197", "v198", "v199", "v200", "v201", "v202", "v203", "v204", "v205", "v206", "v207", "v208", "v209", "v210", "v211", "v212", "v213", "v214", "v215", "v216", "v217", "v218", "v219", "v220", "v221", "v222", "v223", "v224", "v225", "v226", "v227", "v228", "v229", "v230", "v231", "v232", "v233", "v234", "v235", "v236", "v237", "v238", "v239", "v240", "v241", "v242", "v243", "v244", "v245", "v246", "v247", "v248", "v249", "v250", "v251", "v252", "v253", "v254", "v255", "a0", "a1", "a2", "a3", "a4", "a5", "a6", "a7", "a8", "a9", "a10", "a11", "a12", "a13", "a14", "a15", "a16", "a17", "a18", "a19", "a20", "a21", "a22", "a23", "a24", "a25", "a26", "a27", "a28", "a29", "a30", "a31", "a32", "a33", "a34", "a35", "a36", "a37", "a38", "a39", "a40", "a41", "a42", "a43", "a44", "a45", "a46", "a47", "a48", "a49", "a50", "a51", "a52", "a53", "a54", "a55", "a56", "a57", "a58", "a59", "a60", "a61", "a62", "a63", "a64", "a65", "a66", "a67", "a68", "a69", "a70", "a71", "a72", "a73", "a74", "a75", "a76", "a77", "a78", "a79", "a80", "a81", "a82", "a83", "a84", "a85", "a86", "a87", "a88", "a89", "a90", "a91", "a92", "a93", "a94", "a95", "a96", "a97", "a98", "a99", "a100", "a101", "a102", "a103", "a104", "a105", "a106", "a107", "a108", "a109", "a110", "a111", "a112", "a113", "a114", "a115", "a116", "a117", "a118", "a119", "a120", "a121", "a122", "a123", "a124", "a125", "a126", "a127", "a128", "a129", "a130", "a131", "a132", "a133", "a134", "a135", "a136", "a137", "a138", "a139", "a140", "a141", "a142", "a143", "a144", "a145", "a146", "a147", "a148", "a149", "a150", "a151", "a152", "a153", "a154", "a155", "a156", "a157", "a158", "a159", "a160", "a161", "a162", "a163", "a164", "a165", "a166", "a167", "a168", "a169", "a170", "a171", "a172", "a173", "a174", "a175", "a176", "a177", "a178", "a179", "a180", "a181", "a182", "a183", "a184", "a185", "a186", "a187", "a188", "a189", "a190", "a191", "a192", "a193", "a194", "a195", "a196", "a197", "a198", "a199", "a200", "a201", "a202", "a203", "a204", "a205", "a206", "a207", "a208", "a209", "a210", "a211", "a212", "a213", "a214", "a215", "a216", "a217", "a218", "a219", "a220", "a221", "a222", "a223", "a224", "a225", "a226", "a227", "a228", "a229", "a230", "a231", "a232", "a233", "a234", "a235", "a236", "a237", "a238", "a239", "a240", "a241", "a242", "a243", "a244", "a245", "a246", "a247", "a248", "a249", "a250", "a251", "a252", "a253", "a254", "a255", "s8", "s9", "s10", "s11", "s14", "s15", "s16", "s17", "s18", "s19", "s20", "s21", "s22", "s23", "s24", "s25", "s26", "s27", "s28", "s29", "s30", "s31", "s34", "s35", "s36", "s37", "s38", "s39", "s40", "s41", "s42", "s43", "s44", "s45", "s46", "s47", "s48", "s49", "s50", "s51", "s52", "s53", "s54", "s55", "s56", "s57", "s58", "s59", "s60", "s61", "s62", "s63", "s64", "s65", "s66", "s67", "s68", "s69", "s70", "s71", "s72", "s73", "s74", "s75", "s76", "s77", "s78", "s79", "s80", "s81", "s82", "s83", "s84", "s85", "s86", "s87", "s88", "s89", "s90", "s91", "s92", "s93", "s94", "s95", "s96", "s97", "s98", "s99", "vcc", "scc", "memory");
}

extern "C" void kernel_launch(void* const* d_in, const int* in_sizes, int n_in, void* d_out, int out_size, void* d_ws, size_t ws_size, hipStream_t stream) {
  const u16* x = (const u16*)d_in[0];
  const u16 *logA = (const u16*)d_in[1], *Aim = (const u16*)d_in[2], *Cre = (const u16*)d_in[3], *Cim = (const u16*)d_in[4];
  const u16 *logdt = (const u16*)d_in[5], *dskip = (const u16*)d_in[6];
  const u16 *wg = (const u16*)d_in[8], *wu = (const u16*)d_in[9], *wd = (const u16*)d_in[10];
  char* ws = (char*)d_ws;
  (void)hipFuncSetAttribute((const void*)k1_front, hipFuncAttributeMaxDynamicSharedMemorySize, K1_LDS);
  k1_front<<<NPF + 352 + 104, 512, K1_LDS, stream>>>(x, logA, Aim, Cre, Cim, logdt, dskip, wg, wu, ws, (char*)d_out);
  (void)hipFuncSetAttribute((const void*)k2_main, hipFuncAttributeMaxDynamicSharedMemorySize, FFN_LDS);
  k2_main<<<256, 256, FFN_LDS, stream>>>(wg, wu, wd, ws, (char*)d_out);
}
```
